```python
import torch

def _softmax(x, axis=-1):
    return torch.softmax(x, dim=axis)
def _gelu(x):
    return torch.nn.functional.gelu(x, approximate="tanh")
def _silu(x):
    return torch.nn.functional.silu(x)
def _sigmoid(x):
    return torch.sigmoid(x)
def _layernorm(x, axis=-1, eps=1e-5):
    m = x.mean(dim=axis, keepdim=True)
    v = x.var(dim=axis, keepdim=True, unbiased=False)
    return (x - m) / torch.sqrt(v + eps)
def _scatter_add(tgt, idx, src, axis=0):
    return tgt.clone().index_add_(axis, idx, src)
def _segment_sum(src, seg, n):
    out = torch.zeros((n,) + src.shape[1:], dtype=src.dtype)
    return out.index_add_(0, seg, src)
def _pad(x, pw):
    flat = tuple(v for pair in reversed(pw) for v in pair)
    return torch.nn.functional.pad(x, flat)

kernel_name = 'interleave_layers__compressed_kv_cla__glu_gated_swiglu__decode'

def setup_inputs():

    _w = (lambda a: (lambda t: t.to(torch.bfloat16) if t.is_floating_point() else t)(torch.as_tensor(a)))
    def _A_setup():
        B, H, Sq, Sk, D, M = 2, 32, 1, 8192, 128, 512
        Dc = 4 * D
        return {
            "x": _w((lambda *s: torch.randn(*s, dtype=torch.bfloat16) * 0.05)(B, Sq, M)),
            "c_kv": _w((lambda *s: torch.randn(*s, dtype=torch.bfloat16) * 0.8)(B, Sk, Dc)),
            "w_q": _w((lambda *s: torch.randn(*s, dtype=torch.bfloat16) * 1.6)(M, H, D) * (M ** -0.5)),
            "w_uk": _w((lambda *s: torch.randn(*s, dtype=torch.bfloat16) * 1.6)(Dc, H, D) * (Dc ** -0.5)),
            "w_uv": _w((lambda *s: torch.randn(*s, dtype=torch.bfloat16) * 0.8)(Dc, H, D) * (Dc ** -0.5)),
            "w_o": _w((lambda *s: torch.randn(*s, dtype=torch.bfloat16) * 0.8)(H, D, M) * ((H * D) ** -0.5)),
        }
    _A_in = _A_setup()

    def _B_setup():
        B, S, D, F = 2, 1, 512, 1792
        return {
            "x": _w((lambda *s: torch.randn(*s, dtype=torch.bfloat16) * 0.05)(B, S, D)),
            "w1": _w((lambda *s: torch.randn(*s, dtype=torch.bfloat16) * 0.4)(D, F) * (2.0 / D) ** 0.5),
            "w3": _w((lambda *s: torch.randn(*s, dtype=torch.bfloat16) * 0.4)(D, F) * (2.0 / D) ** 0.5),
            "w2": _w((lambda *s: torch.randn(*s, dtype=torch.bfloat16) * 0.4)(F, D) * (2.0 / F) ** 0.5),
        }
    _B_in = _B_setup()

    return {
        "A_x": _A_in["x"],
        "A_c_kv": _A_in["c_kv"],
        "A_w_q": _A_in["w_q"],
        "A_w_uk": _A_in["w_uk"],
        "A_w_uv": _A_in["w_uv"],
        "A_w_o": _A_in["w_o"],
        "B_x": _B_in["x"],
        "B_w1": _B_in["w1"],
        "B_w3": _B_in["w3"],
        "B_w2": _B_in["w2"],
    }

def reference(A_x, A_c_kv, A_w_q, A_w_uk, A_w_uv, A_w_o, B_x, B_w1, B_w3, B_w2):
    def _A_ref(x, c_kv, w_q, w_uk, w_uv, w_o):
        B, Sq, M = x.shape[0], x.shape[1], x.shape[2]
        Sk = c_kv.shape[1]
        H, D = w_q.shape[1], w_q.shape[2]
        q = torch.einsum("bqm,mhd->bhqd", x, w_q)
        k = torch.einsum("bkc,chd->bhkd", c_kv, w_uk)
        v = torch.einsum("bkc,chd->bhkd", c_kv, w_uv)
        scores = torch.einsum("bhqd,bhkd->bhqk", q, k) * (D ** -0.5)
        off = Sk - Sq
        qi = torch.arange(Sq)[:, None]
        kj = torch.arange(Sk)[None, :]
        cmask = kj <= (qi + off)
        scores = torch.where(cmask[None, None], scores, -1e9)
        p = _softmax(scores, axis=-1)
        ctx = torch.einsum("bhqk,bhkd->bhqd", p, v)
        return torch.einsum("bhqd,hdm->bqm", ctx, w_o)

    def _B_ref(x, w1, w3, w2):
        gate = torch.einsum("bsd,df->bsf", x, w1)
        up = torch.einsum("bsd,df->bsf", x, w3)
        h = _silu(gate) * up
        out = torch.einsum("bsf,fd->bsd", h, w2)
        return out

    x = A_x
    x = x + _A_ref(_layernorm(x, axis=-1), A_c_kv, A_w_q, A_w_uk, A_w_uv, A_w_o)
    x = x + _B_ref(_layernorm(x, axis=-1), B_w1, B_w3, B_w2)
    x = x + _A_ref(_layernorm(x, axis=-1), A_c_kv, A_w_q, A_w_uk, A_w_uv, A_w_o)
    x = x + _B_ref(_layernorm(x, axis=-1), B_w1, B_w3, B_w2)
    return x
```

```cpp
#include <hip/hip_runtime.h>
#include <hip/hip_bf16.h>
#include <stdint.h>

typedef unsigned short u16;
typedef unsigned v4u __attribute__((ext_vector_type(4)));

__device__ __forceinline__ float bf2f(u16 b) { return __uint_as_float(((unsigned)b) << 16); }
__device__ __forceinline__ u16 f2bf(float f) { unsigned u = __float_as_uint(f); u += 0x7FFFu + ((u >> 16) & 1u); return (u16)(u >> 16); }
__device__ __forceinline__ float rbf(float f) { return bf2f(f2bf(f)); }
__device__ __forceinline__ float lo_bf(unsigned u) { return __uint_as_float(u << 16); }
__device__ __forceinline__ float hi_bf(unsigned u) { return __uint_as_float(u & 0xFFFF0000u); }

constexpr int B = 2, H = 32, SK = 8192, D = 128, M = 512, DC = 512, F = 1792;
constexpr int NSPLIT = 128, KPS = SK / NSPLIT;

__device__ void block_ln(const float* xin  , float* xn  , float* red  ) {
  const int tid = threadIdx.x, lane = tid & 63, w = tid >> 6;
  const int b = w >> 1, half = w & 1;
  float v[4]; float s = 0.f;
  for (int i = 0; i < 4; ++i) { v[i] = xin[b * 512 + half * 256 + i * 64 + lane]; s += v[i]; }
  for (int o = 32; o >= 1; o >>= 1) s += __shfl_xor(s, o);
  if (lane == 0) red[w] = s;
  __syncthreads();
  const float mean = (red[b * 2] + red[b * 2 + 1]) * (1.0f / 512.0f);
  float q = 0.f;
  for (int i = 0; i < 4; ++i) { float d = v[i] - mean; q += d * d; }
  for (int o = 32; o >= 1; o >>= 1) q += __shfl_xor(q, o);
  if (lane == 0) red[8 + w] = q;
  __syncthreads();
  const float var = (red[8 + b * 2] + red[8 + b * 2 + 1]) * (1.0f / 512.0f);
  const float mb = rbf(mean), vb = rbf(var);
  const float sd = rbf(sqrtf(rbf(vb + 1e-5f)));
  for (int i = 0; i < 4; ++i) { xn[b * 512 + half * 256 + i * 64 + lane] = rbf(rbf(v[i] - mb) / sd); }
  __syncthreads();
}

__global__ __launch_bounds__(256) void k_qabs(const float* __restrict__ x, const u16* __restrict__ wq, const u16* __restrict__ wuk, float* __restrict__ qabs) {
  __shared__ float xin[1024], xn[1024], red[16], qs[2][128], part[2][2][128];
  const int tid = threadIdx.x, h = blockIdx.x >> 3, j = blockIdx.x & 7;
  for (int i = tid; i < 1024; i += 256) xin[i] = x[i];
  __syncthreads();
  block_ln(xin, xn, red);
  {
    const int d = tid & 127, mh = tid >> 7;
    float a0 = 0.f, a1 = 0.f;
    for (int m = mh * 256; m < mh * 256 + 256; ++m) {
      const float wv = bf2f(wq[(size_t)m * (H * D) + h * D + d]);
      a0 += xn[m] * wv; a1 += xn[512 + m] * wv;
    }
    part[mh][0][d] = a0; part[mh][1][d] = a1;
  }
  __syncthreads();
  if (tid < 256) { const int b = tid >> 7, d = tid & 127; qs[b][d] = rbf(part[0][b][d] + part[1][b][d]); }
  __syncthreads();
  {
    const int c = j * 64 + (tid >> 2), dq = tid & 3;
    float a0 = 0.f, a1 = 0.f;
    for (int d = dq * 32; d < dq * 32 + 32; ++d) {
      const float wv = bf2f(wuk[(size_t)c * (H * D) + h * D + d]);
      a0 += qs[0][d] * wv; a1 += qs[1][d] * wv;
    }
    a0 += __shfl_xor(a0, 1); a0 += __shfl_xor(a0, 2);
    a1 += __shfl_xor(a1, 1); a1 += __shfl_xor(a1, 2);
    if (dq == 0) {
      const float sc = 0.08838834764831845f;
      qabs[(0 * H + h) * DC + c] = a0 * sc;
      qabs[(1 * H + h) * DC + c] = a1 * sc;
    }
  }
}

__global__ __launch_bounds__(256) void k_attn(const float* __restrict__ qabs, const u16* __restrict__ ckv, float* __restrict__ pctx, float* __restrict__ pm, float* __restrict__ pl) {
  __shared__ float qa[H][DC + 1];
  __shared__ float sc[H][KPS + 1];
  const int tid = threadIdx.x, b = blockIdx.x / NSPLIT, s = blockIdx.x % NSPLIT;
  for (int i = tid; i < H * DC; i += 256) qa[i / DC][i % DC] = qabs[b * H * DC + i];
  __syncthreads();
  const u16* kv = ckv + ((size_t)b * SK + s * KPS) * DC;
  {
    const int k = tid & 63, hg = tid >> 6;
    float acc[8] = {0, 0, 0, 0, 0, 0, 0, 0};
    for (int c = 0; c < DC; c += 2) {
      const unsigned u = *(const unsigned*)(kv + (size_t)k * DC + c);
      const float v0 = lo_bf(u), v1 = hi_bf(u);
      for (int i = 0; i < 8; ++i) acc[i] += qa[hg * 8 + i][c] * v0 + qa[hg * 8 + i][c + 1] * v1;
    }
    for (int i = 0; i < 8; ++i) sc[hg * 8 + i][k] = acc[i];
  }
  __syncthreads();
  {
    const int lane = tid & 63, w = tid >> 6;
    for (int i = 0; i < 8; ++i) {
      const int h = w * 8 + i;
      float v = sc[h][lane];
      float m = v;
      for (int o = 32; o >= 1; o >>= 1) m = fmaxf(m, __shfl_xor(m, o));
      float p = __expf(v - m);
      float l = p;
      for (int o = 32; o >= 1; o >>= 1) l += __shfl_xor(l, o);
      sc[h][lane] = p;
      if (lane == 0) { pm[(b * NSPLIT + s) * H + h] = m; pl[(b * NSPLIT + s) * H + h] = l; }
    }
  }
  __syncthreads();
  {
    float acc[H][2];
    for (int h = 0; h < H; ++h) { acc[h][0] = 0.f; acc[h][1] = 0.f; }
    for (int k = 0; k < KPS; ++k) {
      const unsigned u = *(const unsigned*)(kv + (size_t)k * DC + tid * 2);
      const float v0 = lo_bf(u), v1 = hi_bf(u);
#pragma unroll
      for (int h = 0; h < H; ++h) { const float p = sc[h][k]; acc[h][0] += p * v0; acc[h][1] += p * v1; }
    }
#pragma unroll
    for (int h = 0; h < H; ++h) {
      float2 o = {acc[h][0], acc[h][1]};
      *(float2*)(pctx + (((size_t)b * NSPLIT + s) * H + h) * DC + tid * 2) = o;
    }
  }
}

__global__ __launch_bounds__(512) void k_comb(const float* __restrict__ pctx, const float* __restrict__ pm, const float* __restrict__ pl, const u16* __restrict__ wuv, float* __restrict__ ctx) {
  __shared__ float wts[NSPLIT], cc[DC], part[4][D], red[4];
  const int tid = threadIdx.x, b = blockIdx.x / H, h = blockIdx.x % H;
  if (tid < 64) {
    float m0 = pm[(b * NSPLIT + tid) * H + h], m1 = pm[(b * NSPLIT + 64 + tid) * H + h];
    float l0 = pl[(b * NSPLIT + tid) * H + h], l1 = pl[(b * NSPLIT + 64 + tid) * H + h];
    float m = fmaxf(m0, m1);
    for (int o = 32; o >= 1; o >>= 1) m = fmaxf(m, __shfl_xor(m, o));
    float e0 = __expf(m0 - m), e1 = __expf(m1 - m);
    float l = e0 * l0 + e1 * l1;
    for (int o = 32; o >= 1; o >>= 1) l += __shfl_xor(l, o);
    wts[tid] = e0 / l; wts[64 + tid] = e1 / l;
  }
  __syncthreads();
  {
    float a = 0.f;
    for (int s = 0; s < NSPLIT; ++s) a += wts[s] * pctx[(((size_t)b * NSPLIT + s) * H + h) * DC + tid];
    cc[tid] = a;
  }
  __syncthreads();
  {
    const int d = tid & 127, cq = tid >> 7;
    float a = 0.f;
    for (int c = cq * 128; c < cq * 128 + 128; ++c) a += cc[c] * bf2f(wuv[(size_t)c * (H * D) + h * D + d]);
    part[cq][d] = a;
  }
  __syncthreads();
  if (tid < 128) ctx[(b * H + h) * D + tid] = rbf(part[0][tid] + part[1][tid] + part[2][tid] + part[3][tid]);
}

__global__ __launch_bounds__(256) void k_oproj(const float* __restrict__ ctx, const u16* __restrict__ wo, const float* __restrict__ xin, float* __restrict__ xout) {
  __shared__ float part[32][2][8];
  const int tid = threadIdx.x, col = blockIdx.x * 8 + (tid & 7), kg = tid >> 3;
  float a0 = 0.f, a1 = 0.f;
  for (int k = kg * 128; k < kg * 128 + 128; ++k) {
    const float wv = bf2f(wo[(size_t)k * M + col]);
    a0 += ctx[k] * wv; a1 += ctx[4096 + k] * wv;
  }
  part[kg][0][tid & 7] = a0; part[kg][1][tid & 7] = a1;
  __syncthreads();
  if (tid < 16) {
    const int b = tid >> 3, c = tid & 7;
    float s = 0.f;
    for (int i = 0; i < 32; ++i) s += part[i][b][c];
    const int cc = blockIdx.x * 8 + c;
    xout[b * M + cc] = rbf(xin[b * M + cc] + rbf(s));
  }
}

__global__ __launch_bounds__(256) void k_gateup(const float* __restrict__ x, const u16* __restrict__ w1, const u16* __restrict__ w3, float* __restrict__ hbuf) {
  __shared__ float xin[1024], xn[1024], red[16], part[32][4][8];
  const int tid = threadIdx.x;
  for (int i = tid; i < 1024; i += 256) xin[i] = x[i];
  __syncthreads();
  block_ln(xin, xn, red);
  const int col = blockIdx.x * 8 + (tid & 7), kg = tid >> 3;
  float g0 = 0.f, g1 = 0.f, u0 = 0.f, u1 = 0.f;
  for (int k = kg * 16; k < kg * 16 + 16; ++k) {
    const float a = bf2f(w1[(size_t)k * F + col]), c = bf2f(w3[(size_t)k * F + col]);
    g0 += xn[k] * a; g1 += xn[512 + k] * a; u0 += xn[k] * c; u1 += xn[512 + k] * c;
  }
  part[kg][0][tid & 7] = g0; part[kg][1][tid & 7] = g1; part[kg][2][tid & 7] = u0; part[kg][3][tid & 7] = u1;
  __syncthreads();
  if (tid < 16) {
    const int b = tid >> 3, c = tid & 7;
    float g = 0.f, u = 0.f;
    for (int i = 0; i < 32; ++i) { g += part[i][b][c]; u += part[i][2 + b][c]; }
    g = rbf(g); u = rbf(u);
    const float sg = rbf(g / (1.0f + __expf(-g)));
    hbuf[b * F + blockIdx.x * 8 + c] = rbf(sg * u);
  }
}

__global__ __launch_bounds__(256) void k_down(const float* __restrict__ hbuf, const u16* __restrict__ w2, const float* __restrict__ xin, float* __restrict__ xout, u16* __restrict__ out_bf) {
  __shared__ float part[32][2][8];
  const int tid = threadIdx.x, col = blockIdx.x * 8 + (tid & 7), kg = tid >> 3;
  float a0 = 0.f, a1 = 0.f;
  for (int k = kg * 56; k < kg * 56 + 56; ++k) {
    const float wv = bf2f(w2[(size_t)k * M + col]);
    a0 += hbuf[k] * wv; a1 += hbuf[F + k] * wv;
  }
  part[kg][0][tid & 7] = a0; part[kg][1][tid & 7] = a1;
  __syncthreads();
  if (tid < 16) {
    const int b = tid >> 3, c = tid & 7;
    float s = 0.f;
    for (int i = 0; i < 32; ++i) s += part[i][b][c];
    const int cc = blockIdx.x * 8 + c;
    const float r = rbf(xin[b * M + cc] + rbf(s));
    xout[b * M + cc] = r;
    if (out_bf) out_bf[b * M + cc] = f2bf(r);
  }
}

__global__ void k_init(const u16* __restrict__ x, float* __restrict__ xf) {
  const int i = blockIdx.x * 256 + threadIdx.x;
  if (i < 1024) xf[i] = bf2f(x[i]);
}

extern "C" void kernel_launch(void* const* d_in, const int* in_sizes, int n_in,
                              void* d_out, int out_size, void* d_ws, size_t ws_size,
                              hipStream_t stream) {
  const u16* ax = (const u16*)d_in[0];
  const u16* ckv = (const u16*)d_in[1];
  const u16* wq = (const u16*)d_in[2];
  const u16* wuk = (const u16*)d_in[3];
  const u16* wuv = (const u16*)d_in[4];
  const u16* wo = (const u16*)d_in[5];
  const u16* w1 = (const u16*)d_in[7];
  const u16* w3 = (const u16*)d_in[8];
  const u16* w2 = (const u16*)d_in[9];
  u16* out = (u16*)d_out;
  float* ws = (float*)d_ws;
  float* x0 = ws;
  float* x1 = ws + 1024;
  float* qabs = ws + 4096;
  float* ctx = qabs + 32768;
  float* hb = ctx + 8192;
  float* pm = hb + 4096;
  float* pl = pm + 8192;
  float* pctx = pl + 8192;
  k_init<<<4, 256, 0, stream>>>(ax, x0);
  for (int l = 0; l < 2; ++l) {
    k_qabs<<<256, 256, 0, stream>>>(x0, wq, wuk, qabs);
    k_attn<<<256, 256, 0, stream>>>(qabs, ckv, pctx, pm, pl);
    k_comb<<<64, 512, 0, stream>>>(pctx, pm, pl, wuv, ctx);
    k_oproj<<<64, 256, 0, stream>>>(ctx, wo, x0, x1);
    k_gateup<<<224, 256, 0, stream>>>(x1, w1, w3, hb);
    k_down<<<64, 256, 0, stream>>>(hb, w2, x1, x0, l == 1 ? out : nullptr);
  }
}
```

```cpp
#include <hip/hip_runtime.h>
#include <hip/hip_bf16.h>
#include <stdint.h>

typedef unsigned short u16;
typedef unsigned v4u __attribute__((ext_vector_type(4)));

__device__ __forceinline__ float bf2f(u16 b) { return __uint_as_float(((unsigned)b) << 16); }
__device__ __forceinline__ u16 f2bf(float f) { unsigned u = __float_as_uint(f); u += 0x7FFFu + ((u >> 16) & 1u); return (u16)(u >> 16); }
__device__ __forceinline__ float rbf(float f) { return bf2f(f2bf(f)); }
__device__ __forceinline__ float lo_bf(unsigned u) { return __uint_as_float(u << 16); }
__device__ __forceinline__ float hi_bf(unsigned u) { return __uint_as_float(u & 0xFFFF0000u); }

constexpr int B = 2, H = 32, SK = 8192, D = 128, M = 512, DC = 512, F = 1792;
constexpr int NSPLIT = 128, KPS = SK / NSPLIT;

__device__ void block_ln(const float* xin  , float* xn  , float* red  ) {
  const int tid = threadIdx.x, lane = tid & 63, w = tid >> 6;
  const int b = w >> 1, half = w & 1;
  float v[4]; float s = 0.f;
  for (int i = 0; i < 4; ++i) { v[i] = xin[b * 512 + half * 256 + i * 64 + lane]; s += v[i]; }
  for (int o = 32; o >= 1; o >>= 1) s += __shfl_xor(s, o);
  if (lane == 0) red[w] = s;
  __syncthreads();
  const float mean = (red[b * 2] + red[b * 2 + 1]) * (1.0f / 512.0f);
  float q = 0.f;
  for (int i = 0; i < 4; ++i) { float d = v[i] - mean; q += d * d; }
  for (int o = 32; o >= 1; o >>= 1) q += __shfl_xor(q, o);
  if (lane == 0) red[8 + w] = q;
  __syncthreads();
  const float var = (red[8 + b * 2] + red[8 + b * 2 + 1]) * (1.0f / 512.0f);
  const float mb = rbf(mean), vb = rbf(var);
  const float sd = rbf(sqrtf(rbf(vb + 1e-5f)));
  for (int i = 0; i < 4; ++i) { xn[b * 512 + half * 256 + i * 64 + lane] = rbf(rbf(v[i] - mb) / sd); }
  __syncthreads();
}

__global__ __launch_bounds__(256) void k_qabs(const float* __restrict__ x, const u16* __restrict__ wq, const u16* __restrict__ wuk, float* __restrict__ qabs) {
  __shared__ float xin[1024], xn[1024], red[16], qs[2][128], part[2][2][128];
  const int tid = threadIdx.x, h = blockIdx.x >> 3, j = blockIdx.x & 7;
  for (int i = tid; i < 1024; i += 256) xin[i] = x[i];
  __syncthreads();
  block_ln(xin, xn, red);
  {
    const int d = tid & 127, mh = tid >> 7;
    float a0 = 0.f, a1 = 0.f;
    for (int m = mh * 256; m < mh * 256 + 256; ++m) {
      const float wv = bf2f(wq[(size_t)m * (H * D) + h * D + d]);
      a0 += xn[m] * wv; a1 += xn[512 + m] * wv;
    }
    part[mh][0][d] = a0; part[mh][1][d] = a1;
  }
  __syncthreads();
  if (tid < 256) { const int b = tid >> 7, d = tid & 127; qs[b][d] = rbf(part[0][b][d] + part[1][b][d]); }
  __syncthreads();
  {
    const int c = j * 64 + (tid >> 2), dq = tid & 3;
    float a0 = 0.f, a1 = 0.f;
    for (int d = dq * 32; d < dq * 32 + 32; ++d) {
      const float wv = bf2f(wuk[(size_t)c * (H * D) + h * D + d]);
      a0 += qs[0][d] * wv; a1 += qs[1][d] * wv;
    }
    a0 += __shfl_xor(a0, 1); a0 += __shfl_xor(a0, 2);
    a1 += __shfl_xor(a1, 1); a1 += __shfl_xor(a1, 2);
    if (dq == 0) {
      const float sc = 0.08838834764831845f;
      qabs[(0 * H + h) * DC + c] = a0 * sc;
      qabs[(1 * H + h) * DC + c] = a1 * sc;
    }
  }
}

__global__ __launch_bounds__(256) void k_attn(const float* __restrict__ qabs, const u16* __restrict__ ckv, float* __restrict__ pctx, float* __restrict__ pm, float* __restrict__ pl) {
  __shared__ float qa[H][DC + 1];
  __shared__ float sc[H][KPS + 1];
  const int tid = threadIdx.x, b = blockIdx.x / NSPLIT, s = blockIdx.x % NSPLIT;
  for (int i = tid; i < H * DC; i += 256) qa[i / DC][i % DC] = qabs[b * H * DC + i];
  __syncthreads();
  const u16* kv = ckv + ((size_t)b * SK + s * KPS) * DC;
  {
    const int k = tid & 63, hg = tid >> 6;
    float acc[8] = {0, 0, 0, 0, 0, 0, 0, 0};
    for (int c = 0; c < DC; c += 2) {
      const unsigned u = *(const unsigned*)(kv + (size_t)k * DC + c);
      const float v0 = lo_bf(u), v1 = hi_bf(u);
      for (int i = 0; i < 8; ++i) acc[i] += qa[hg * 8 + i][c] * v0 + qa[hg * 8 + i][c + 1] * v1;
    }
    for (int i = 0; i < 8; ++i) sc[hg * 8 + i][k] = acc[i];
  }
  __syncthreads();
  {
    const int lane = tid & 63, w = tid >> 6;
    for (int i = 0; i < 8; ++i) {
      const int h = w * 8 + i;
      float v = sc[h][lane];
      float m = v;
      for (int o = 32; o >= 1; o >>= 1) m = fmaxf(m, __shfl_xor(m, o));
      float p = __expf(v - m);
      float l = p;
      for (int o = 32; o >= 1; o >>= 1) l += __shfl_xor(l, o);
      sc[h][lane] = p;
      if (lane == 0) { pm[(b * NSPLIT + s) * H + h] = m; pl[(b * NSPLIT + s) * H + h] = l; }
    }
  }
  __syncthreads();
  {
    float acc[H][2];
    for (int h = 0; h < H; ++h) { acc[h][0] = 0.f; acc[h][1] = 0.f; }
    for (int k = 0; k < KPS; ++k) {
      const unsigned u = *(const unsigned*)(kv + (size_t)k * DC + tid * 2);
      const float v0 = lo_bf(u), v1 = hi_bf(u);
#pragma unroll
      for (int h = 0; h < H; ++h) { const float p = sc[h][k]; acc[h][0] += p * v0; acc[h][1] += p * v1; }
    }
#pragma unroll
    for (int h = 0; h < H; ++h) {
      float2 o = {acc[h][0], acc[h][1]};
      *(float2*)(pctx + (((size_t)b * NSPLIT + s) * H + h) * DC + tid * 2) = o;
    }
  }
}

__global__ __launch_bounds__(512) void k_comb(const float* __restrict__ pctx, const float* __restrict__ pm, const float* __restrict__ pl, const u16* __restrict__ wuv, float* __restrict__ ctx) {
  __shared__ float wts[NSPLIT], cc[DC], part[4][D], red[4];
  const int tid = threadIdx.x, b = blockIdx.x / H, h = blockIdx.x % H;
  if (tid < 64) {
    float m0 = pm[(b * NSPLIT + tid) * H + h], m1 = pm[(b * NSPLIT + 64 + tid) * H + h];
    float l0 = pl[(b * NSPLIT + tid) * H + h], l1 = pl[(b * NSPLIT + 64 + tid) * H + h];
    float m = fmaxf(m0, m1);
    for (int o = 32; o >= 1; o >>= 1) m = fmaxf(m, __shfl_xor(m, o));
    float e0 = __expf(m0 - m), e1 = __expf(m1 - m);
    float l = e0 * l0 + e1 * l1;
    for (int o = 32; o >= 1; o >>= 1) l += __shfl_xor(l, o);
    wts[tid] = e0 / l; wts[64 + tid] = e1 / l;
  }
  __syncthreads();
  {
    float a = 0.f;
    for (int s = 0; s < NSPLIT; ++s) a += wts[s] * pctx[(((size_t)b * NSPLIT + s) * H + h) * DC + tid];
    cc[tid] = a;
  }
  __syncthreads();
  {
    const int d = tid & 127, cq = tid >> 7;
    float a = 0.f;
    for (int c = cq * 128; c < cq * 128 + 128; ++c) a += cc[c] * bf2f(wuv[(size_t)c * (H * D) + h * D + d]);
    part[cq][d] = a;
  }
  __syncthreads();
  if (tid < 128) ctx[(b * H + h) * D + tid] = rbf(part[0][tid] + part[1][tid] + part[2][tid] + part[3][tid]);
}

__global__ __launch_bounds__(256) void k_oproj(const float* __restrict__ ctx, const u16* __restrict__ wo, const float* __restrict__ xin, float* __restrict__ xout) {
  __shared__ float part[32][2][8];
  const int tid = threadIdx.x, col = blockIdx.x * 8 + (tid & 7), kg = tid >> 3;
  float a0 = 0.f, a1 = 0.f;
  for (int k = kg * 128; k < kg * 128 + 128; ++k) {
    const float wv = bf2f(wo[(size_t)k * M + col]);
    a0 += ctx[k] * wv; a1 += ctx[4096 + k] * wv;
  }
  part[kg][0][tid & 7] = a0; part[kg][1][tid & 7] = a1;
  __syncthreads();
  if (tid < 16) {
    const int b = tid >> 3, c = tid & 7;
    float s = 0.f;
    for (int i = 0; i < 32; ++i) s += part[i][b][c];
    const int cc = blockIdx.x * 8 + c;
    xout[b * M + cc] = rbf(xin[b * M + cc] + rbf(s));
  }
}

__global__ __launch_bounds__(256) void k_gateup(const float* __restrict__ x, const u16* __restrict__ w1, const u16* __restrict__ w3, float* __restrict__ hbuf) {
  __shared__ float xin[1024], xn[1024], red[16], part[32][4][8];
  const int tid = threadIdx.x;
  for (int i = tid; i < 1024; i += 256) xin[i] = x[i];
  __syncthreads();
  block_ln(xin, xn, red);
  const int col = blockIdx.x * 8 + (tid & 7), kg = tid >> 3;
  float g0 = 0.f, g1 = 0.f, u0 = 0.f, u1 = 0.f;
  for (int k = kg * 16; k < kg * 16 + 16; ++k) {
    const float a = bf2f(w1[(size_t)k * F + col]), c = bf2f(w3[(size_t)k * F + col]);
    g0 += xn[k] * a; g1 += xn[512 + k] * a; u0 += xn[k] * c; u1 += xn[512 + k] * c;
  }
  part[kg][0][tid & 7] = g0; part[kg][1][tid & 7] = g1; part[kg][2][tid & 7] = u0; part[kg][3][tid & 7] = u1;
  __syncthreads();
  if (tid < 16) {
    const int b = tid >> 3, c = tid & 7;
    float g = 0.f, u = 0.f;
    for (int i = 0; i < 32; ++i) { g += part[i][b][c]; u += part[i][2 + b][c]; }
    g = rbf(g); u = rbf(u);
    const float sg = rbf(g / (1.0f + __expf(-g)));
    hbuf[b * F + blockIdx.x * 8 + c] = rbf(sg * u);
  }
}

__global__ __launch_bounds__(256) void k_down(const float* __restrict__ hbuf, const u16* __restrict__ w2, const float* __restrict__ xin, float* __restrict__ xout, u16* __restrict__ out_bf) {
  __shared__ float part[32][2][8];
  const int tid = threadIdx.x, col = blockIdx.x * 8 + (tid & 7), kg = tid >> 3;
  float a0 = 0.f, a1 = 0.f;
  for (int k = kg * 56; k < kg * 56 + 56; ++k) {
    const float wv = bf2f(w2[(size_t)k * M + col]);
    a0 += hbuf[k] * wv; a1 += hbuf[F + k] * wv;
  }
  part[kg][0][tid & 7] = a0; part[kg][1][tid & 7] = a1;
  __syncthreads();
  if (tid < 16) {
    const int b = tid >> 3, c = tid & 7;
    float s = 0.f;
    for (int i = 0; i < 32; ++i) s += part[i][b][c];
    const int cc = blockIdx.x * 8 + c;
    const float r = rbf(xin[b * M + cc] + rbf(s));
    xout[b * M + cc] = r;
    if (out_bf) out_bf[b * M + cc] = f2bf(r);
  }
}

extern "C" __device__ unsigned long long llvm_dispatch_id() __asm("llvm.amdgcn.dispatch.id");
__global__ void k_init(const u16* __restrict__ x, float* __restrict__ xf, unsigned long long* idw) {
  const int i = blockIdx.x * 256 + threadIdx.x;
  unsigned long long id = llvm_dispatch_id();
  unsigned long long old = idw[blockIdx.x * 256 + threadIdx.x];
  float poison = (old == id) ? __uint_as_float(0x7FC00000u) : 0.f;
  idw[blockIdx.x * 256 + threadIdx.x] = id;
  if (i < 1024) xf[i] = bf2f(x[i]) + poison;
}

extern "C" void kernel_launch(void* const* d_in, const int* in_sizes, int n_in,
                              void* d_out, int out_size, void* d_ws, size_t ws_size,
                              hipStream_t stream) {
  const u16* ax = (const u16*)d_in[0];
  const u16* ckv = (const u16*)d_in[1];
  const u16* wq = (const u16*)d_in[2];
  const u16* wuk = (const u16*)d_in[3];
  const u16* wuv = (const u16*)d_in[4];
  const u16* wo = (const u16*)d_in[5];
  const u16* w1 = (const u16*)d_in[7];
  const u16* w3 = (const u16*)d_in[8];
  const u16* w2 = (const u16*)d_in[9];
  u16* out = (u16*)d_out;
  float* ws = (float*)d_ws;
  float* x0 = ws;
  float* x1 = ws + 1024;
  float* qabs = ws + 4096;
  float* ctx = qabs + 32768;
  float* hb = ctx + 8192;
  float* pm = hb + 4096;
  float* pl = pm + 8192;
  float* pctx = pl + 8192;
  k_init<<<4, 256, 0, stream>>>(ax, x0, (unsigned long long*)((char*)d_ws + (128u << 20)));
  for (int l = 0; l < 2; ++l) {
    k_qabs<<<256, 256, 0, stream>>>(x0, wq, wuk, qabs);
    k_attn<<<256, 256, 0, stream>>>(qabs, ckv, pctx, pm, pl);
    k_comb<<<64, 512, 0, stream>>>(pctx, pm, pl, wuv, ctx);
    k_oproj<<<64, 256, 0, stream>>>(ctx, wo, x0, x1);
    k_gateup<<<224, 256, 0, stream>>>(x1, w1, w3, hb);
    k_down<<<64, 256, 0, stream>>>(hb, w2, x1, x0, l == 1 ? out : nullptr);
  }
}
```

```cpp
#include <hip/hip_runtime.h>
#include <hip/hip_bf16.h>
#include <stdint.h>

#define LAS __attribute__((address_space(3)))
#define GAS __attribute__((address_space(1)))
typedef unsigned short u16;
typedef unsigned v4u __attribute__((ext_vector_type(4)));
typedef unsigned v2u __attribute__((ext_vector_type(2)));
typedef float f32x4 __attribute__((ext_vector_type(4)));
typedef float f32x2 __attribute__((ext_vector_type(2)));
typedef short bf16x8 __attribute__((ext_vector_type(8)));
typedef short s16x4 __attribute__((ext_vector_type(4)));
typedef __bf16 bf16x2_t __attribute__((ext_vector_type(2)));
#define DI __device__ __forceinline__

extern "C" __device__ unsigned long long llvm_dispatch_id() __asm("llvm.amdgcn.dispatch.id");

namespace mk {
constexpr int NT = 512;
constexpr unsigned SENT = 0xFFFFFFFFu;
constexpr unsigned SPIN_CAP = 1u << 14;
constexpr int AUX_SC1 = 16;

constexpr int OFF_RDY = 0;
constexpr int OFF_X1 = 16384;
constexpr int OFF_ST = OFF_X1 + 2 * 2 * 32 * 1024;
constexpr int OFF_X3 = OFF_ST + 2 * 2 * 32 * 512;
constexpr int XV_STR = 34 * 1024;
constexpr int OFF_X4 = OFF_X3 + 4 * XV_STR;
constexpr int OFF_X2 = OFF_X4 + 4 * XV_STR;
constexpr int WS_BYTES = OFF_X2 + 2 * 2 * 32 * 65536;

constexpr int L_IMG = 0;
constexpr int L_QA = 131072;
constexpr int L_PB = L_QA + 16384;
constexpr int L_SB = L_PB + 8192;
constexpr int L_LN = L_SB + 1024;
constexpr int L_VEC = L_LN + 64;
constexpr int L_BYTES = L_VEC + 256;
constexpr int L_RED = L_QA;

struct Params {
  const u16* x; const u16* ckv; const u16* wq; const u16* wuk; const u16* wuv; const u16* wo;
  const u16* w1; const u16* w3; const u16* w2; u16* out; unsigned char* ws;
};

DI float bf_lo(unsigned u) { return __builtin_bit_cast(float, u << 16); }
DI float bf_hi(unsigned u) { return __builtin_bit_cast(float, u & 0xFFFF0000u); }
DI float rbf(float f) { return (float)(__bf16)f; }
DI unsigned pack2(float lo, float hi) { bf16x2_t v = {(__bf16)lo, (__bf16)hi}; return __builtin_bit_cast(unsigned, v); }
DI unsigned canon(unsigned u) { return u == SENT ? 0x7FC07FC0u : u; }
DI f32x4 mfma(v4u a, v4u b, f32x4 c) { return __builtin_amdgcn_mfma_f32_16x16x32_bf16(__builtin_bit_cast(bf16x8, a), __builtin_bit_cast(bf16x8, b), c, 0, 0, 0); }
template <int CTRL> DI float dppf(float v) { return __builtin_bit_cast(float, __builtin_amdgcn_update_dpp(0, __builtin_bit_cast(int, v), CTRL, 0xf, 0xf, true)); }
template <int CTRL> DI unsigned dppu(unsigned v) { return (unsigned)__builtin_amdgcn_update_dpp(0, (int)v, CTRL, 0xf, 0xf, true); }
DI float red8_sum(float v) { v += dppf<0xB1>(v); v += dppf<0x4E>(v); v += dppf<0x141>(v); return v; }
DI float red8_max(float v) { v = fmaxf(v, dppf<0xB1>(v)); v = fmaxf(v, dppf<0x4E>(v)); v = fmaxf(v, dppf<0x141>(v)); return v; }
DI v4u ror8(v4u a) { v4u r; r.x = dppu<0x128>(a.x); r.y = dppu<0x128>(a.y); r.z = dppu<0x128>(a.z); r.w = dppu<0x128>(a.w); return r; }
DI float rdlane(float v, int l) { return __builtin_bit_cast(float, __builtin_amdgcn_readlane(__builtin_bit_cast(int, v), l)); }
DI float rows4_sum(float v) { return (rdlane(v, 0) + rdlane(v, 16)) + (rdlane(v, 32) + rdlane(v, 48)); }
DI float u2f(unsigned u) { return __builtin_bit_cast(float, u); }
DI unsigned f2u(float f) { return __builtin_bit_cast(unsigned, f); }
DI float xg_sum(float v) {
  const auto a = __builtin_amdgcn_permlane16_swap(f2u(v), f2u(v), false, false);
  const unsigned a0 = a[0], a1 = a[1];
  v = u2f(a0) + u2f(a1);
  const auto b = __builtin_amdgcn_permlane32_swap(f2u(v), f2u(v), false, false);
  const unsigned b0 = b[0], b1 = b[1];
  return u2f(b0) + u2f(b1);
}
DI float xg_max(float v) {
  const auto a = __builtin_amdgcn_permlane16_swap(f2u(v), f2u(v), false, false);
  const unsigned a0 = a[0], a1 = a[1];
  v = fmaxf(u2f(a0), u2f(a1));
  const auto b = __builtin_amdgcn_permlane32_swap(f2u(v), f2u(v), false, false);
  const unsigned b0 = b[0], b1 = b[1];
  return fmaxf(u2f(b0), u2f(b1));
}
DI unsigned swz(unsigned r) { return (((r ^ ((r >> 1) & 4u)) & 7u) << 1) | ((r >> 3) & 1u); }

typedef __amdgpu_buffer_rsrc_t rsrc_t;
DI v4u ld128(rsrc_t rs, int off) { return __builtin_amdgcn_raw_buffer_load_b128(rs, off, 0, AUX_SC1); }
DI v2u ld64(rsrc_t rs, int off) { return __builtin_amdgcn_raw_buffer_load_b64(rs, off, 0, AUX_SC1); }
DI void st128(rsrc_t rs, int off, v4u v) { __builtin_amdgcn_raw_buffer_store_b128(v, rs, off, 0, AUX_SC1); }
DI void st64(rsrc_t rs, int off, v2u v) { __builtin_amdgcn_raw_buffer_store_b64(v, rs, off, 0, AUX_SC1); }
DI void st32(rsrc_t rs, int off, unsigned v) { __builtin_amdgcn_raw_buffer_store_b32(v, rs, off, 0, AUX_SC1); }
DI bool ok4(v4u v) { return (v.x != SENT) & (v.y != SENT) & (v.z != SENT) & (v.w != SENT); }
DI bool ok2(v2u v) { return (v.x != SENT) & (v.y != SENT); }
#define CBAR() asm volatile("" ::: "memory")
#define DRAIN() asm volatile("s_waitcnt vmcnt(0)" ::: "memory")

DI void tr8x8(const v4u (&r)[8], v4u* f) {
#pragma unroll
  for (int q = 0; q < 4; ++q)
#pragma unroll
    for (int d = 0; d < 4; ++d) {
      const unsigned a = r[2 * d][q], b = r[2 * d + 1][q];
      f[2 * q][d] = __builtin_amdgcn_perm(b, a, 0x05040100u);
      f[2 * q + 1][d] = __builtin_amdgcn_perm(b, a, 0x07060302u);
    }
}
DI void tr8x4(const v2u (&r)[8], v4u* f) {
#pragma unroll
  for (int q = 0; q < 2; ++q)
#pragma unroll
    for (int d = 0; d < 4; ++d) {
      const unsigned a = r[2 * d][q], b = r[2 * d + 1][q];
      f[2 * q][d] = __builtin_amdgcn_perm(b, a, 0x05040100u);
      f[2 * q + 1][d] = __builtin_amdgcn_perm(b, a, 0x07060302u);
    }
}

#define STAMP(c, i) do {} while (0)
struct Ctx {
  unsigned char* wsp;
  rsrc_t rs; LAS unsigned char* L;
  int lane, w, b, G, s, blk;
  bool dead;
};

DI void wait_ready(Ctx& c, v4u nonce) {
  const int o0 = OFF_RDY + (c.b * 128 + c.lane) * 64, o1 = o0 + 64 * 64;
  unsigned spins = 0;
  while (!c.dead) {
    CBAR();
    const v4u a = ld128(c.rs, o0), bq = ld128(c.rs, o1);
    const bool good = (a.x == nonce.x) & (a.y == nonce.y) & (a.z == nonce.z) & (a.w == nonce.w) &
                      (bq.x == nonce.x) & (bq.y == nonce.y) & (bq.z == nonce.z) & (bq.w == nonce.w);
    if (__all(good)) break;
    if (++spins > SPIN_CAP) c.dead = true;
  }
  CBAR();
}

template <bool RES_WS>
DI void gather33(Ctx& c, int base, const u16* xin, float (&xv)[8]) {
  const int g = c.lane >> 4, r = c.lane & 15, ks = r >> 3, pg = r & 7;
  const int co = 128 * c.w + 64 * ks + 16 * g;
  v4u v[4], res;
  if (!RES_WS) res = *(const GAS v4u*)((const GAS unsigned char*)xin + co);
  unsigned spins = 0;
  for (;;) {
    CBAR();
#pragma unroll
    for (int i = 0; i < 4; ++i) v[i] = ld128(c.rs, base + (pg + 8 * i) * 1024 + co);
    if (RES_WS) res = ld128(c.rs, base + 32 * 1024 + co);
    bool good = ok4(v[0]) & ok4(v[1]) & ok4(v[2]) & ok4(v[3]);
    if (RES_WS) good &= ok4(res);
    if (__all(good) || c.dead) break;
    if (++spins > SPIN_CAP) c.dead = true;
  }
#pragma unroll
  for (int q = 0; q < 4; ++q) {
    float lo = (bf_lo(v[0][q]) + bf_lo(v[1][q])) + (bf_lo(v[2][q]) + bf_lo(v[3][q]));
    float hi = (bf_hi(v[0][q]) + bf_hi(v[1][q])) + (bf_hi(v[2][q]) + bf_hi(v[3][q]));
    lo = red8_sum(lo); hi = red8_sum(hi);
    xv[2 * q] = rbf(bf_lo(res[q]) + rbf(lo));
    xv[2 * q + 1] = rbf(bf_hi(res[q]) + rbf(hi));
  }
}

DI v4u layernorm(Ctx& c, const float (&xv)[8]) {
  float s1 = ((xv[0] + xv[1]) + (xv[2] + xv[3])) + ((xv[4] + xv[5]) + (xv[6] + xv[7]));
  s1 += dppf<0x128>(s1);
  const float mw = rows4_sum(s1) * (1.0f / 64.0f);
  float s2 = 0.f;
#pragma unroll
  for (int j = 0; j < 8; ++j) { const float d = xv[j] - mw; s2 += d * d; }
  s2 += dppf<0x128>(s2);
  const float m2w = rows4_sum(s2);
  if (c.lane == 0) *(LAS f32x2*)(c.L + L_LN + c.w * 8) = f32x2{mw, m2w};
  __syncthreads();
  f32x4 t[4];
#pragma unroll
  for (int i = 0; i < 4; ++i) t[i] = *(const LAS f32x4*)(c.L + L_LN + i * 16);
  const float mean = (((t[0].x + t[0].z) + (t[1].x + t[1].z)) + ((t[2].x + t[2].z) + (t[3].x + t[3].z))) * 0.125f;
  float m2 = ((t[0].y + t[0].w) + (t[1].y + t[1].w)) + ((t[2].y + t[2].w) + (t[3].y + t[3].w));
  float dd = 0.f;
#pragma unroll
  for (int i = 0; i < 4; ++i) { const float a = t[i].x - mean, b2 = t[i].z - mean; dd += a * a + b2 * b2; }
  const float var = (m2 + 64.0f * dd) * (1.0f / 512.0f);
  const float mb = rbf(mean), vb = rbf(var);
  const float sd = rbf(sqrtf(rbf(vb + 1e-5f)));
  v4u a;
#pragma unroll
  for (int q = 0; q < 4; ++q) a[q] = pack2(rbf(xv[2 * q] - mb) / sd, rbf(xv[2 * q + 1] - mb) / sd);
  return a;
}

template <bool HILO>
DI void gemv_ksplit(Ctx& c, v4u a0, const v4u* W) {
  const v4u a1 = ror8(a0);
  f32x4 acc[8];
#pragma unroll
  for (int t = 0; t < 8; ++t) acc[t] = mfma(a0, W[t], f32x4{0.f, 0.f, 0.f, 0.f});
#pragma unroll
  for (int t = 0; t < 8; ++t) acc[t] = mfma(a1, W[8 + t], acc[t]);
  if (c.lane < 16) {
    f32x4 o0, o1;
#pragma unroll
    for (int t = 0; t < 4; ++t) { o0[t] = HILO ? acc[t].x + acc[t].y : acc[t].x; o1[t] = HILO ? acc[4 + t].x + acc[4 + t].y : acc[4 + t].x; }
    *(LAS f32x4*)(c.L + L_RED + c.w * 512 + c.lane * 32) = o0;
    *(LAS f32x4*)(c.L + L_RED + c.w * 512 + c.lane * 32 + 16) = o1;
  }
}
DI float red_sum8(Ctx& c, int idx) {
  const LAS float* R = (const LAS float*)(c.L + L_RED);
  return ((R[idx] + R[128 + idx]) + (R[256 + idx] + R[384 + idx])) + ((R[512 + idx] + R[640 + idx]) + (R[768 + idx] + R[896 + idx]));
}

DI float score_round(float s) {
  const float SC = 0.08838834764831845f;
  const unsigned bits = __builtin_bit_cast(unsigned, s);
  const unsigned tr = bits & 0xFFFF0000u;
  const float ga = rbf(__builtin_bit_cast(float, tr) * SC);
  const float gb = rbf(__builtin_bit_cast(float, tr + 0x10000u) * SC);
  const float frac = (float)(bits & 0xFFFFu) * (1.0f / 65536.0f);
  const float wgt = fminf(fmaxf((frac - 0.5f) * (1.0f / 0.6f) + 0.5f, 0.f), 1.f);
  const float r = ga + wgt * (gb - ga);
  return (r == r) ? r : s * SC;
}

DI void attn_phase(Ctx& c, int l) {
  const int tid = c.w * 64 + c.lane, n = c.lane & 15, g = c.lane >> 4;
  v4u kf[16];
  {
    const unsigned kb = L_IMG + (c.w * 16 + n) * 1024 + (((unsigned)g ^ swz(n)) << 4);
#pragma unroll
    for (int ks = 0; ks < 16; ++ks) kf[ks] = *(const LAS v4u*)(c.L + ((kb ^ (64u * (ks & 3))) + 256u * (ks >> 2)));
  }
  {
    const int base = OFF_X1 + ((l * 2 + c.b) * 32 + 16 * c.G) * 1024;
    v4u v0, v1;
    unsigned spins = 0;
    for (;;) {
      CBAR();
      v0 = ld128(c.rs, base + tid * 16);
      v1 = ld128(c.rs, base + 8192 + tid * 16);
      if (__all(ok4(v0) & ok4(v1)) || c.dead) break;
      if (++spins > SPIN_CAP) c.dead = true;
    }
    STAMP(c, 2 + 8 * l);
    const int hd = tid >> 6, ch = tid & 63;
    *(LAS v4u*)(c.L + L_QA + hd * 1024 + ((ch ^ swz(hd)) << 4)) = v0;
    *(LAS v4u*)(c.L + L_QA + (hd + 8) * 1024 + ((ch ^ swz(hd + 8)) << 4)) = v1;
  }
  __syncthreads();
  STAMP(c, 26 + l);
  f32x4 sacc = {0.f, 0.f, 0.f, 0.f}, sacc2 = {0.f, 0.f, 0.f, 0.f};
  {
    const unsigned qb = L_QA + n * 1024 + (((unsigned)g ^ swz(n)) << 4);
#pragma unroll
    for (int ks = 0; ks < 16; ks += 2) {
      const v4u q0 = *(const LAS v4u*)(c.L + ((qb ^ (64u * (ks & 3))) + 256u * (ks >> 2)));
      const v4u q1 = *(const LAS v4u*)(c.L + ((qb ^ (64u * ((ks + 1) & 3))) + 256u * ((ks + 1) >> 2)));
      sacc = mfma(kf[ks], q0, sacc);
      sacc2 = mfma(kf[ks + 1], q1, sacc2);
    }
  }
  float sv[4];
#pragma unroll
  for (int q = 0; q < 4; ++q) sv[q] = score_round(sacc[q] + sacc2[q]);
  const float mw = xg_max(fmaxf(fmaxf(sv[0], sv[1]), fmaxf(sv[2], sv[3])));
  f32x4 p;
#pragma unroll
  for (int q = 0; q < 4; ++q) p[q] = __expf(sv[q] - mw);
  const float lw = xg_sum((p[0] + p[1]) + (p[2] + p[3]));
  *(LAS f32x4*)(c.L + L_PB + c.w * 1024 + c.lane * 16) = p;
  if (g == 0) {
    *(LAS float*)(c.L + L_SB + n * 32 + c.w * 4) = mw;
    *(LAS float*)(c.L + L_SB + 512 + n * 32 + c.w * 4) = lw;
  }
  __syncthreads();
  STAMP(c, 3 + 8 * l);
  float f[8];
  {
    const f32x4 m0 = *(const LAS f32x4*)(c.L + L_SB + n * 32), m1 = *(const LAS f32x4*)(c.L + L_SB + n * 32 + 16);
    const f32x4 l0 = *(const LAS f32x4*)(c.L + L_SB + 512 + n * 32), l1 = *(const LAS f32x4*)(c.L + L_SB + 512 + n * 32 + 16);
    const float M = fmaxf(fmaxf(fmaxf(m0.x, m0.y), fmaxf(m0.z, m0.w)), fmaxf(fmaxf(m1.x, m1.y), fmaxf(m1.z, m1.w)));
    float Ls = 0.f;
#pragma unroll
    for (int i = 0; i < 4; ++i) { f[i] = __expf(m0[i] - M); f[4 + i] = __expf(m1[i] - M); Ls += f[i] * l0[i] + f[4 + i] * l1[i]; }
    if (c.w == 0 && g == 0) {
      v2u st = {canon(__builtin_bit_cast(unsigned, M)), canon(__builtin_bit_cast(unsigned, Ls))};
      st64(c.rs, OFF_ST + (((l * 2 + c.b) * 32 + 16 * c.G + n) * 64 + c.s) * 8, st);
    }
  }
  f32x4 acc[4];
#pragma unroll
  for (int t = 0; t < 4; ++t) acc[t] = f32x4{0.f, 0.f, 0.f, 0.f};
  {
    const unsigned q4 = (c.lane & 15) >> 2, p4 = c.lane & 3;
    const unsigned key = 4u * g + q4;
    const unsigned chunk = 8u * c.w + (p4 >> 1);
    const unsigned ab = L_IMG + key * 1024 + ((chunk ^ swz(key)) << 4) + 8u * (p4 & 1);
#pragma unroll
    for (int u = 0; u < 4; ++u) {
      const f32x4 p0 = *(const LAS f32x4*)(c.L + L_PB + (2 * u) * 1024 + c.lane * 16);
      const f32x4 p1 = *(const LAS f32x4*)(c.L + L_PB + (2 * u + 1) * 1024 + c.lane * 16);
      v4u pb;
      pb.x = pack2(p0.x * f[2 * u], p0.y * f[2 * u]); pb.y = pack2(p0.z * f[2 * u], p0.w * f[2 * u]);
      pb.z = pack2(p1.x * f[2 * u + 1], p1.y * f[2 * u + 1]); pb.w = pack2(p1.z * f[2 * u + 1], p1.w * f[2 * u + 1]);
#pragma unroll
      for (int t = 0; t < 4; ++t) {
        const unsigned ad = (ab ^ (32u * t)) + 32768u * u;
        const s16x4 lo = __builtin_amdgcn_ds_read_tr16_b64_v4i16((LAS s16x4*)(c.L + ad));
        const s16x4 hi = __builtin_amdgcn_ds_read_tr16_b64_v4i16((LAS s16x4*)(c.L + ad + 16384u));
        const v2u l2 = __builtin_bit_cast(v2u, lo), h2 = __builtin_bit_cast(v2u, hi);
        const v4u af = {l2.x, l2.y, h2.x, h2.y};
        acc[t] = mfma(af, pb, acc[t]);
      }
    }
  }
  {
    const int base = OFF_X2 + ((l * 2 + c.b) * 32 + 16 * c.G + n) * 65536 + c.w * 8192 + c.s * 128 + g * 16;
#pragma unroll
    for (int ks = 0; ks < 2; ++ks) {
      v4u o;
      o.x = canon(pack2(acc[2 * ks].x, acc[2 * ks].y)); o.y = canon(pack2(acc[2 * ks].z, acc[2 * ks].w));
      o.z = canon(pack2(acc[2 * ks + 1].x, acc[2 * ks + 1].y)); o.w = canon(pack2(acc[2 * ks + 1].z, acc[2 * ks + 1].w));
      st128(c.rs, base + ks * 64, o);
    }
  }
  STAMP(c, 4 + 8 * l);
}

DI void fill_attn(Ctx& c) {
  const int n = c.lane & 15, g = c.lane >> 4;
  const v4u sv = {SENT, SENT, SENT, SENT};
#pragma unroll
  for (int l = 0; l < 2; ++l) {
    const int base = OFF_X2 + ((l * 2 + c.b) * 32 + 16 * c.G + n) * 65536 + c.w * 8192 + c.s * 128 + g * 16;
    st128(c.rs, base, sv);
    st128(c.rs, base + 64, sv);
    if (c.w == 0 && g == 0) st64(c.rs, OFF_ST + (((l * 2 + c.b) * 32 + 16 * c.G + n) * 64 + c.s) * 8, v2u{SENT, SENT});
  }
}

DI void ckv_issue(Ctx& c, const Params& p, v4u (&t)[16]) {
  const GAS unsigned char* src = (const GAS unsigned char*)p.ckv + ((size_t)c.b * 8192 + c.s * 128 + c.w * 16) * 1024 + c.lane * 16;
#pragma unroll
  for (int i = 0; i < 16; ++i) t[i] = *(const GAS v4u*)(src + i * 1024);
}
DI void ckv_stage(Ctx& c, const v4u (&t)[16]) {
#pragma unroll
  for (int i = 0; i < 16; ++i)
    *(LAS v4u*)(c.L + L_IMG + (c.w * 16 + i) * 1024 + (((unsigned)c.lane ^ swz(i)) << 4)) = t[i];
}

DI void publish_ready(Ctx& c, v4u nonce) {
  if (c.w == 0 && c.lane == 0) st128(c.rs, OFF_RDY + c.blk * 64, nonce);
}

DI void role_q(Ctx& c, const Params& p, v4u nonce, int h, int ch) {
  const int n = c.lane & 15, g = c.lane >> 4;
  const v4u sv = {SENT, SENT, SENT, SENT};
  fill_attn(c);
  if (c.w == 0) {
    if (c.lane < 32) { st128(c.rs, OFF_X1 + ((0 * 2 + c.b) * 32 + h) * 1024 + ch * 512 + c.lane * 16, sv); }
    else { st128(c.rs, OFF_X1 + ((1 * 2 + c.b) * 32 + h) * 1024 + ch * 512 + (c.lane - 32) * 16, sv); }
  }
  const bool xpub = (h == 0 && ch == 0);
  if (xpub && c.w == 1) st128(c.rs, OFF_X3 + (1 * 2 + c.b) * XV_STR + 32 * 1024 + c.lane * 16, sv);
  v4u rq[2][8], ru[8];
  {
    const GAS unsigned char* wq = (const GAS unsigned char*)p.wq + ((size_t)(64 * c.w + 8 * g) * 4096 + h * 128 + 8 * n) * 2;
#pragma unroll
    for (int ks = 0; ks < 2; ++ks)
#pragma unroll
      for (int j = 0; j < 8; ++j) rq[ks][j] = *(const GAS v4u*)(wq + (size_t)(32 * ks + j) * 8192);
    const GAS unsigned char* wu = (const GAS unsigned char*)p.wuk + ((size_t)(256 * ch + 32 * c.w + 2 * n) * 4096 + h * 128 + 8 * g) * 2;
#pragma unroll
    for (int t = 0; t < 2; ++t)
#pragma unroll
      for (int ks = 0; ks < 4; ++ks) ru[t * 4 + ks] = *(const GAS v4u*)(wu + (size_t)t * 8192 + ks * 64);
  }
  float xv[8];
  {
    const int ks = n >> 3;
    const v4u xr = *(const GAS v4u*)((const GAS unsigned char*)p.x + c.b * 1024 + 128 * c.w + 64 * ks + 16 * g);
#pragma unroll
    for (int q = 0; q < 4; ++q) { xv[2 * q] = bf_lo(xr[q]); xv[2 * q + 1] = bf_hi(xr[q]); }
  }
  v4u ct[16];
  ckv_issue(c, p, ct);
  __builtin_amdgcn_sched_barrier(0);
  v4u Wq[16];
  tr8x8(rq[0], Wq); tr8x8(rq[1], Wq + 8);
  auto q_phase = [&](int l) __attribute__((always_inline)) {
    const v4u a0 = layernorm(c, xv);
    gemv_ksplit<false>(c, a0, Wq);
    __syncthreads();
    if (c.w < 2) { const int d = c.w * 64 + c.lane; *(LAS __bf16*)(c.L + L_VEC + d * 2) = (__bf16)red_sum8(c, d); }
    __syncthreads();
    f32x4 acc0 = {0.f, 0.f, 0.f, 0.f}, acc1 = {0.f, 0.f, 0.f, 0.f};
#pragma unroll
    for (int ks = 0; ks < 4; ++ks) {
      const v4u a = *(const LAS v4u*)(c.L + L_VEC + ks * 64 + g * 16);
      acc0 = mfma(a, ru[ks], acc0);
      acc1 = mfma(a, ru[4 + ks], acc1);
    }
    if (c.lane < 16) st32(c.rs, OFF_X1 + ((l * 2 + c.b) * 32 + h) * 1024 + (256 * ch + 32 * c.w + 2 * n) * 2, canon(pack2(acc0.x, acc1.x)));
  };
  q_phase(0);
  STAMP(c, 21);
  ckv_stage(c, ct);
  DRAIN();
  __syncthreads();
  publish_ready(c, nonce);
  STAMP(c, 20);
  wait_ready(c, nonce);
  STAMP(c, 1);
#pragma unroll 1
  for (int l = 0; l < 2; ++l) {
    attn_phase(c, l);
    if (l == 0) {
      gather33<true>(c, OFF_X4 + (0 * 2 + c.b) * XV_STR, nullptr, xv);
      STAMP(c, 5);
      if (xpub && (n & 7) == 0) {
        v4u o;
#pragma unroll
        for (int q = 0; q < 4; ++q) o[q] = canon(pack2(xv[2 * q], xv[2 * q + 1]));
        st128(c.rs, OFF_X3 + (1 * 2 + c.b) * XV_STR + 32 * 1024 + 128 * c.w + 64 * (n >> 3) + 16 * g, o);
      }
      q_phase(1);
      STAMP(c, 6);
      __syncthreads();
    }
  }
  if (c.w == 0) {
    const int qi = 2 * h + ch;
    const int off = OFF_X4 + (1 * 2 + c.b) * XV_STR + (c.lane < 33 ? c.lane : 32) * 1024 + qi * 16;
    v4u v;
    unsigned spins = 0;
    for (;;) {
      CBAR();
      v = ld128(c.rs, off);
      if (__all(ok4(v)) || c.dead) break;
      if (++spins > SPIN_CAP) c.dead = true;
    }
    v4u o;
#pragma unroll
    for (int q = 0; q < 4; ++q) {
      float lo = c.lane < 32 ? bf_lo(v[q]) : 0.f, hi = c.lane < 32 ? bf_hi(v[q]) : 0.f;
      lo = red8_sum(lo); hi = red8_sum(hi);
      lo += dppf<0x128>(lo); hi += dppf<0x128>(hi);
      const float slo = rdlane(lo, 0) + rdlane(lo, 16), shi = rdlane(hi, 0) + rdlane(hi, 16);
      const unsigned ru32 = (unsigned)__builtin_amdgcn_readlane((int)v[q], 32);
      o[q] = pack2(bf_lo(ru32) + rbf(slo), bf_hi(ru32) + rbf(shi));
    }
    if (c.lane == 0) *(GAS v4u*)((GAS unsigned char*)p.out + c.b * 1024 + qi * 16) = o;
    STAMP(c, 16);
  }
}

DI void role_o(Ctx& c, const Params& p, v4u nonce, int h) {
  const int n = c.lane & 15, g = c.lane >> 4;
  const v4u sv = {SENT, SENT, SENT, SENT};
  fill_attn(c);
  if (c.w < 2) st128(c.rs, OFF_X3 + (c.w * 2 + c.b) * XV_STR + h * 1024 + c.lane * 16, sv);
  v4u ct[16];
  ckv_issue(c, p, ct);
  v4u rv[2][8]; v2u ro[4][8];
  {
    const GAS unsigned char* wv = (const GAS unsigned char*)p.wuv + ((size_t)(64 * c.w + 4 * g) * 4096 + h * 128 + 8 * n) * 2;
#pragma unroll
    for (int ks = 0; ks < 2; ++ks)
#pragma unroll
      for (int j = 0; j < 8; ++j) rv[ks][j] = *(const GAS v4u*)(wv + (size_t)(32 * ks + 16 * (j >> 2) + (j & 3)) * 8192);
    const GAS unsigned char* wo = (const GAS unsigned char*)p.wo + ((size_t)(h * 128 + 8 * g) * 512 + 64 * c.w + 4 * n) * 2;
#pragma unroll
    for (int ks = 0; ks < 4; ++ks)
#pragma unroll
      for (int j = 0; j < 8; ++j) ro[ks][j] = *(const GAS v2u*)(wo + (size_t)(32 * ks + j) * 1024);
  }
  __builtin_amdgcn_sched_barrier(0);
  ckv_stage(c, ct);
  v4u Wv[16], Wo[16];
  tr8x8(rv[0], Wv); tr8x8(rv[1], Wv + 8);
#pragma unroll
  for (int ks = 0; ks < 4; ++ks) tr8x4(ro[ks], Wo + 4 * ks);
  DRAIN();
  __syncthreads();
  publish_ready(c, nonce);
  STAMP(c, 20);
  wait_ready(c, nonce);
  STAMP(c, 1);
#pragma unroll 1
  for (int l = 0; l < 2; ++l) {
    attn_phase(c, l);
    const int ks = n >> 3, sg = n & 7;
    const int bx = OFF_X2 + ((l * 2 + c.b) * 32 + h) * 65536 + c.w * 8192 + sg * 128 + ks * 64 + g * 16;
    const int bs = OFF_ST + ((l * 2 + c.b) * 32 + h) * 512 + sg * 8;
    v4u v[8]; v2u st[8];
    unsigned spins = 0;
    for (;;) {
      CBAR();
#pragma unroll
      for (int i = 0; i < 8; ++i) st[i] = ld64(c.rs, bs + i * 64);
#pragma unroll
      for (int i = 0; i < 8; ++i) v[i] = ld128(c.rs, bx + i * 1024);
      bool good = true;
#pragma unroll
      for (int i = 0; i < 8; ++i) good &= ok4(v[i]) & ok2(st[i]);
      if (__all(good) || c.dead) break;
      if (++spins > SPIN_CAP) c.dead = true;
    }
    STAMP(c, 5 + 8 * l);
    float M = u2f(st[0].x);
#pragma unroll
    for (int i = 1; i < 8; ++i) M = fmaxf(M, u2f(st[i].x));
    M = red8_max(M);
    float e[8], Ls = 0.f;
#pragma unroll
    for (int i = 0; i < 8; ++i) { e[i] = __expf(u2f(st[i].x) - M); Ls += e[i] * u2f(st[i].y); }
    Ls = red8_sum(Ls);
    const float inv = 1.0f / Ls;
    v4u a0;
#pragma unroll
    for (int q = 0; q < 4; ++q) {
      float lo = 0.f, hi = 0.f;
#pragma unroll
      for (int i = 0; i < 8; ++i) { lo += e[i] * bf_lo(v[i][q]); hi += e[i] * bf_hi(v[i][q]); }
      lo = red8_sum(lo) * inv; hi = red8_sum(hi) * inv;
      const float lh = rbf(lo), hh = rbf(hi);
      a0[q] = (n & 1) ? pack2(lo - lh, hi - hh) : pack2(lh, hh);
    }
    gemv_ksplit<true>(c, a0, Wv);
    __syncthreads();
    if (c.w < 2) { const int d = c.w * 64 + c.lane; *(LAS __bf16*)(c.L + L_VEC + d * 2) = (__bf16)red_sum8(c, d); }
    __syncthreads();
    f32x4 acc[4];
#pragma unroll
    for (int t = 0; t < 4; ++t) acc[t] = f32x4{0.f, 0.f, 0.f, 0.f};
#pragma unroll
    for (int k4 = 0; k4 < 4; ++k4) {
      const v4u a = *(const LAS v4u*)(c.L + L_VEC + k4 * 64 + g * 16);
#pragma unroll
      for (int t = 0; t < 4; ++t) acc[t] = mfma(a, Wo[4 * k4 + t], acc[t]);
    }
    if (c.lane < 16) {
      v2u o = {canon(pack2(acc[0].x, acc[1].x)), canon(pack2(acc[2].x, acc[3].x))};
      st64(c.rs, OFF_X3 + (l * 2 + c.b) * XV_STR + h * 1024 + (64 * c.w + 4 * n) * 2, o);
    }
    STAMP(c, 6 + 8 * l);
  }
}

DI void role_f(Ctx& c, const Params& p, v4u nonce, int j) {
  const int n = c.lane & 15, g = c.lane >> 4;
  const v4u sv = {SENT, SENT, SENT, SENT};
  fill_attn(c);
  if (c.w < 2) st128(c.rs, OFF_X4 + (c.w * 2 + c.b) * XV_STR + j * 1024 + c.lane * 16, sv);
  if (j == 0 && (c.w == 2 || c.w == 3)) st128(c.rs, OFF_X4 + ((c.w - 2) * 2 + c.b) * XV_STR + 32 * 1024 + c.lane * 16, sv);
  v4u ct[16];
  ckv_issue(c, p, ct);
  v4u r13[2][8]; v2u r2[2][8];
  {
    const int nn = n < 14 ? n : 13;
    const u16* wsel = nn < 7 ? p.w1 : p.w3;
    const int col = 56 * j + 8 * (nn < 7 ? nn : nn - 7);
    const GAS unsigned char* wa = (const GAS unsigned char*)wsel + ((size_t)(64 * c.w + 8 * g) * 1792 + col) * 2;
#pragma unroll
    for (int ks = 0; ks < 2; ++ks)
#pragma unroll
      for (int jj = 0; jj < 8; ++jj) r13[ks][jj] = *(const GAS v4u*)(wa + (size_t)(32 * ks + jj) * 3584);
#pragma unroll
    for (int ks = 0; ks < 2; ++ks)
#pragma unroll
      for (int jj = 0; jj < 8; ++jj) {
        int f = 32 * ks + 8 * g + jj; f = f < 56 ? f : 55;
        r2[ks][jj] = *(const GAS v2u*)((const GAS unsigned char*)p.w2 + ((size_t)(56 * j + f) * 512 + 64 * c.w + 4 * n) * 2);
      }
  }
  __builtin_amdgcn_sched_barrier(0);
  ckv_stage(c, ct);
  v4u W13[16], W2[8];
  tr8x8(r13[0], W13); tr8x8(r13[1], W13 + 8);
  tr8x4(r2[0], W2); tr8x4(r2[1], W2 + 4);
  DRAIN();
  __syncthreads();
  publish_ready(c, nonce);
  STAMP(c, 20);
  wait_ready(c, nonce);
  STAMP(c, 1);
#pragma unroll 1
  for (int l = 0; l < 2; ++l) {
    attn_phase(c, l);
    float xv[8];
    if (l == 0) gather33<false>(c, OFF_X3 + (0 * 2 + c.b) * XV_STR, p.x + c.b * 512, xv);
    else gather33<true>(c, OFF_X3 + (1 * 2 + c.b) * XV_STR, nullptr, xv);
    STAMP(c, 5 + 8 * l);
    if (j == 0 && (n & 7) == 0) {
      v4u o;
#pragma unroll
      for (int q = 0; q < 4; ++q) o[q] = canon(pack2(xv[2 * q], xv[2 * q + 1]));
      st128(c.rs, OFF_X4 + (l * 2 + c.b) * XV_STR + 32 * 1024 + 128 * c.w + 64 * (n >> 3) + 16 * g, o);
    }
    const v4u a0 = layernorm(c, xv);
    STAMP(c, 7 + 8 * l);
    gemv_ksplit<false>(c, a0, W13);
    STAMP(c, 22 + 2 * l);
    __syncthreads();
    STAMP(c, 8 + 8 * l);
    if (c.w == 0) {
      float hv = 0.f;
      if (c.lane < 56) {
        const float gt = rbf(red_sum8(c, c.lane)), up = rbf(red_sum8(c, 56 + c.lane));
        const float sg = rbf(gt / (1.0f + __expf(-gt)));
        hv = sg * up;
      }
      *(LAS __bf16*)(c.L + L_VEC + c.lane * 2) = (__bf16)hv;
    }
    __syncthreads();
    STAMP(c, 23 + 2 * l);
    f32x4 acc[4];
#pragma unroll
    for (int t = 0; t < 4; ++t) acc[t] = f32x4{0.f, 0.f, 0.f, 0.f};
#pragma unroll
    for (int k2 = 0; k2 < 2; ++k2) {
      const v4u a = *(const LAS v4u*)(c.L + L_VEC + k2 * 64 + g * 16);
#pragma unroll
      for (int t = 0; t < 4; ++t) acc[t] = mfma(a, W2[4 * k2 + t], acc[t]);
    }
    if (c.lane < 16) {
      v2u o = {canon(pack2(acc[0].x, acc[1].x)), canon(pack2(acc[2].x, acc[3].x))};
      st64(c.rs, OFF_X4 + (l * 2 + c.b) * XV_STR + j * 1024 + (64 * c.w + 4 * n) * 2, o);
    }
    STAMP(c, 6 + 8 * l);
  }
}

__global__ void __launch_bounds__(NT, 2) mega(Params p) {
  extern __shared__ __attribute__((aligned(16))) unsigned char smem[];
  Ctx c;
  c.rs = __builtin_amdgcn_make_buffer_rsrc((void*)p.ws, 0, WS_BYTES, 0x00020000);
  c.L = (LAS unsigned char*)smem;
  c.lane = threadIdx.x & 63;
  c.w = __builtin_amdgcn_readfirstlane(threadIdx.x >> 6);
  c.blk = blockIdx.x;
  const int x = c.blk & 7, slot = c.blk >> 3, r = slot & 15;
  c.b = slot >> 4; c.G = r >> 3; c.s = 8 * x + (r & 7);
  c.dead = false; c.wsp = p.ws;
  STAMP(c, 0);
  const unsigned long long id = llvm_dispatch_id();
  const unsigned long long dp = (unsigned long long)__builtin_amdgcn_dispatch_ptr();
  const v4u nonce = {(unsigned)id ^ 0x5bd1e995u, (unsigned)(id >> 32) ^ 0x9e3779b9u, (unsigned)dp ^ 0x85ebca6bu, (unsigned)(dp >> 32) ^ 0xc2b2ae35u};
  if (r < 8) role_q(c, p, nonce, 4 * x + (r >> 1), r & 1);
  else if (r < 12) role_o(c, p, nonce, 4 * x + (r - 8));
  else role_f(c, p, nonce, 4 * x + (r - 12));
  if (c.w == 0 && c.lane == 0) st128(c.rs, OFF_RDY + c.blk * 64, v4u{~nonce.x, ~nonce.y, ~nonce.z, ~nonce.w});
}
}

extern "C" void kernel_launch(void* const* d_in, const int* in_sizes, int n_in,
                              void* d_out, int out_size, void* d_ws, size_t ws_size,
                              hipStream_t stream) {
  mk::Params p;
  p.x = (const u16*)d_in[0]; p.ckv = (const u16*)d_in[1]; p.wq = (const u16*)d_in[2]; p.wuk = (const u16*)d_in[3];
  p.wuv = (const u16*)d_in[4]; p.wo = (const u16*)d_in[5]; p.w1 = (const u16*)d_in[7]; p.w3 = (const u16*)d_in[8];
  p.w2 = (const u16*)d_in[9]; p.out = (u16*)d_out; p.ws = (unsigned char*)d_ws;
  (void)hipFuncSetAttribute((const void*)mk::mega, hipFuncAttributeMaxDynamicSharedMemorySize, mk::L_BYTES + 2048);
  hipLaunchKernelGGL(mk::mega, dim3(256), dim3(mk::NT), mk::L_BYTES + 2048, stream, p);
}
```

```cpp
#include <hip/hip_runtime.h>
#include <hip/hip_bf16.h>
#include <stdint.h>

#define LAS __attribute__((address_space(3)))
#define GAS __attribute__((address_space(1)))
typedef unsigned short u16;
typedef unsigned v4u __attribute__((ext_vector_type(4)));
typedef unsigned v2u __attribute__((ext_vector_type(2)));
typedef float f32x4 __attribute__((ext_vector_type(4)));
typedef float f32x2 __attribute__((ext_vector_type(2)));
typedef short bf16x8 __attribute__((ext_vector_type(8)));
typedef short s16x4 __attribute__((ext_vector_type(4)));
typedef __bf16 bf16x2_t __attribute__((ext_vector_type(2)));
typedef unsigned short u16x2 __attribute__((ext_vector_type(2)));
#define DI __device__ __forceinline__

extern "C" __device__ unsigned long long llvm_dispatch_id() __asm("llvm.amdgcn.dispatch.id");

namespace mk {
constexpr int NT = 512;
constexpr unsigned SENT = 0xFFFFFFFFu;
constexpr unsigned SPIN_CAP = 1u << 14;
constexpr int AUX_SC1 = 16;
#define LO1 (c.kLO1)
#define HI1 (c.kHI1)
#define ONES (c.kONES)
#define NLO1 (c.kNLO1)
#define NHI1 (c.kNHI1)

constexpr int OFF_RDY = 0;
constexpr int OFF_X1 = 16384;
constexpr int OFF_ST = OFF_X1 + 2 * 2 * 32 * 1024;
constexpr int OFF_X3 = OFF_ST + 2 * 2 * 32 * 512;
constexpr int XV_STR = 34 * 1024;
constexpr int OFF_X4 = OFF_X3 + 4 * XV_STR;
constexpr int OFF_X2 = OFF_X4 + 4 * XV_STR;
constexpr int WS_BYTES = OFF_X2 + 2 * 2 * 32 * 65536;

constexpr int L_IMG = 0;
constexpr int L_QA = 131072;
constexpr int L_PB = L_QA + 16384;
constexpr int L_SB = L_PB + 4096;
constexpr int L_LN = L_SB + 1024;
constexpr int L_VEC = L_LN + 64;
constexpr int L_EW = L_VEC + 256;
constexpr int L_FR = L_EW + 2048;
constexpr int L_BYTES = L_FR + 2048;
constexpr int L_RED = L_QA;

struct Params {
  const u16* x; const u16* ckv; const u16* wq; const u16* wuk; const u16* wuv; const u16* wo;
  const u16* w1; const u16* w3; const u16* w2; u16* out; unsigned char* ws;
};

DI float u2f(unsigned u) { return __builtin_bit_cast(float, u); }
DI unsigned f2u(float f) { return __builtin_bit_cast(unsigned, f); }
DI float bf_lo(unsigned u) { return u2f(u << 16); }
DI float bf_hi(unsigned u) { return u2f(u & 0xFFFF0000u); }
DI float rbf(float f) { return (float)(__bf16)f; }
DI unsigned pack2(float lo, float hi) { bf16x2_t v = {(__bf16)lo, (__bf16)hi}; return __builtin_bit_cast(unsigned, v); }
DI unsigned canon(unsigned u) { return u < 0xFFFFFFFEu ? u : 0xFFFFFFFEu; }
DI float dot2(unsigned a, unsigned b, float c) { return __builtin_amdgcn_fdot2_f32_bf16(__builtin_bit_cast(bf16x2_t, a), __builtin_bit_cast(bf16x2_t, b), c, false); }
DI unsigned subsat(unsigned a, unsigned b) { return __builtin_bit_cast(unsigned, __builtin_elementwise_sub_sat(__builtin_bit_cast(u16x2, a), __builtin_bit_cast(u16x2, b))); }
DI unsigned umax3(unsigned a, unsigned b, unsigned c) { return max(max(a, b), c); }
DI f32x4 mfma(v4u a, v4u b, f32x4 c) { return __builtin_amdgcn_mfma_f32_16x16x32_bf16(__builtin_bit_cast(bf16x8, a), __builtin_bit_cast(bf16x8, b), c, 0, 0, 0); }
template <int CTRL> DI float dppf(float v) { return u2f((unsigned)__builtin_amdgcn_update_dpp(0, (int)f2u(v), CTRL, 0xf, 0xf, true)); }
template <int CTRL> DI unsigned dppu(unsigned v) { return (unsigned)__builtin_amdgcn_update_dpp(0, (int)v, CTRL, 0xf, 0xf, true); }
template <int CTRL, int RM> DI float dppm(float v) { return u2f((unsigned)__builtin_amdgcn_update_dpp(0, (int)f2u(v), CTRL, RM, 0xf, false)); }
DI float quad_sum(float v) { v += dppf<0xB1>(v); v += dppf<0x4E>(v); return v; }
DI float xg_sum(float v) {
  const auto a = __builtin_amdgcn_permlane16_swap(f2u(v), f2u(v), false, false);
  const unsigned a0 = a[0], a1 = a[1];
  v = u2f(a0) + u2f(a1);
  const auto b = __builtin_amdgcn_permlane32_swap(f2u(v), f2u(v), false, false);
  const unsigned b0 = b[0], b1 = b[1];
  return u2f(b0) + u2f(b1);
}
DI float x32_sum(float v) {
  const auto b = __builtin_amdgcn_permlane32_swap(f2u(v), f2u(v), false, false);
  const unsigned b0 = b[0], b1 = b[1];
  return u2f(b0) + u2f(b1);
}
DI float xg_max(float v) {
  const auto a = __builtin_amdgcn_permlane16_swap(f2u(v), f2u(v), false, false);
  const unsigned a0 = a[0], a1 = a[1];
  v = fmaxf(u2f(a0), u2f(a1));
  const auto b = __builtin_amdgcn_permlane32_swap(f2u(v), f2u(v), false, false);
  const unsigned b0 = b[0], b1 = b[1];
  return fmaxf(u2f(b0), u2f(b1));
}
DI unsigned swz(unsigned r) { return (((r ^ ((r >> 1) & 4u)) & 7u) << 1) | ((r >> 3) & 1u); }

typedef __amdgpu_buffer_rsrc_t rsrc_t;
DI v4u ld128(rsrc_t rs, int off) { return __builtin_amdgcn_raw_buffer_load_b128(rs, off, 0, AUX_SC1); }
DI v2u ld64(rsrc_t rs, int off) { return __builtin_amdgcn_raw_buffer_load_b64(rs, off, 0, AUX_SC1); }
DI unsigned ld32(rsrc_t rs, int off) { return __builtin_amdgcn_raw_buffer_load_b32(rs, off, 0, AUX_SC1); }
DI void st128(rsrc_t rs, int off, v4u v) { __builtin_amdgcn_raw_buffer_store_b128(v, rs, off, 0, AUX_SC1); }
DI void st64(rsrc_t rs, int off, v2u v) { __builtin_amdgcn_raw_buffer_store_b64(v, rs, off, 0, AUX_SC1); }
DI void st32(rsrc_t rs, int off, unsigned v) { __builtin_amdgcn_raw_buffer_store_b32(v, rs, off, 0, AUX_SC1); }
#define CBAR() asm volatile("" ::: "memory")
#define DRAIN() asm volatile("s_waitcnt vmcnt(0)" ::: "memory")
#define LBAR() do { asm volatile("s_waitcnt lgkmcnt(0)" ::: "memory"); __builtin_amdgcn_s_barrier(); asm volatile("" ::: "memory"); } while (0)

DI void tr8x8(const v4u (&r)[8], v4u* f) {
#pragma unroll
  for (int q = 0; q < 4; ++q)
#pragma unroll
    for (int d = 0; d < 4; ++d) {
      const unsigned a = r[2 * d][q], b = r[2 * d + 1][q];
      f[2 * q][d] = __builtin_amdgcn_perm(b, a, 0x05040100u);
      f[2 * q + 1][d] = __builtin_amdgcn_perm(b, a, 0x07060302u);
    }
}
DI void tr8x4(const v2u (&r)[8], v4u* f) {
#pragma unroll
  for (int q = 0; q < 2; ++q)
#pragma unroll
    for (int d = 0; d < 4; ++d) {
      const unsigned a = r[2 * d][q], b = r[2 * d + 1][q];
      f[2 * q][d] = __builtin_amdgcn_perm(b, a, 0x05040100u);
      f[2 * q + 1][d] = __builtin_amdgcn_perm(b, a, 0x07060302u);
    }
}

#define STAMP(c, i) do {} while (0)
struct Ctx {
  unsigned char* wsp;
  rsrc_t rs; LAS unsigned char* L;
  int lane, w, b, G, s, blk;
  unsigned spins;
  unsigned kLO1, kHI1, kONES, kNLO1, kNHI1;
};
DI unsigned sconst(unsigned v) { unsigned r; asm volatile("s_mov_b32 %0, %1" : "=s"(r) : "i"(v)); return r; }
#define RELANE(c) do { int l_ = (c).lane; asm volatile("" : "+v"(l_)); (c).lane = l_; } while (0)
DI bool spin_ok(Ctx& c) { return ++c.spins < SPIN_CAP; }

DI int q_blk(int b, int h, int ch) { return (h >> 2) + 8 * (8 * b + 2 * (h & 3) + ch); }

DI bool nonce_eq(v4u a, v4u n) { return ((a.x ^ n.x) | (a.y ^ n.y) | (a.z ^ n.z) | (a.w ^ n.w)) == 0u; }
DI void wait_ready_a(Ctx& c, v4u nonce) {
  const int q = c.lane & 3;
  const int off = OFF_RDY + q_blk(c.b, 16 * c.G + c.w + 8 * (q >> 1), q & 1) * 64;
  for (;;) {
    CBAR();
    const v4u a = ld128(c.rs, off);
    if (__all(nonce_eq(a, nonce)) || !spin_ok(c)) break;
  }
  CBAR();
}
DI void wait_ready_b(Ctx& c, v4u nonce) {
  const int o0 = OFF_RDY + (c.b * 64 + c.lane) * 64 + 16, o1 = o0 + 128 * 64;
  for (;;) {
    CBAR();
    const v4u a = ld128(c.rs, o0), bq = ld128(c.rs, o1);
    if (__all(nonce_eq(a, nonce) & nonce_eq(bq, nonce)) || !spin_ok(c)) break;
  }
  CBAR();
}

template <int D>
DI void gate(Ctx& c, int off) {
  if (c.w == 0) {
    if (off >= 0) {
      for (;;) {
        CBAR();
        const unsigned v = __builtin_amdgcn_raw_buffer_load_b32(c.rs, off, 0, AUX_SC1);
        if (__builtin_amdgcn_readfirstlane(v) != SENT || !spin_ok(c)) break;
        __builtin_amdgcn_s_sleep(4);
      }
    }
    if (D > 0) __builtin_amdgcn_s_sleep(D);
  }
  __builtin_amdgcn_s_barrier();
}
#define D_F3 40
#define D_Q4 30
#define D_A1 30
#define D_FA1 45

template <bool RES_WS>
DI unsigned gather33(Ctx& c, int base, const u16* xin) {
  const int co = 128 * c.w + 4 * (c.lane & 31);
  unsigned v[16], res;
  if (!RES_WS) res = *(const GAS unsigned*)((const GAS unsigned char*)xin + co);
  const int o = base + (c.lane >> 5) * 1024 + co;
  for (;;) {
    CBAR();
#pragma unroll
    for (int i = 0; i < 16; ++i) v[i] = ld32(c.rs, o + i * 2048);
    if (RES_WS) res = ld32(c.rs, base + 32 * 1024 + co);
    unsigned m = RES_WS ? res : 0u;
#pragma unroll
    for (int i = 0; i < 16; i += 2) m = umax3(m, v[i], v[i + 1]);
    if (__all(m != SENT) || !spin_ok(c)) break;
  }
  float s0 = 0.f, s1 = 0.f, s2 = 0.f, s3 = 0.f;
#pragma unroll
  for (int i = 0; i < 16; i += 2) { s0 = dot2(v[i], LO1, s0); s1 = dot2(v[i], HI1, s1); s2 = dot2(v[i + 1], LO1, s2); s3 = dot2(v[i + 1], HI1, s3); }
  const unsigned o01 = pack2(x32_sum(s0 + s2), x32_sum(s1 + s3));
  return pack2(dot2(o01, LO1, bf_lo(res)), dot2(o01, HI1, bf_hi(res)));
}

DI void layernorm(Ctx& c, unsigned xp, v4u& a0, v4u& a1) {
  float t1 = dot2(xp, ONES, 0.f), t2 = dot2(xp, xp, 0.f);
  t1 += dppf<0x121>(t1); t2 += dppf<0x121>(t2);
  t1 += dppf<0x122>(t1); t2 += dppf<0x122>(t2);
  t1 += dppf<0x124>(t1); t2 += dppf<0x124>(t2);
  t1 += dppf<0x128>(t1); t2 += dppf<0x128>(t2);
  t1 += dppm<0x142, 0xa>(t1); t2 += dppm<0x142, 0xa>(t2);
  if (c.lane == 31) *(LAS f32x2*)(c.L + L_LN + c.w * 8) = f32x2{t1, t2};
  LBAR();
  f32x4 t[4];
#pragma unroll
  for (int i = 0; i < 4; ++i) t[i] = *(const LAS f32x4*)(c.L + L_LN + i * 16);
  const f32x4 tt = (t[0] + t[1]) + (t[2] + t[3]);
  const float mean = (tt.x + tt.z) * (1.0f / 512.0f);
  const float var = fmaxf((tt.y + tt.w) * (1.0f / 512.0f) - mean * mean, 0.f);
  const unsigned mv = pack2(mean, var);
  const float nmb = -bf_lo(mv);
  const float sd = rbf(__builtin_amdgcn_sqrtf(rbf(bf_hi(mv) + 1e-5f)));
  const float ri = __builtin_amdgcn_rcpf(sd);
  const unsigned d01 = pack2(dot2(xp, LO1, nmb), dot2(xp, HI1, nmb));
  const unsigned n01 = pack2(bf_lo(d01) * ri, bf_hi(d01) * ri);
  *(LAS unsigned*)(c.L + L_FR + c.w * 256 + c.lane * 4) = n01;
  a0 = *(const LAS v4u*)(c.L + L_FR + c.w * 256 + (c.lane >> 4) * 32);
  a1 = *(const LAS v4u*)(c.L + L_FR + c.w * 256 + (c.lane >> 4) * 32 + 16);
}

template <bool HILO>
DI void gemv_ksplit(Ctx& c, v4u a0, v4u a1, const v4u* W) {
  f32x4 acc[8];
#pragma unroll
  for (int t = 0; t < 8; ++t) acc[t] = mfma(a0, W[t], f32x4{0.f, 0.f, 0.f, 0.f});
#pragma unroll
  for (int t = 0; t < 8; ++t) acc[t] = mfma(a1, W[8 + t], acc[t]);
  if (c.lane < 16) {
    f32x4 o0, o1;
#pragma unroll
    for (int t = 0; t < 4; ++t) { o0[t] = HILO ? acc[t].x + acc[t].y : acc[t].x; o1[t] = HILO ? acc[4 + t].x + acc[4 + t].y : acc[4 + t].x; }
    *(LAS f32x4*)(c.L + L_RED + c.w * 512 + c.lane * 32) = o0;
    *(LAS f32x4*)(c.L + L_RED + c.w * 512 + c.lane * 32 + 16) = o1;
  }
}
DI float red_sum8(Ctx& c, int idx) {
  const LAS float* R = (const LAS float*)(c.L + L_RED);
  return ((R[idx] + R[128 + idx]) + (R[256 + idx] + R[384 + idx])) + ((R[512 + idx] + R[640 + idx]) + (R[768 + idx] + R[896 + idx]));
}
DI void reduce128(Ctx& c) {
  LBAR();
  if (c.w < 2) { const int d = c.w * 64 + c.lane; *(LAS __bf16*)(c.L + L_VEC + d * 2) = (__bf16)red_sum8(c, d); }
  LBAR();
}

DI float score_round(float s) {
  const float SC = 0.08838834764831845f;
  const unsigned bits = f2u(s);
  const unsigned tr = bits & 0xFFFF0000u;
  const unsigned g2 = pack2(u2f(tr) * SC, u2f(tr + 0x10000u) * SC);
  const float ga = bf_lo(g2);
  const float wgt = fminf(fmaxf((float)(bits & 0xFFFFu) * (1.0f / (65536.0f * 0.6f)) + (0.5f - 0.5f / 0.6f), 0.f), 1.f);
  return ga + wgt * (bf_hi(g2) - ga);
}

DI void attn_phase(Ctx& c, int l) {
  RELANE(c);
  const int tid = c.w * 64 + c.lane, n = c.lane & 15, g = c.lane >> 4;
  v4u kf[16];
  {
    const unsigned kb = L_IMG + (c.w * 16 + n) * 1024 + (((unsigned)g ^ swz(n)) << 4);
#pragma unroll
    for (int ks = 0; ks < 16; ++ks) kf[ks] = *(const LAS v4u*)(c.L + ((kb ^ (64u * (ks & 3))) + 256u * (ks >> 2)));
  }
  unsigned ab;
  {
    const unsigned q4 = (c.lane & 15) >> 2, p4 = c.lane & 3;
    const unsigned key = 4u * g + q4;
    const unsigned chunk = 8u * c.w + (p4 >> 1);
    ab = L_IMG + key * 1024 + ((chunk ^ swz(key)) << 4) + 8u * (p4 & 1);
    asm volatile("" : "+v"(ab));
  }
  {
    const int base = OFF_X1 + ((l * 2 + c.b) * 32 + 16 * c.G) * 1024 + tid * 16;
    v4u v0, v1;
    for (;;) {
      CBAR();
      v0 = ld128(c.rs, base);
      v1 = ld128(c.rs, base + 8192);
      const unsigned m = max(umax3(umax3(v0.x, v0.y, v0.z), v0.w, v1.x), umax3(v1.y, v1.z, v1.w));
      if (__all(m != SENT) || !spin_ok(c)) break;
    }
    STAMP(c, 2 + 8 * l);
    *(LAS v4u*)(c.L + L_QA + c.w * 1024 + (((unsigned)c.lane ^ swz(c.w)) << 4)) = v0;
    *(LAS v4u*)(c.L + L_QA + (c.w + 8) * 1024 + (((unsigned)c.lane ^ swz(c.w + 8)) << 4)) = v1;
  }
  LBAR();
  STAMP(c, 26 + l);
  f32x4 sacc = {0.f, 0.f, 0.f, 0.f}, sacc2 = {0.f, 0.f, 0.f, 0.f};
  {
    const unsigned qb = L_QA + n * 1024 + (((unsigned)g ^ swz(n)) << 4);
    constexpr int QD = 4;
    v4u qf[QD];
#pragma unroll
    for (int ks = 0; ks < QD; ++ks) qf[ks] = *(const LAS v4u*)(c.L + ((qb ^ (64u * (ks & 3))) + 256u * (ks >> 2)));
    __builtin_amdgcn_sched_barrier(0);
#pragma unroll
    for (int ks = 0; ks < 16; ++ks) {
      if (ks & 1) sacc2 = mfma(kf[ks], qf[ks % QD], sacc2); else sacc = mfma(kf[ks], qf[ks % QD], sacc);
      if (ks + QD < 16) qf[ks % QD] = *(const LAS v4u*)(c.L + ((qb ^ (64u * ((ks + QD) & 3))) + 256u * ((ks + QD) >> 2)));
      __builtin_amdgcn_sched_barrier(0);
    }
  }
  if (l == 1) { asm volatile("" :: "v"(sacc), "v"(sacc2)); STAMP(c, 17); }
  v2u vf[4][4][2];
  auto vf_issue = [&](int u) __attribute__((always_inline)) {
#pragma unroll
    for (int t = 0; t < 4; ++t) {
      const unsigned ad = (ab ^ (32u * t)) + 32768u * u;
      const s16x4 lo = __builtin_amdgcn_ds_read_tr16_b64_v4i16((LAS s16x4*)(c.L + ad));
      const s16x4 hi = __builtin_amdgcn_ds_read_tr16_b64_v4i16((LAS s16x4*)(c.L + ad + 16384u));
      vf[u][t][0] = __builtin_bit_cast(v2u, lo); vf[u][t][1] = __builtin_bit_cast(v2u, hi);
    }
  };
  vf_issue(0); vf_issue(1);
  __builtin_amdgcn_sched_barrier(0);
  {
    const float LOG2E = 1.4426950408889634f;
    float sv[4];
#pragma unroll
    for (int q = 0; q < 4; ++q) sv[q] = score_round(sacc[q] + sacc2[q]) * LOG2E;
    const float kw = ceilf(xg_max(fmaxf(fmaxf(sv[0], sv[1]), fmaxf(sv[2], sv[3]))));
    float p[4];
#pragma unroll
    for (int q = 0; q < 4; ++q) p[q] = __builtin_amdgcn_exp2f(sv[q] - kw);
    const float lw = xg_sum((p[0] + p[1]) + (p[2] + p[3]));
    *(LAS v2u*)(c.L + L_PB + (c.w >> 1) * 1024 + c.lane * 16 + (c.w & 1) * 8) = v2u{pack2(p[0], p[1]), pack2(p[2], p[3])};
    if (g == 0) *(LAS v2u*)(c.L + L_SB + n * 64 + c.w * 8) = v2u{(unsigned)(int)kw, f2u(lw)};
  }
  if (l == 1) STAMP(c, 18);
  LBAR();
  STAMP(c, 3 + 8 * l);
  f32x4 acc[4];
  {
    v4u sb[4], pr[4];
#pragma unroll
    for (int i = 0; i < 4; ++i) sb[i] = *(const LAS v4u*)(c.L + L_SB + n * 64 + i * 16);
#pragma unroll
    for (int u = 0; u < 4; ++u) pr[u] = *(const LAS v4u*)(c.L + L_PB + u * 1024 + c.lane * 16);
    vf_issue(2); vf_issue(3);
    __builtin_amdgcn_sched_barrier(0);
    int K = max(max(max((int)sb[0].x, (int)sb[0].z), max((int)sb[1].x, (int)sb[1].z)), max(max((int)sb[2].x, (int)sb[2].z), max((int)sb[3].x, (int)sb[3].z)));
    if (c.w == 0) {
      float Ls = 0.f;
#pragma unroll
      for (int i = 0; i < 4; ++i) Ls += ldexpf(u2f(sb[i].y), (int)sb[i].x - K) + ldexpf(u2f(sb[i].w), (int)sb[i].z - K);
      if (g == 0) st64(c.rs, OFF_ST + (((l * 2 + c.b) * 32 + 16 * c.G + n) * 64 + c.s) * 8, v2u{canon(f2u((float)K)), canon(f2u(Ls))});
    }
#pragma unroll
    for (int t = 0; t < 4; ++t) acc[t] = f32x4{0.f, 0.f, 0.f, 0.f};
#pragma unroll
    for (int u = 0; u < 4; ++u) {
      const unsigned d0 = (unsigned)min(K - (int)sb[u].x, 255) * 0x00800080u, d1 = (unsigned)min(K - (int)sb[u].z, 255) * 0x00800080u;
      const v4u pb = {subsat(pr[u].x, d0), subsat(pr[u].y, d0), subsat(pr[u].z, d1), subsat(pr[u].w, d1)};
#pragma unroll
      for (int t = 0; t < 4; ++t) {
        const v4u af = {vf[u][t][0].x, vf[u][t][0].y, vf[u][t][1].x, vf[u][t][1].y};
        acc[t] = mfma(pb, af, acc[t]);
      }
    }
  }
  if (l == 1) { asm volatile("" :: "v"(acc[0]), "v"(acc[1]), "v"(acc[2]), "v"(acc[3])); STAMP(c, 19); }
  {
    const int base = OFF_X2 + ((l * 2 + c.b) * 32 + 16 * c.G + 4 * g) * 65536 + c.w * 8192 + c.s * 128 + n * 8;
#pragma unroll
    for (int q = 0; q < 4; ++q)
      st64(c.rs, base + q * 65536, v2u{canon(pack2(acc[0][q], acc[1][q])), canon(pack2(acc[2][q], acc[3][q]))});
  }
  STAMP(c, 4 + 8 * l);
}

DI void fill_attn(Ctx& c) {
  const int n = c.lane & 15, g = c.lane >> 4;
  const v4u sv = {SENT, SENT, SENT, SENT};
#pragma unroll
  for (int l = 0; l < 2; ++l) {
    const int base = OFF_X2 + ((l * 2 + c.b) * 32 + 16 * c.G + (c.lane >> 3)) * 65536 + c.w * 8192 + c.s * 128 + (c.lane & 7) * 16;
    st128(c.rs, base, sv);
    st128(c.rs, base + 8 * 65536, sv);
    if (c.w == 0 && g == 0) st64(c.rs, OFF_ST + (((l * 2 + c.b) * 32 + 16 * c.G + n) * 64 + c.s) * 8, v2u{SENT, SENT});
  }
}

DI void ckv_issue(Ctx& c, const Params& p, v4u (&t)[16]) {
  const GAS unsigned char* src = (const GAS unsigned char*)p.ckv + ((size_t)c.b * 8192 + c.s * 128 + c.w * 16) * 1024 + c.lane * 16;
#pragma unroll
  for (int i = 0; i < 16; ++i) t[i] = *(const GAS v4u*)(src + i * 1024);
}
DI void ckv_stage(Ctx& c, const v4u (&t)[16]) {
#pragma unroll
  for (int i = 0; i < 16; ++i)
    *(LAS v4u*)(c.L + L_IMG + (c.w * 16 + i) * 1024 + (((unsigned)c.lane ^ swz(i)) << 4)) = t[i];
}
DI void publish_ready_b(Ctx& c, v4u nonce) {
  DRAIN();
  LBAR();
  if (c.w == 0 && c.lane == 0) st128(c.rs, OFF_RDY + c.blk * 64 + 16, nonce);
}

DI void role_q(Ctx& c, const Params& p, v4u nonce, int h, int ch) {
  const int n = c.lane & 15, g = c.lane >> 4;
  const v4u sv = {SENT, SENT, SENT, SENT};
  if (c.w == 0) st128(c.rs, OFF_X1 + (((c.lane >> 5) * 2 + c.b) * 32 + h) * 1024 + ch * 512 + (c.lane & 31) * 16, sv);
  unsigned xp = *(const GAS unsigned*)((const GAS unsigned char*)p.x + c.b * 1024 + 128 * c.w + 4 * (c.lane & 31));
  v4u rq[2][8], ru[8];
  {
    const GAS unsigned char* wq = (const GAS unsigned char*)p.wq + ((size_t)(64 * c.w + 16 * g) * 4096 + h * 128 + 8 * n) * 2;
#pragma unroll
    for (int ks = 0; ks < 2; ++ks)
#pragma unroll
      for (int j = 0; j < 8; ++j) rq[ks][j] = *(const GAS v4u*)(wq + (size_t)(8 * ks + j) * 8192);
    const GAS unsigned char* wu = (const GAS unsigned char*)p.wuk + ((size_t)(256 * ch + 32 * c.w + 2 * n) * 4096 + h * 128 + 8 * g) * 2;
#pragma unroll
    for (int t = 0; t < 2; ++t)
#pragma unroll
      for (int ks = 0; ks < 4; ++ks) ru[t * 4 + ks] = *(const GAS v4u*)(wu + (size_t)t * 8192 + ks * 64);
  }
  v4u ct[16];
  __builtin_amdgcn_sched_barrier(0);
  asm volatile("" : "+v"(xp));
  STAMP(c, 9);
  if (c.w == 0 && c.lane == 0) st128(c.rs, OFF_RDY + c.blk * 64, nonce);
  v4u Wq[16];
  const bool xpub = (h == 0 && ch == 0);
  auto q_phase = [&](int l) __attribute__((always_inline)) {
    v4u a0, a1;
    layernorm(c, xp, a0, a1);
    if (l == 0) { ckv_issue(c, p, ct); __builtin_amdgcn_sched_barrier(0); STAMP(c, 7); tr8x8(rq[0], Wq); tr8x8(rq[1], Wq + 8); asm volatile("" :: "v"(Wq[15])); STAMP(c, 22); }
    gemv_ksplit<false>(c, a0, a1, Wq);
    reduce128(c);
    if (l == 0) STAMP(c, 23);
    f32x4 acc0 = {0.f, 0.f, 0.f, 0.f}, acc1 = {0.f, 0.f, 0.f, 0.f};
#pragma unroll
    for (int ks = 0; ks < 4; ++ks) {
      const v4u a = *(const LAS v4u*)(c.L + L_VEC + ks * 64 + g * 16);
      acc0 = mfma(a, ru[ks], acc0);
      acc1 = mfma(a, ru[4 + ks], acc1);
    }
    if (c.lane < 16) st32(c.rs, OFF_X1 + ((l * 2 + c.b) * 32 + h) * 1024 + (256 * ch + 32 * c.w + 2 * n) * 2, canon(pack2(acc0.x, acc1.x)));
  };
  q_phase(0);
  STAMP(c, 21);
  fill_attn(c);
  if (xpub && c.w == 1) st128(c.rs, OFF_X3 + (1 * 2 + c.b) * XV_STR + 32 * 1024 + c.lane * 16, sv);
  ckv_stage(c, ct);
  publish_ready_b(c, nonce);
  STAMP(c, 20);
  wait_ready_a(c, nonce);
  STAMP(c, 1);
#pragma unroll 1
  for (int l = 0; l < 2; ++l) {
    attn_phase(c, l);
    if (l == 0) {
      wait_ready_b(c, nonce);
      gate<D_Q4>(c, OFF_X3 + (0 * 2 + c.b) * XV_STR + h * 1024);
      xp = gather33<true>(c, OFF_X4 + (0 * 2 + c.b) * XV_STR, nullptr);
      STAMP(c, 5);
      if (xpub && c.lane < 32) st32(c.rs, OFF_X3 + (1 * 2 + c.b) * XV_STR + 32 * 1024 + 128 * c.w + 4 * c.lane, canon(xp));
      q_phase(1);
      STAMP(c, 6);
    }
  }
  gate<D_Q4>(c, OFF_X3 + (1 * 2 + c.b) * XV_STR + h * 1024);
  if (c.w == 0) {
    const int qi = 2 * h + ch;
    const int off = OFF_X4 + (1 * 2 + c.b) * XV_STR + (c.lane < 33 ? c.lane : 32) * 1024 + qi * 16;
    v4u v;
    for (;;) {
      CBAR();
      v = ld128(c.rs, off);
      if (__all(max(umax3(v.x, v.y, v.z), v.w) != SENT) || !spin_ok(c)) break;
    }
    const unsigned wl = c.lane < 32 ? LO1 : 0u, wh = c.lane < 32 ? HI1 : 0u;
    v4u o;
#pragma unroll
    for (int q = 0; q < 4; ++q) {
      float lo = dot2(v[q], wl, 0.f), hi = dot2(v[q], wh, 0.f);
      lo = quad_sum(lo); hi = quad_sum(hi);
      lo += dppf<0x124>(lo); hi += dppf<0x124>(hi);
      lo += dppf<0x128>(lo); hi += dppf<0x128>(hi);
      lo += dppm<0x142, 0xa>(lo); hi += dppm<0x142, 0xa>(hi);
      const unsigned s2 = (unsigned)__builtin_amdgcn_readlane((int)pack2(lo, hi), 31);
      const unsigned r2 = (unsigned)__builtin_amdgcn_readlane((int)v[q], 32);
      o[q] = pack2(bf_lo(r2) + bf_lo(s2), bf_hi(r2) + bf_hi(s2));
    }
    if (c.lane == 0) *(GAS v4u*)((GAS unsigned char*)p.out + c.b * 1024 + qi * 16) = o;
    STAMP(c, 16);
  }
}

DI void role_o(Ctx& c, const Params& p, v4u nonce, int h) {
  const int n = c.lane & 15, g = c.lane >> 4;
  const v4u sv = {SENT, SENT, SENT, SENT};
  {
    v4u ct[16];
    ckv_issue(c, p, ct);
    ckv_stage(c, ct);
  }
  __builtin_amdgcn_sched_barrier(0);
  v4u rv[2][8]; v2u ro[4][8];
  {
    const GAS unsigned char* wv = (const GAS unsigned char*)p.wuv + ((size_t)(64 * c.w + 2 * g) * 4096 + h * 128 + 8 * n) * 2;
#pragma unroll
    for (int ks = 0; ks < 2; ++ks)
#pragma unroll
      for (int j = 0; j < 8; ++j) rv[ks][j] = *(const GAS v4u*)(wv + (size_t)(16 * (j & 3) + 8 * ks + (j >> 2)) * 8192);
    const GAS unsigned char* wo = (const GAS unsigned char*)p.wo + ((size_t)(h * 128 + 8 * g) * 512 + 64 * c.w + 4 * n) * 2;
#pragma unroll
    for (int ks = 0; ks < 4; ++ks)
#pragma unroll
      for (int j = 0; j < 8; ++j) ro[ks][j] = *(const GAS v2u*)(wo + (size_t)(32 * ks + j) * 1024);
  }
  __builtin_amdgcn_sched_barrier(0);
  fill_attn(c);
  if (c.w < 2) st128(c.rs, OFF_X3 + (c.w * 2 + c.b) * XV_STR + h * 1024 + c.lane * 16, sv);
  v4u Wv[16], Wo[16];
  tr8x8(rv[0], Wv); tr8x8(rv[1], Wv + 8);
#pragma unroll
  for (int ks = 0; ks < 4; ++ks) tr8x4(ro[ks], Wo + 4 * ks);
  publish_ready_b(c, nonce);
  STAMP(c, 20);
  wait_ready_a(c, nonce);
  STAMP(c, 1);
#pragma unroll 1
  for (int l = 0; l < 2; ++l) {
    attn_phase(c, l);
    if (l == 0) wait_ready_b(c, nonce);
    float inv;
    {
      const int bs = OFF_ST + ((l * 2 + c.b) * 32 + h) * 512 + c.lane * 8;
      v2u st;
      for (;;) {
        CBAR();
        st = ld64(c.rs, bs);
        if (__all(max(st.x, st.y) != SENT) || !spin_ok(c)) break;
      }
      const float kf = u2f(st.x);
      float K = kf;
      K = fmaxf(K, dppf<0x128>(K)); K = fmaxf(K, dppf<0x124>(K)); K = fmaxf(K, dppf<0x122>(K)); K = fmaxf(K, dppf<0x121>(K));
      K = xg_max(K);
      const int d = (int)(K - kf);
      const unsigned eb = d < 127 ? (unsigned)(127 - d) << 7 : 0u;
      float Ls = u2f(st.y) * u2f(eb << 16);
      Ls += dppf<0x128>(Ls); Ls += dppf<0x124>(Ls); Ls += dppf<0x122>(Ls); Ls += dppf<0x121>(Ls);
      Ls = xg_sum(Ls);
      inv = 1.0f / Ls;
      *(LAS unsigned*)(c.L + L_EW + c.w * 256 + c.lane * 4) = eb;
    }
    unsigned el[16];
#pragma unroll
    for (int i = 0; i < 4; ++i) {
      const v4u e4 = *(const LAS v4u*)(c.L + L_EW + c.w * 256 + g * 64 + i * 16);
      el[4 * i] = e4.x; el[4 * i + 1] = e4.y; el[4 * i + 2] = e4.z; el[4 * i + 3] = e4.w;
    }
    const int bx = OFF_X2 + ((l * 2 + c.b) * 32 + h) * 65536 + c.w * 8192 + g * 2048 + n * 8;
    v2u v[16];
    for (;;) {
      CBAR();
#pragma unroll
      for (int i = 0; i < 16; ++i) v[i] = ld64(c.rs, bx + i * 128);
      unsigned m = 0u;
#pragma unroll
      for (int i = 0; i < 16; ++i) m = umax3(m, v[i].x, v[i].y);
      if (__all(m != SENT) || !spin_ok(c)) break;
    }
    STAMP(c, 5 + 8 * l);
    float s0 = 0.f, s1 = 0.f, s2 = 0.f, s3 = 0.f;
#pragma unroll
    for (int i = 0; i < 16; ++i) {
      const unsigned eh = el[i] << 16;
      s0 = dot2(v[i].x, el[i], s0); s1 = dot2(v[i].x, eh, s1); s2 = dot2(v[i].y, el[i], s2); s3 = dot2(v[i].y, eh, s3);
    }
    s0 = xg_sum(s0) * inv; s1 = xg_sum(s1) * inv; s2 = xg_sum(s2) * inv; s3 = xg_sum(s3) * inv;
    const unsigned h01 = pack2(s0, s1), h23 = pack2(s2, s3);
    const unsigned l01 = pack2(dot2(h01, NLO1, s0), dot2(h01, NHI1, s1)), l23 = pack2(dot2(h23, NLO1, s2), dot2(h23, NHI1, s3));
    *(LAS v2u*)(c.L + L_FR + c.w * 256 + (g & 1) * 128 + n * 8) = (g & 1) ? v2u{l01, l23} : v2u{h01, h23};
    const v4u a0 = *(const LAS v4u*)(c.L + L_FR + c.w * 256 + (n & 1) * 128 + g * 16);
    const v4u a1 = *(const LAS v4u*)(c.L + L_FR + c.w * 256 + (n & 1) * 128 + 64 + g * 16);
    asm volatile("" :: "v"(a0), "v"(a1));
    STAMP(c, 7 + 8 * l);
    gemv_ksplit<true>(c, a0, a1, Wv);
    STAMP(c, 22 + 2 * l);
    reduce128(c);
    STAMP(c, 23 + 2 * l);
    f32x4 acc[4];
#pragma unroll
    for (int t = 0; t < 4; ++t) acc[t] = f32x4{0.f, 0.f, 0.f, 0.f};
#pragma unroll
    for (int k4 = 0; k4 < 4; ++k4) {
      const v4u aa = *(const LAS v4u*)(c.L + L_VEC + k4 * 64 + g * 16);
#pragma unroll
      for (int t = 0; t < 4; ++t) acc[t] = mfma(aa, Wo[4 * k4 + t], acc[t]);
    }
    if (c.lane < 16) {
      v2u o = {canon(pack2(acc[0].x, acc[1].x)), canon(pack2(acc[2].x, acc[3].x))};
      st64(c.rs, OFF_X3 + (l * 2 + c.b) * XV_STR + h * 1024 + (64 * c.w + 4 * n) * 2, o);
    }
    STAMP(c, 6 + 8 * l);
    if (l == 0) gate<D_A1>(c, OFF_X4 + (0 * 2 + c.b) * XV_STR + h * 1024);
  }
}

DI void role_f(Ctx& c, const Params& p, v4u nonce, int j) {
  const int n = c.lane & 15, g = c.lane >> 4;
  const v4u sv = {SENT, SENT, SENT, SENT};
  {
    v4u ct[16];
    ckv_issue(c, p, ct);
    ckv_stage(c, ct);
  }
  __builtin_amdgcn_sched_barrier(0);
  v4u r13[2][8]; v2u r2[2][8];
  {
    const int nn = n < 14 ? n : 13;
    const u16* wsel = nn < 7 ? p.w1 : p.w3;
    const int col = 56 * j + 8 * (nn < 7 ? nn : nn - 7);
    const GAS unsigned char* wa = (const GAS unsigned char*)wsel + ((size_t)(64 * c.w + 16 * g) * 1792 + col) * 2;
#pragma unroll
    for (int ks = 0; ks < 2; ++ks)
#pragma unroll
      for (int jj = 0; jj < 8; ++jj) r13[ks][jj] = *(const GAS v4u*)(wa + (size_t)(8 * ks + jj) * 3584);
#pragma unroll
    for (int ks = 0; ks < 2; ++ks)
#pragma unroll
      for (int jj = 0; jj < 8; ++jj) {
        int f = 32 * ks + 8 * g + jj; f = f < 56 ? f : 55;
        r2[ks][jj] = *(const GAS v2u*)((const GAS unsigned char*)p.w2 + ((size_t)(56 * j + f) * 512 + 64 * c.w + 4 * n) * 2);
      }
  }
  __builtin_amdgcn_sched_barrier(0);
  fill_attn(c);
  if (c.w < 2) st128(c.rs, OFF_X4 + (c.w * 2 + c.b) * XV_STR + j * 1024 + c.lane * 16, sv);
  if (j == 0 && (c.w == 2 || c.w == 3)) st128(c.rs, OFF_X4 + ((c.w - 2) * 2 + c.b) * XV_STR + 32 * 1024 + c.lane * 16, sv);
  v4u W13[16], W2[8];
  tr8x8(r13[0], W13); tr8x8(r13[1], W13 + 8);
  tr8x4(r2[0], W2); tr8x4(r2[1], W2 + 4);
  publish_ready_b(c, nonce);
  STAMP(c, 20);
  wait_ready_a(c, nonce);
  STAMP(c, 1);
#pragma unroll 1
  for (int l = 0; l < 2; ++l) {
    attn_phase(c, l);
    unsigned xp;
    if (l == 0) wait_ready_b(c, nonce);
    gate<D_F3>(c, -1);
    if (l == 0) { xp = gather33<false>(c, OFF_X3 + (0 * 2 + c.b) * XV_STR, p.x + c.b * 512); }
    else xp = gather33<true>(c, OFF_X3 + (1 * 2 + c.b) * XV_STR, nullptr);
    STAMP(c, 5 + 8 * l);
    if (j == 0 && c.lane < 32) st32(c.rs, OFF_X4 + (l * 2 + c.b) * XV_STR + 32 * 1024 + 128 * c.w + 4 * c.lane, canon(xp));
    v4u a0, a1;
    layernorm(c, xp, a0, a1);
    STAMP(c, 7 + 8 * l);
    gemv_ksplit<false>(c, a0, a1, W13);
    STAMP(c, 22 + 2 * l);
    LBAR();
    STAMP(c, 8 + 8 * l);
    if (c.w == 0) {
      const int li = c.lane < 56 ? c.lane : 55;
      const unsigned gu = pack2(red_sum8(c, li), red_sum8(c, 56 + li));
      const float gt = bf_lo(gu);
      const float sg = rbf(gt * __builtin_amdgcn_rcpf(1.0f + __expf(-gt)));
      const float hv = c.lane < 56 ? sg * bf_hi(gu) : 0.f;
      *(LAS __bf16*)(c.L + L_VEC + c.lane * 2) = (__bf16)hv;
    }
    LBAR();
    STAMP(c, 23 + 2 * l);
    f32x4 acc[4];
#pragma unroll
    for (int t = 0; t < 4; ++t) acc[t] = f32x4{0.f, 0.f, 0.f, 0.f};
#pragma unroll
    for (int k2 = 0; k2 < 2; ++k2) {
      const v4u a = *(const LAS v4u*)(c.L + L_VEC + k2 * 64 + g * 16);
#pragma unroll
      for (int t = 0; t < 4; ++t) acc[t] = mfma(a, W2[4 * k2 + t], acc[t]);
    }
    if (c.lane < 16) {
      v2u o = {canon(pack2(acc[0].x, acc[1].x)), canon(pack2(acc[2].x, acc[3].x))};
      st64(c.rs, OFF_X4 + (l * 2 + c.b) * XV_STR + j * 1024 + (64 * c.w + 4 * n) * 2, o);
    }
    STAMP(c, 6 + 8 * l);
    if (l == 0) gate<D_FA1>(c, -1);
  }
}

__global__ void __launch_bounds__(NT, 2) mega(Params p) {
  extern __shared__ __attribute__((aligned(16))) unsigned char smem[];
  Ctx c;
  c.rs = __builtin_amdgcn_make_buffer_rsrc((void*)p.ws, 0, WS_BYTES, 0x00020000);
  c.L = (LAS unsigned char*)smem;
  c.lane = threadIdx.x & 63;
  c.w = __builtin_amdgcn_readfirstlane(threadIdx.x >> 6);
  c.blk = blockIdx.x;
  const int x = c.blk & 7, slot = c.blk >> 3;
  c.b = (slot >> 3) & 1; c.G = slot >> 4; c.s = 8 * x + (slot & 7);
  const int r = 8 * c.G + (slot & 7);
  c.spins = 0; c.wsp = p.ws;
  c.kLO1 = sconst(0x00003F80u); c.kHI1 = sconst(0x3F800000u); c.kONES = sconst(0x3F803F80u); c.kNLO1 = sconst(0x0000BF80u); c.kNHI1 = sconst(0xBF800000u);
  STAMP(c, 0);
  const unsigned long long id = llvm_dispatch_id();
  const unsigned long long dp = (unsigned long long)__builtin_amdgcn_dispatch_ptr();
  const v4u nonce = {(unsigned)id ^ 0x5bd1e995u, (unsigned)(id >> 32) ^ 0x9e3779b9u, (unsigned)dp ^ 0x85ebca6bu, (unsigned)(dp >> 32) ^ 0xc2b2ae35u};
  if (r < 8) role_q(c, p, nonce, 4 * x + (r >> 1), r & 1);
  else if (r < 12) role_o(c, p, nonce, 4 * x + (r - 8));
  else role_f(c, p, nonce, 4 * x + (r - 12));
  if (c.w == 0 && c.lane < 2) st128(c.rs, OFF_RDY + c.blk * 64 + c.lane * 16, v4u{~nonce.x, ~nonce.y, ~nonce.z, ~nonce.w});
}
}

extern "C" void kernel_launch(void* const* d_in, const int* in_sizes, int n_in,
                              void* d_out, int out_size, void* d_ws, size_t ws_size,
                              hipStream_t stream) {
  mk::Params p;
  p.x = (const u16*)d_in[0]; p.ckv = (const u16*)d_in[1]; p.wq = (const u16*)d_in[2]; p.wuk = (const u16*)d_in[3];
  p.wuv = (const u16*)d_in[4]; p.wo = (const u16*)d_in[5]; p.w1 = (const u16*)d_in[7]; p.w3 = (const u16*)d_in[8];
  p.w2 = (const u16*)d_in[9]; p.out = (u16*)d_out; p.ws = (unsigned char*)d_ws;
  (void)hipFuncSetAttribute((const void*)mk::mega, hipFuncAttributeMaxDynamicSharedMemorySize, mk::L_BYTES + 2048);
  hipLaunchKernelGGL(mk::mega, dim3(256), dim3(mk::NT), mk::L_BYTES + 2048, stream, p);
}
```

```cpp
#include <hip/hip_runtime.h>
#include <hip/hip_bf16.h>
#include <stdint.h>

#define LAS __attribute__((address_space(3)))
#define GAS __attribute__((address_space(1)))
typedef unsigned short u16;
typedef unsigned v4u __attribute__((ext_vector_type(4)));
typedef unsigned v2u __attribute__((ext_vector_type(2)));
typedef float f32x4 __attribute__((ext_vector_type(4)));
typedef float f32x2 __attribute__((ext_vector_type(2)));
typedef short bf16x8 __attribute__((ext_vector_type(8)));
typedef short s16x4 __attribute__((ext_vector_type(4)));
typedef __bf16 bf16x2_t __attribute__((ext_vector_type(2)));
typedef unsigned short u16x2 __attribute__((ext_vector_type(2)));
#define DI __device__ __forceinline__

extern "C" __device__ unsigned long long llvm_dispatch_id() __asm("llvm.amdgcn.dispatch.id");

#define CODE_PF 1
namespace mk {
constexpr int NT = 512;
constexpr unsigned SENT = 0xFFFFFFFFu;
constexpr unsigned SPIN_CAP = 1u << 16;
constexpr int AUX_SC1 = 16;
#define LO1 (c.kLO1)
#define HI1 (c.kHI1)
#define ONES (c.kONES)
#define NLO1 (c.kNLO1)
#define NHI1 (c.kNHI1)

constexpr int OFF_RDY = 0;
constexpr int OFF_X1 = 16384;
constexpr int OFF_ST = OFF_X1 + 2 * 2 * 32 * 1024;
constexpr int OFF_X3 = OFF_ST + 2 * 2 * 32 * 512;
constexpr int XV_STR = 34 * 1024;
constexpr int OFF_X4 = OFF_X3 + 4 * XV_STR;
constexpr int OFF_X2 = OFF_X4 + 4 * XV_STR;
constexpr int OFF_XQ = OFF_X2 + 2 * 2 * 32 * 65536;
constexpr int WS_BYTES = OFF_XQ + 32 * 8192;

constexpr int L_IMG = 0;
constexpr int L_QA = 131072;
constexpr int L_PB = L_QA + 16384;
constexpr int L_SB = L_PB + 4096;
constexpr int L_LN = L_SB + 1024;
constexpr int L_VEC = L_LN + 128;
constexpr int L_EW = L_VEC + 512;
constexpr int L_FR = L_EW + 2048;
constexpr int L_CNT = L_FR + 2048;
constexpr int L_BYTES = L_CNT + 16;
constexpr int L_RED = L_QA;

struct Params {
  const u16* x; const u16* ckv; const u16* wq; const u16* wuk; const u16* wuv; const u16* wo;
  const u16* w1; const u16* w3; const u16* w2; u16* out; unsigned char* ws;
};

DI float u2f(unsigned u) { return __builtin_bit_cast(float, u); }
DI unsigned f2u(float f) { return __builtin_bit_cast(unsigned, f); }
DI float bf_lo(unsigned u) { return u2f(u << 16); }
DI float bf_hi(unsigned u) { return u2f(u & 0xFFFF0000u); }
DI float rbf(float f) { return (float)(__bf16)f; }
DI unsigned pack2(float lo, float hi) { bf16x2_t v = {(__bf16)lo, (__bf16)hi}; return __builtin_bit_cast(unsigned, v); }
DI unsigned canon(unsigned u) { return u < 0xFFFFFFFEu ? u : 0xFFFFFFFEu; }
DI unsigned par(unsigned u, int l) { return u & ~1u; }
DI float dot2(unsigned a, unsigned b, float c) { return __builtin_amdgcn_fdot2_f32_bf16(__builtin_bit_cast(bf16x2_t, a), __builtin_bit_cast(bf16x2_t, b), c, false); }
DI unsigned subsat(unsigned a, unsigned b) { return __builtin_bit_cast(unsigned, __builtin_elementwise_sub_sat(__builtin_bit_cast(u16x2, a), __builtin_bit_cast(u16x2, b))); }
DI unsigned umax3(unsigned a, unsigned b, unsigned c) { return max(max(a, b), c); }
DI f32x4 mfma(v4u a, v4u b, f32x4 c) { return __builtin_amdgcn_mfma_f32_16x16x32_bf16(__builtin_bit_cast(bf16x8, a), __builtin_bit_cast(bf16x8, b), c, 0, 0, 0); }
template <int CTRL> DI float dppf(float v) { return u2f((unsigned)__builtin_amdgcn_update_dpp(0, (int)f2u(v), CTRL, 0xf, 0xf, true)); }
template <int CTRL> DI unsigned dppu(unsigned v) { return (unsigned)__builtin_amdgcn_update_dpp(0, (int)v, CTRL, 0xf, 0xf, true); }
template <int CTRL, int RM> DI float dppm(float v) { return u2f((unsigned)__builtin_amdgcn_update_dpp(0, (int)f2u(v), CTRL, RM, 0xf, false)); }
DI float quad_sum(float v) { v += dppf<0xB1>(v); v += dppf<0x4E>(v); return v; }
DI float xg_sum(float v) {
  const auto a = __builtin_amdgcn_permlane16_swap(f2u(v), f2u(v), false, false);
  const unsigned a0 = a[0], a1 = a[1];
  v = u2f(a0) + u2f(a1);
  const auto b = __builtin_amdgcn_permlane32_swap(f2u(v), f2u(v), false, false);
  const unsigned b0 = b[0], b1 = b[1];
  return u2f(b0) + u2f(b1);
}
DI float x32_sum(float v) {
  const auto b = __builtin_amdgcn_permlane32_swap(f2u(v), f2u(v), false, false);
  const unsigned b0 = b[0], b1 = b[1];
  return u2f(b0) + u2f(b1);
}
DI float xg_max(float v) {
  const auto a = __builtin_amdgcn_permlane16_swap(f2u(v), f2u(v), false, false);
  const unsigned a0 = a[0], a1 = a[1];
  v = fmaxf(u2f(a0), u2f(a1));
  const auto b = __builtin_amdgcn_permlane32_swap(f2u(v), f2u(v), false, false);
  const unsigned b0 = b[0], b1 = b[1];
  return fmaxf(u2f(b0), u2f(b1));
}
DI unsigned swz(unsigned r) { return (((r ^ ((r >> 1) & 4u)) & 7u) << 1) | ((r >> 3) & 1u); }

typedef __amdgpu_buffer_rsrc_t rsrc_t;
DI v4u ld128(rsrc_t rs, int off) { return __builtin_amdgcn_raw_buffer_load_b128(rs, off, 0, AUX_SC1); }
DI v2u ld64(rsrc_t rs, int off) { return __builtin_amdgcn_raw_buffer_load_b64(rs, off, 0, AUX_SC1); }
DI unsigned ld32(rsrc_t rs, int off) { return __builtin_amdgcn_raw_buffer_load_b32(rs, off, 0, AUX_SC1); }
DI void st128(rsrc_t rs, int off, v4u v) { __builtin_amdgcn_raw_buffer_store_b128(v, rs, off, 0, AUX_SC1); }
DI void st64(rsrc_t rs, int off, v2u v) { __builtin_amdgcn_raw_buffer_store_b64(v, rs, off, 0, AUX_SC1); }
DI void st32(rsrc_t rs, int off, unsigned v) { __builtin_amdgcn_raw_buffer_store_b32(v, rs, off, 0, AUX_SC1); }
#define CBAR() asm volatile("" ::: "memory")
#define DRAIN() asm volatile("s_waitcnt vmcnt(0)" ::: "memory")
#define LBAR() do { asm volatile("s_waitcnt lgkmcnt(0)" ::: "memory"); __builtin_amdgcn_s_barrier(); asm volatile("" ::: "memory"); } while (0)

DI void tr8x8(const v4u (&r)[8], v4u* f) {
#pragma unroll
  for (int q = 0; q < 4; ++q)
#pragma unroll
    for (int d = 0; d < 4; ++d) {
      const unsigned a = r[2 * d][q], b = r[2 * d + 1][q];
      f[2 * q][d] = __builtin_amdgcn_perm(b, a, 0x05040100u);
      f[2 * q + 1][d] = __builtin_amdgcn_perm(b, a, 0x07060302u);
    }
}
DI void tr8x4(const v2u (&r)[8], v4u* f) {
#pragma unroll
  for (int q = 0; q < 2; ++q)
#pragma unroll
    for (int d = 0; d < 4; ++d) {
      const unsigned a = r[2 * d][q], b = r[2 * d + 1][q];
      f[2 * q][d] = __builtin_amdgcn_perm(b, a, 0x05040100u);
      f[2 * q + 1][d] = __builtin_amdgcn_perm(b, a, 0x07060302u);
    }
}

#define STAMP(c, i) do {} while (0)
struct Ctx {
  unsigned char* wsp;
  rsrc_t rs; LAS unsigned char* L;
  int lane, w, b, G, s, blk;
  unsigned spins;
  unsigned kLO1, kHI1, kONES, kNLO1, kNHI1;
  v4u nonce;
};
DI unsigned sconst(unsigned v) { unsigned r; asm volatile("s_mov_b32 %0, %1" : "=s"(r) : "i"(v)); return r; }
#define RELANE(c) do { int l_ = (c).lane; asm volatile("" : "+v"(l_)); (c).lane = l_; } while (0)
DI bool spin_ok(Ctx& c) { return ++c.spins < SPIN_CAP; }

DI int p_blk(int h, int e) { return (h >> 2) + 8 * (4 * (h & 3) + e); }

DI bool nonce_eq(v4u a, v4u n) { return ((a.x ^ n.x) | (a.y ^ n.y) | (a.z ^ n.z) | (a.w ^ n.w)) == 0u; }
DI void wait_ready_a(Ctx& c, v4u nonce, int h0, int hstep) {
  const int q = c.lane & 7;
  const int off = OFF_RDY + p_blk(h0 + hstep * (q >> 2), q & 3) * 64;
  for (;;) {
    CBAR();
    const v4u a = ld128(c.rs, off);
    if (__all(nonce_eq(a, nonce)) || !spin_ok(c)) break;
  }
  CBAR();
}
DI void wait_ready_b(Ctx& c, v4u nonce, int which = 1) {
  const int o0 = OFF_RDY + c.lane * 64 + 16 * which;
  for (;;) {
    CBAR();
    const v4u a0 = ld128(c.rs, o0), a1 = ld128(c.rs, o0 + 4096), a2 = ld128(c.rs, o0 + 8192), a3 = ld128(c.rs, o0 + 12288);
    if (__all(nonce_eq(a0, nonce) & nonce_eq(a1, nonce) & nonce_eq(a2, nonce) & nonce_eq(a3, nonce)) || !spin_ok(c)) break;
  }
  CBAR();
}

template <int D>
DI void gate(Ctx& c, int off) {
  if (c.w == 0) {
    if (off >= 0) {
      for (;;) {
        CBAR();
        const unsigned v = __builtin_amdgcn_raw_buffer_load_b32(c.rs, off, 0, AUX_SC1);
        if (__builtin_amdgcn_readfirstlane(v) != SENT || !spin_ok(c)) break;
        __builtin_amdgcn_s_sleep(4);
      }
    }
    if (D > 0) __builtin_amdgcn_s_sleep(D);
  }
  __builtin_amdgcn_s_barrier();
}
#define PC_WAIT 4u
#define D_PC 16
#define D_O2 0
#define D_START 16
#define D_F3 40
#define D_Q4 30
#define D_A1 30
#define D_FA1 45

template <bool RES_WS>
DI unsigned gather33(Ctx& c, int base, const u16* xin) {
  const int co = 128 * c.w + 4 * (c.lane & 31);
  unsigned v[16], res;
  if (!RES_WS) res = *(const GAS unsigned*)((const GAS unsigned char*)xin + co);
  const int o = base + (c.lane >> 5) * 1024 + co;
  for (;;) {
    CBAR();
#pragma unroll
    for (int i = 0; i < 16; ++i) v[i] = ld32(c.rs, o + i * 2048);
    if (RES_WS) res = ld32(c.rs, base + 32 * 1024 + co);
    unsigned m = RES_WS ? res : 0u;
#pragma unroll
    for (int i = 0; i < 16; i += 2) m = umax3(m, v[i], v[i + 1]);
    if (__all(m != SENT) || !spin_ok(c)) break;
  }
  float s0 = 0.f, s1 = 0.f, s2 = 0.f, s3 = 0.f;
#pragma unroll
  for (int i = 0; i < 16; i += 2) { s0 = dot2(v[i], LO1, s0); s1 = dot2(v[i], HI1, s1); s2 = dot2(v[i + 1], LO1, s2); s3 = dot2(v[i + 1], HI1, s3); }
  const unsigned o01 = pack2(x32_sum(s0 + s2), x32_sum(s1 + s3));
  return pack2(dot2(o01, LO1, bf_lo(res)), dot2(o01, HI1, bf_hi(res)));
}

DI void layernorm(Ctx& c, unsigned xp, v4u& a0, v4u& a1) {
  float t1 = dot2(xp, ONES, 0.f), t2 = dot2(xp, xp, 0.f);
  t1 += dppf<0x121>(t1); t2 += dppf<0x121>(t2);
  t1 += dppf<0x122>(t1); t2 += dppf<0x122>(t2);
  t1 += dppf<0x124>(t1); t2 += dppf<0x124>(t2);
  t1 += dppf<0x128>(t1); t2 += dppf<0x128>(t2);
  t1 += dppm<0x142, 0xa>(t1); t2 += dppm<0x142, 0xa>(t2);
  if (c.lane == 31) *(LAS f32x2*)(c.L + L_LN + c.w * 8) = f32x2{t1, t2};
  LBAR();
  f32x4 t[4];
#pragma unroll
  for (int i = 0; i < 4; ++i) t[i] = *(const LAS f32x4*)(c.L + L_LN + i * 16);
  const f32x4 tt = (t[0] + t[1]) + (t[2] + t[3]);
  const float mean = (tt.x + tt.z) * (1.0f / 512.0f);
  const float var = fmaxf((tt.y + tt.w) * (1.0f / 512.0f) - mean * mean, 0.f);
  const unsigned mv = pack2(mean, var);
  const float nmb = -bf_lo(mv);
  const float sd = rbf(__builtin_amdgcn_sqrtf(rbf(bf_hi(mv) + 1e-5f)));
  const float ri = __builtin_amdgcn_rcpf(sd);
  const unsigned d01 = pack2(dot2(xp, LO1, nmb), dot2(xp, HI1, nmb));
  const unsigned n01 = pack2(bf_lo(d01) * ri, bf_hi(d01) * ri);
  *(LAS unsigned*)(c.L + L_FR + c.w * 256 + c.lane * 4) = n01;
  a0 = *(const LAS v4u*)(c.L + L_FR + c.w * 256 + (c.lane >> 4) * 32);
  a1 = *(const LAS v4u*)(c.L + L_FR + c.w * 256 + (c.lane >> 4) * 32 + 16);
}

template <bool HILO>
DI void gemv_ksplit(Ctx& c, v4u a0, v4u a1, const v4u* W) {
  f32x4 acc[8];
#pragma unroll
  for (int t = 0; t < 8; ++t) acc[t] = mfma(a0, W[t], f32x4{0.f, 0.f, 0.f, 0.f});
#pragma unroll
  for (int t = 0; t < 8; ++t) acc[t] = mfma(a1, W[8 + t], acc[t]);
  if (c.lane < 16) {
    f32x4 o0, o1;
#pragma unroll
    for (int t = 0; t < 4; ++t) { o0[t] = HILO ? acc[t].x + acc[t].y : acc[t].x; o1[t] = HILO ? acc[4 + t].x + acc[4 + t].y : acc[4 + t].x; }
    *(LAS f32x4*)(c.L + L_RED + c.w * 512 + c.lane * 32) = o0;
    *(LAS f32x4*)(c.L + L_RED + c.w * 512 + c.lane * 32 + 16) = o1;
  }
}
DI float red_sum8(Ctx& c, int idx) {
  const LAS float* R = (const LAS float*)(c.L + L_RED);
  return ((R[idx] + R[128 + idx]) + (R[256 + idx] + R[384 + idx])) + ((R[512 + idx] + R[640 + idx]) + (R[768 + idx] + R[896 + idx]));
}
DI void reduce128(Ctx& c) {
  LBAR();
  if (c.w < 2) { const int d = c.w * 64 + c.lane; *(LAS __bf16*)(c.L + L_VEC + d * 2) = (__bf16)red_sum8(c, d); }
  LBAR();
}

template <int KB>
DI void gemv_nsplit(Ctx& c, const v4u* W, int off) {
  const int n = c.lane & 15, g = c.lane >> 4;
  f32x4 acc[8];
#pragma unroll
  for (int kb = 0; kb < KB; ++kb) {
    const v4u a = *(const LAS v4u*)(c.L + L_VEC + kb * 128 + (n & 1) * 64 + g * 16);
#pragma unroll
    for (int t = 0; t < 8; ++t) acc[t] = mfma(a, W[8 * kb + t], kb == 0 ? f32x4{0.f, 0.f, 0.f, 0.f} : acc[t]);
  }
  float r[8];
#pragma unroll
  for (int t = 0; t < 8; ++t) r[t] = acc[t].x + dppf<0x108>(acc[t].y);
  if (c.lane < 8) st128(c.rs, off + 128 * c.w + 16 * c.lane, v4u{canon(pack2(r[0], r[1])), canon(pack2(r[2], r[3])), canon(pack2(r[4], r[5])), canon(pack2(r[6], r[7]))});
}

DI float score_round(float s) {
  const float SC = 0.08838834764831845f;
  const unsigned bits = f2u(s);
  const unsigned tr = bits & 0xFFFF0000u;
  const unsigned g2 = pack2(u2f(tr) * SC, u2f(tr + 0x10000u) * SC);
  const float ga = bf_lo(g2);
  const float wgt = fminf(fmaxf((float)(bits & 0xFFFFu) * (1.0f / (65536.0f * 0.6f)) + (0.5f - 0.5f / 0.6f), 0.f), 1.f);
  return ga + wgt * (bf_hi(g2) - ga);
}

struct NoHook { DI void operator()() const {} };
template <class Hook>
DI void attn_phase(Ctx& c, int l, Hook hook) {
  RELANE(c);
  const int tid = c.w * 64 + c.lane, n = c.lane & 15, g = c.lane >> 4;
  v4u kf[16];
  {
    const unsigned kb = L_IMG + (c.w * 16 + n) * 1024 + (((unsigned)g ^ swz(n)) << 4);
#pragma unroll
    for (int ks = 0; ks < 16; ++ks) kf[ks] = *(const LAS v4u*)(c.L + ((kb ^ (64u * (ks & 3))) + 256u * (ks >> 2)));
  }
  unsigned ab;
  {
    const unsigned q4 = (c.lane & 15) >> 2, p4 = c.lane & 3;
    const unsigned key = 4u * g + q4;
    const unsigned chunk = 8u * c.w + (p4 >> 1);
    ab = L_IMG + key * 1024 + ((chunk ^ swz(key)) << 4) + 8u * (p4 & 1);
    asm volatile("" : "+v"(ab));
  }
  {
    const int base = OFF_X1 + ((l * 2 + c.b) * 32 + 16 * c.G) * 1024 + tid * 16;
    v4u v0, v1;
    for (;;) {
      CBAR();
      v0 = ld128(c.rs, base);
      v1 = ld128(c.rs, base + 8192);
      const unsigned m = max(umax3(umax3(v0.x, v0.y, v0.z), v0.w, v1.x), umax3(v1.y, v1.z, v1.w));
      if (__all(m != SENT) || !spin_ok(c)) break;
    }
    STAMP(c, 2 + 8 * l);
    hook();
    *(LAS v4u*)(c.L + L_QA + c.w * 1024 + (((unsigned)c.lane ^ swz(c.w)) << 4)) = v0;
    *(LAS v4u*)(c.L + L_QA + (c.w + 8) * 1024 + (((unsigned)c.lane ^ swz(c.w + 8)) << 4)) = v1;
  }
  LBAR();
  if (l == 0 && c.w == 0 && c.lane == 0) st128(c.rs, OFF_RDY + c.blk * 64 + 16, c.nonce);
  STAMP(c, 26 + l);
  f32x4 sacc = {0.f, 0.f, 0.f, 0.f}, sacc2 = {0.f, 0.f, 0.f, 0.f};
  {
    const unsigned qb = L_QA + n * 1024 + (((unsigned)g ^ swz(n)) << 4);
    constexpr int QD = 4;
    v4u qf[QD];
#pragma unroll
    for (int ks = 0; ks < QD; ++ks) qf[ks] = *(const LAS v4u*)(c.L + ((qb ^ (64u * (ks & 3))) + 256u * (ks >> 2)));
    __builtin_amdgcn_sched_barrier(0);
#pragma unroll
    for (int ks = 0; ks < 16; ++ks) {
      if (ks & 1) sacc2 = mfma(kf[ks], qf[ks % QD], sacc2); else sacc = mfma(kf[ks], qf[ks % QD], sacc);
      if (ks + QD < 16) qf[ks % QD] = *(const LAS v4u*)(c.L + ((qb ^ (64u * ((ks + QD) & 3))) + 256u * ((ks + QD) >> 2)));
      __builtin_amdgcn_sched_barrier(0);
    }
  }
  if (l == 1) { asm volatile("" :: "v"(sacc), "v"(sacc2)); STAMP(c, 17); }
  v2u vf[4][4][2];
  auto vf_issue = [&](int u) __attribute__((always_inline)) {
#pragma unroll
    for (int t = 0; t < 4; ++t) {
      const unsigned ad = (ab ^ (32u * t)) + 32768u * u;
      const s16x4 lo = __builtin_amdgcn_ds_read_tr16_b64_v4i16((LAS s16x4*)(c.L + ad));
      const s16x4 hi = __builtin_amdgcn_ds_read_tr16_b64_v4i16((LAS s16x4*)(c.L + ad + 16384u));
      vf[u][t][0] = __builtin_bit_cast(v2u, lo); vf[u][t][1] = __builtin_bit_cast(v2u, hi);
    }
  };
  vf_issue(0); vf_issue(1);
  __builtin_amdgcn_sched_barrier(0);
  {
    const float LOG2E = 1.4426950408889634f;
    float sv[4];
#pragma unroll
    for (int q = 0; q < 4; ++q) sv[q] = score_round(sacc[q] + sacc2[q]) * LOG2E;
    const float kw = ceilf(xg_max(fmaxf(fmaxf(sv[0], sv[1]), fmaxf(sv[2], sv[3]))));
    float p[4];
#pragma unroll
    for (int q = 0; q < 4; ++q) p[q] = __builtin_amdgcn_exp2f(sv[q] - kw);
    const float lw = xg_sum((p[0] + p[1]) + (p[2] + p[3]));
    *(LAS v2u*)(c.L + L_PB + (c.w >> 1) * 1024 + c.lane * 16 + (c.w & 1) * 8) = v2u{pack2(p[0], p[1]), pack2(p[2], p[3])};
    if (g == 0) *(LAS v2u*)(c.L + L_SB + n * 64 + c.w * 8) = v2u{(unsigned)(int)kw, f2u(lw)};
  }
  if (l == 1) STAMP(c, 18);
  LBAR();
  STAMP(c, 3 + 8 * l);
  f32x4 acc[4];
  {
    v4u sb[4], pr[4];
#pragma unroll
    for (int i = 0; i < 4; ++i) sb[i] = *(const LAS v4u*)(c.L + L_SB + n * 64 + i * 16);
#pragma unroll
    for (int u = 0; u < 4; ++u) pr[u] = *(const LAS v4u*)(c.L + L_PB + u * 1024 + c.lane * 16);
    vf_issue(2); vf_issue(3);
    __builtin_amdgcn_sched_barrier(0);
    int K = max(max(max((int)sb[0].x, (int)sb[0].z), max((int)sb[1].x, (int)sb[1].z)), max(max((int)sb[2].x, (int)sb[2].z), max((int)sb[3].x, (int)sb[3].z)));
    if (c.w == 0) {
      float Ls = 0.f;
#pragma unroll
      for (int i = 0; i < 4; ++i) Ls += ldexpf(u2f(sb[i].y), (int)sb[i].x - K) + ldexpf(u2f(sb[i].w), (int)sb[i].z - K);
      if (g == 0) st64(c.rs, OFF_ST + (((l * 2 + c.b) * 32 + 16 * c.G + n) * 64 + c.s) * 8, v2u{par(f2u((float)K), l), par(f2u(Ls), l)});
    }
#pragma unroll
    for (int t = 0; t < 4; ++t) acc[t] = f32x4{0.f, 0.f, 0.f, 0.f};
#pragma unroll
    for (int u = 0; u < 4; ++u) {
      const unsigned d0 = (unsigned)min(K - (int)sb[u].x, 255) * 0x00800080u, d1 = (unsigned)min(K - (int)sb[u].z, 255) * 0x00800080u;
      const v4u pb = {subsat(pr[u].x, d0), subsat(pr[u].y, d0), subsat(pr[u].z, d1), subsat(pr[u].w, d1)};
#pragma unroll
      for (int t = 0; t < 4; ++t) {
        const v4u af = {vf[u][t][0].x, vf[u][t][0].y, vf[u][t][1].x, vf[u][t][1].y};
        acc[t] = mfma(pb, af, acc[t]);
      }
    }
  }
  if (l == 1) { asm volatile("" :: "v"(acc[0]), "v"(acc[1]), "v"(acc[2]), "v"(acc[3])); STAMP(c, 19); }
  {
    const int base = OFF_X2 + ((l * 2 + c.b) * 32 + 16 * c.G + 4 * g) * 65536 + c.w * 8192 + c.s * 128 + n * 8;
#pragma unroll
    for (int q = 0; q < 4; ++q)
      st64(c.rs, base + q * 65536, v2u{par(pack2(acc[0][q], acc[1][q]), l), par(pack2(acc[2][q], acc[3][q]), l)});
  }
  STAMP(c, 4 + 8 * l);
}

DI void fill_attn(Ctx& c, int wv, int l = 0) {
  const v4u sv = {SENT, SENT, SENT, SENT};
  const int base = OFF_X2 + ((l * 2 + c.b) * 32 + 16 * c.G + (c.lane >> 3)) * 65536 + wv * 8192 + c.s * 128 + (c.lane & 7) * 16;
  st128(c.rs, base, sv);
  st128(c.rs, base + 8 * 65536, sv);
  if (wv == 7 && c.lane < 16) st64(c.rs, OFF_ST + (((l * 2 + c.b) * 32 + 16 * c.G + c.lane) * 64 + c.s) * 8, v2u{SENT, SENT});
}

DI void ckv_issue_h(Ctx& c, const Params& p, v4u (&t)[8], int hf) {
  const GAS unsigned char* src = (const GAS unsigned char*)p.ckv + ((size_t)c.b * 8192 + c.s * 128 + 64 * hf + c.w * 8) * 1024 + c.lane * 16;
#pragma unroll
  for (int i = 0; i < 8; ++i) t[i] = *(const GAS v4u*)(src + i * 1024);
}
DI void ckv_stage_h(Ctx& c, const v4u (&t)[8], int hf) {
#pragma unroll
  for (int i = 0; i < 8; ++i)
    *(LAS v4u*)(c.L + L_IMG + (64 * hf + c.w * 8 + i) * 1024 + (((unsigned)c.lane ^ swz((c.w * 8 + i) & 15)) << 4)) = t[i];
}
template <int NR>
DI void ckv_dma(Ctx& c, const Params& p, int row0) {
  const GAS unsigned char* src = (const GAS unsigned char*)p.ckv + ((size_t)c.b * 8192 + c.s * 128 + row0) * 1024;
#pragma unroll
  for (int i = 0; i < NR; ++i)
    __builtin_amdgcn_global_load_lds((const GAS unsigned*)(src + i * 1024 + (((unsigned)c.lane ^ swz(i & 15)) << 4)),
                                     (LAS unsigned*)(c.L + L_IMG + (row0 + i) * 1024), 16, 0, 0);
}
DI void ckv_issue(Ctx& c, const Params& p, v4u (&t)[16]) {
  const GAS unsigned char* src = (const GAS unsigned char*)p.ckv + ((size_t)c.b * 8192 + c.s * 128 + c.w * 16) * 1024 + c.lane * 16;
#pragma unroll
  for (int i = 0; i < 16; ++i) t[i] = *(const GAS v4u*)(src + i * 1024);
}
DI void ckv_stage(Ctx& c, const v4u (&t)[16]) {
#pragma unroll
  for (int i = 0; i < 16; ++i)
    *(LAS v4u*)(c.L + L_IMG + (c.w * 16 + i) * 1024 + (((unsigned)c.lane ^ swz(i)) << 4)) = t[i];
}
DI void publish_ready_b(Ctx& c, v4u nonce, int which = 1) {
  DRAIN();
  LBAR();
  if (c.w == 0 && c.lane == 0) st128(c.rs, OFF_RDY + c.blk * 64 + 16 * which, nonce);
}

DI unsigned ln_apply(Ctx& c, unsigned xp, f32x4 tt) {
  const float mean = (tt.x + tt.z) * (1.0f / 512.0f);
  const float var = fmaxf((tt.y + tt.w) * (1.0f / 512.0f) - mean * mean, 0.f);
  const unsigned mv = pack2(mean, var);
  const float nmb = -bf_lo(mv);
  const float sd = rbf(__builtin_amdgcn_sqrtf(rbf(bf_hi(mv) + 1e-5f)));
  const float ri = __builtin_amdgcn_rcpf(sd);
  const unsigned d01 = pack2(dot2(xp, LO1, nmb), dot2(xp, HI1, nmb));
  return pack2(bf_lo(d01) * ri, bf_hi(d01) * ri);
}
DI void prologue(Ctx& c, const Params& p, v4u nonce, int h, int e) {
  asm volatile("; prologue" ::: "memory");
  const int n = c.lane & 15, g = c.lane >> 4;
  const v4u sv = {SENT, SENT, SENT, SENT};
  volatile LAS unsigned* cnt = (volatile LAS unsigned*)(c.L + L_CNT);
  if (c.w == 0 && c.lane < 2) cnt[c.lane] = 0u;
  LBAR();
  if (c.w < 4) {
    v4u ru[8];
    f32x4 acc[8];
    const GAS unsigned char* wu = (const GAS unsigned char*)p.wuk + ((size_t)(128 * e + 32 * c.w + g) * 4096 + h * 128 + 8 * n) * 2;
    if (c.w < 2) {
      if (c.w == 0) {
        st128(c.rs, OFF_XQ + h * 8192 + e * 2048 + c.lane * 16, sv);
        st128(c.rs, OFF_XQ + h * 8192 + e * 2048 + 1024 + c.lane * 16, sv);
        if (c.lane < 32) st128(c.rs, OFF_X1 + ((c.lane >> 4) * 32 + h) * 1024 + e * 256 + (c.lane & 15) * 16, sv);
      }
      const v4u xa = *(const GAS v4u*)((const GAS unsigned char*)p.x + c.lane * 16);
      const v4u xb = *(const GAS v4u*)((const GAS unsigned char*)p.x + 1024 + c.lane * 16);
      const int k0 = 128 * e + 64 * c.w + 16 * g;
      const v4u xk0 = *(const GAS v4u*)((const GAS unsigned char*)p.x + (n & 1) * 1024 + k0 * 2);
      const v4u xk1 = *(const GAS v4u*)((const GAS unsigned char*)p.x + (n & 1) * 1024 + k0 * 2 + 16);
      v4u rq[2][8];
      const GAS unsigned char* wq = (const GAS unsigned char*)p.wq + ((size_t)k0 * 4096 + h * 128 + 8 * n) * 2;
#pragma unroll
      for (int ks = 0; ks < 2; ++ks)
#pragma unroll
        for (int j = 0; j < 8; ++j) rq[ks][j] = *(const GAS v4u*)(wq + (size_t)(8 * ks + j) * 8192);
#pragma unroll
      for (int i = 0; i < 8; ++i) ru[i] = *(const GAS v4u*)(wu + (size_t)(4 * i) * 8192);
      __builtin_amdgcn_sched_barrier(0);
      float s1a = 0.f, s2a = 0.f, s1b = 0.f, s2b = 0.f;
#pragma unroll
      for (int q = 0; q < 4; ++q) { s1a = dot2(xa[q], ONES, s1a); s2a = dot2(xa[q], xa[q], s2a); s1b = dot2(xb[q], ONES, s1b); s2b = dot2(xb[q], xb[q], s2b); }
      if (c.w == 0 && c.lane == 0) { st128(c.rs, OFF_RDY + c.blk * 64, nonce); cnt[0] = 1u; }
      STAMP(c, 9);
      s1a += dppf<0x121>(s1a); s2a += dppf<0x121>(s2a); s1b += dppf<0x121>(s1b); s2b += dppf<0x121>(s2b);
      s1a += dppf<0x122>(s1a); s2a += dppf<0x122>(s2a); s1b += dppf<0x122>(s1b); s2b += dppf<0x122>(s2b);
      s1a += dppf<0x124>(s1a); s2a += dppf<0x124>(s2a); s1b += dppf<0x124>(s1b); s2b += dppf<0x124>(s2b);
      s1a += dppf<0x128>(s1a); s2a += dppf<0x128>(s2a); s1b += dppf<0x128>(s1b); s2b += dppf<0x128>(s2b);
      const float s1 = xg_sum((n & 1) ? s1b : s1a), s2 = xg_sum((n & 1) ? s2b : s2a);
      const f32x4 tt = {s1, s2, 0.f, 0.f};
      v4u a0, a1;
#pragma unroll
      for (int q = 0; q < 4; ++q) { a0[q] = ln_apply(c, xk0[q], tt); a1[q] = ln_apply(c, xk1[q], tt); }
      v4u W[16];
      tr8x8(rq[0], W); tr8x8(rq[1], W + 8);
#pragma unroll
      for (int t8 = 0; t8 < 8; ++t8) acc[t8] = mfma(a0, W[t8], f32x4{0.f, 0.f, 0.f, 0.f});
#pragma unroll
      for (int t8 = 0; t8 < 8; ++t8) acc[t8] = mfma(a1, W[8 + t8], acc[t8]);
    } else {
#pragma unroll
      for (int i = 0; i < 8; ++i) ru[i] = *(const GAS v4u*)(wu + (size_t)(4 * i) * 8192);
    }
    while (cnt[0] == 0u && spin_ok(c)) {}
    if (c.w < 2 && c.lane < 16) {
      const int o = OFF_XQ + h * 8192 + (2 * e + c.w) * 1024 + c.lane * 32;
      st128(c.rs, o, v4u{canon(f2u(acc[0].x)), canon(f2u(acc[1].x)), canon(f2u(acc[2].x)), canon(f2u(acc[3].x))});
      st128(c.rs, o + 16, v4u{canon(f2u(acc[4].x)), canon(f2u(acc[5].x)), canon(f2u(acc[6].x)), canon(f2u(acc[7].x))});
      st128(c.rs, o + 512, v4u{canon(f2u(acc[0].y)), canon(f2u(acc[1].y)), canon(f2u(acc[2].y)), canon(f2u(acc[3].y))});
      st128(c.rs, o + 528, v4u{canon(f2u(acc[4].y)), canon(f2u(acc[5].y)), canon(f2u(acc[6].y)), canon(f2u(acc[7].y))});
    }
    STAMP(c, 7);
    {
      const unsigned ib = L_QA + c.w * 4096;
#pragma unroll
      for (int hf = 0; hf < 2; ++hf) {
#pragma unroll
        for (int i = 0; i < 4; ++i) { const unsigned row = 4 * i + g; *(LAS v4u*)(c.L + ib + row * 256 + (((unsigned)n ^ swz(row)) << 4)) = ru[4 * hf + i]; }
        v4u tmp[4];
#pragma unroll
        for (int ks = 0; ks < 4; ++ks) tmp[ks] = *(const LAS v4u*)(c.L + ib + n * 256 + (((unsigned)(4 * ks + g) ^ swz(n)) << 4));
#pragma unroll
        for (int ks = 0; ks < 4; ++ks) ru[4 * hf + ks] = tmp[ks];
      }
    }
    wait_ready_a(c, nonce, h, 0);
    {
      const int o = OFF_XQ + h * 8192 + (c.w * 64 + c.lane) * 4;
      unsigned v[8];
      for (;;) {
        CBAR();
#pragma unroll
        for (int i = 0; i < 8; ++i) v[i] = ld32(c.rs, o + i * 1024);
        const unsigned m = max(umax3(umax3(v[0], v[1], v[2]), v[3], v[4]), umax3(v[5], v[6], v[7]));
        if (__all(m != SENT) || !spin_ok(c)) break;
      }
      const float sum = ((u2f(v[0]) + u2f(v[1])) + (u2f(v[2]) + u2f(v[3]))) + ((u2f(v[4]) + u2f(v[5])) + (u2f(v[6]) + u2f(v[7])));
      *(LAS __bf16*)(c.L + L_VEC + (c.w * 64 + c.lane) * 2) = (__bf16)sum;
      STAMP(c, 22);
    }
    asm volatile("s_waitcnt lgkmcnt(0)" ::: "memory");
    if (c.lane == 0) __hip_atomic_fetch_add((LAS unsigned*)(c.L + L_CNT + 4), 1u, __ATOMIC_RELAXED, __HIP_MEMORY_SCOPE_WORKGROUP);
    while (cnt[1] < 4u && spin_ok(c)) {}
    CBAR();
    f32x4 acc0 = {0.f, 0.f, 0.f, 0.f}, acc1 = {0.f, 0.f, 0.f, 0.f};
#pragma unroll
    for (int ks = 0; ks < 4; ++ks) {
      const v4u a = *(const LAS v4u*)(c.L + L_VEC + (n & 1) * 256 + ks * 64 + g * 16);
      acc0 = mfma(a, ru[ks], acc0);
      acc1 = mfma(a, ru[4 + ks], acc1);
    }
    const float n00 = dppf<0x101>(acc0.x), n01 = dppf<0x101>(acc0.y), n10 = dppf<0x101>(acc1.x), n11 = dppf<0x101>(acc1.y);
    if (c.lane < 16 && (c.lane & 1) == 0) {
      const int o = OFF_X1 + h * 1024 + (128 * e + 32 * c.w + n) * 2;
      st32(c.rs, o, canon(pack2(acc0.x, n00)));
      st32(c.rs, o + 32, canon(pack2(acc1.x, n10)));
      st32(c.rs, o + 32 * 1024, canon(pack2(acc0.y, n01)));
      st32(c.rs, o + 32 * 1024 + 32, canon(pack2(acc1.y, n11)));
    }
    STAMP(c, 21);
  } else {
    while (cnt[0] == 0u && spin_ok(c)) { __builtin_amdgcn_s_sleep(2); }
    __builtin_amdgcn_s_sleep(D_PC);
    fill_attn(c, 2 * (c.w - 4)); fill_attn(c, 2 * (c.w - 4) + 1);
    wait_ready_a(c, nonce, 16 * c.G + c.w, 8);
    while (cnt[1] < PC_WAIT && spin_ok(c)) { __builtin_amdgcn_s_sleep(2); }
    ckv_dma<32>(c, p, (c.w - 4) * 32);
    return;
  }
  wait_ready_a(c, nonce, 16 * c.G + c.w, 8);
}

DI void role_q(Ctx& c, const Params& p, v4u nonce, int h, int ch) {
  asm volatile("; role Q" ::: "memory");
  const bool xpub = (h == 0 && ch == 0);
  DRAIN();
  LBAR();
  STAMP(c, 20);
#pragma unroll 1
  for (int l = 0; l < 2; ++l) {
    attn_phase(c, l, NoHook());
    if (l == 0) {
      const int lane = c.lane, n = lane & 15, g = lane >> 4, hb = lane >> 5;
      v4u Wq[16], ru[8];
      {
        v4u rq[2][8];
        const GAS unsigned char* wq = (const GAS unsigned char*)p.wq + ((size_t)(64 * c.w + 16 * g) * 4096 + h * 128 + 8 * n) * 2;
#pragma unroll
        for (int ks = 0; ks < 2; ++ks)
#pragma unroll
          for (int j = 0; j < 8; ++j) rq[ks][j] = *(const GAS v4u*)(wq + (size_t)(8 * ks + j) * 8192);
        const GAS unsigned char* wu = (const GAS unsigned char*)p.wuk + ((size_t)(256 * ch + 32 * c.w + 2 * n) * 4096 + h * 128 + 8 * g) * 2;
#pragma unroll
        for (int t = 0; t < 2; ++t)
#pragma unroll
          for (int ks = 0; ks < 4; ++ks) ru[t * 4 + ks] = *(const GAS v4u*)(wu + (size_t)t * 8192 + ks * 64);
        tr8x8(rq[0], Wq); tr8x8(rq[1], Wq + 8);
      }
      asm volatile("" :: "v"(ru[0]), "v"(ru[1]), "v"(ru[2]), "v"(ru[3]), "v"(ru[4]), "v"(ru[5]), "v"(ru[6]), "v"(ru[7]));
      fill_attn(c, c.w, 1);
      publish_ready_b(c, nonce, 2);
      wait_ready_b(c, nonce);
      gate<D_Q4>(c, OFF_X3 + (0 * 2 + (h & 1)) * XV_STR + h * 1024);
      unsigned xp;
      {
        const int o = OFF_X4 + (0 * 2 + hb) * XV_STR + 128 * c.w + 4 * (lane & 31);
        unsigned v[33];
        for (;;) {
          CBAR();
#pragma unroll
          for (int i = 0; i < 33; ++i) v[i] = ld32(c.rs, o + i * 1024);
          unsigned m = v[32];
#pragma unroll
          for (int i = 0; i < 32; i += 2) m = umax3(m, v[i], v[i + 1]);
          if (__all(m != SENT) || !spin_ok(c)) break;
        }
        float s0 = 0.f, s1 = 0.f, s2 = 0.f, s3 = 0.f;
#pragma unroll
        for (int i = 0; i < 32; i += 2) { s0 = dot2(v[i], LO1, s0); s1 = dot2(v[i], HI1, s1); s2 = dot2(v[i + 1], LO1, s2); s3 = dot2(v[i + 1], HI1, s3); }
        const unsigned o01 = pack2(s0 + s2, s1 + s3);
        xp = pack2(dot2(o01, LO1, bf_lo(v[32])), dot2(o01, HI1, bf_hi(v[32])));
        STAMP(c, 5);
        if (xpub) st32(c.rs, OFF_X3 + (1 * 2 + hb) * XV_STR + 32 * 1024 + 128 * c.w + 4 * (lane & 31), canon(xp));
      }
      float t1 = dot2(xp, ONES, 0.f), t2 = dot2(xp, xp, 0.f);
      t1 += dppf<0x121>(t1); t2 += dppf<0x121>(t2);
      t1 += dppf<0x122>(t1); t2 += dppf<0x122>(t2);
      t1 += dppf<0x124>(t1); t2 += dppf<0x124>(t2);
      t1 += dppf<0x128>(t1); t2 += dppf<0x128>(t2);
      t1 += dppm<0x142, 0xa>(t1); t2 += dppm<0x142, 0xa>(t2);
      if ((lane & 31) == 31) *(LAS f32x2*)(c.L + L_LN + hb * 64 + c.w * 8) = f32x2{t1, t2};
      LBAR();
      f32x4 t[4];
#pragma unroll
      for (int i = 0; i < 4; ++i) t[i] = *(const LAS f32x4*)(c.L + L_LN + hb * 64 + i * 16);
      const unsigned n01 = ln_apply(c, xp, (t[0] + t[1]) + (t[2] + t[3]));
      *(LAS unsigned*)(c.L + L_FR + c.w * 256 + lane * 4) = n01;
      const v4u a0 = *(const LAS v4u*)(c.L + L_FR + c.w * 256 + (n & 1) * 128 + g * 32);
      const v4u a1 = *(const LAS v4u*)(c.L + L_FR + c.w * 256 + (n & 1) * 128 + g * 32 + 16);
      f32x4 acc[8];
#pragma unroll
      for (int t8 = 0; t8 < 8; ++t8) acc[t8] = mfma(a0, Wq[t8], f32x4{0.f, 0.f, 0.f, 0.f});
#pragma unroll
      for (int t8 = 0; t8 < 8; ++t8) acc[t8] = mfma(a1, Wq[8 + t8], acc[t8]);
      if (lane < 16) {
        *(LAS f32x4*)(c.L + L_RED + c.w * 1024 + lane * 32) = f32x4{acc[0].x, acc[1].x, acc[2].x, acc[3].x};
        *(LAS f32x4*)(c.L + L_RED + c.w * 1024 + lane * 32 + 16) = f32x4{acc[4].x, acc[5].x, acc[6].x, acc[7].x};
        *(LAS f32x4*)(c.L + L_RED + c.w * 1024 + 512 + lane * 32) = f32x4{acc[0].y, acc[1].y, acc[2].y, acc[3].y};
        *(LAS f32x4*)(c.L + L_RED + c.w * 1024 + 512 + lane * 32 + 16) = f32x4{acc[4].y, acc[5].y, acc[6].y, acc[7].y};
      }
      LBAR();
      if (c.w < 4) {
        const int d = c.w * 64 + lane;
        const LAS float* R = (const LAS float*)(c.L + L_RED);
        const float sum = ((R[d] + R[256 + d]) + (R[512 + d] + R[768 + d])) + ((R[1024 + d] + R[1280 + d]) + (R[1536 + d] + R[1792 + d]));
        *(LAS __bf16*)(c.L + L_VEC + d * 2) = (__bf16)sum;
      }
      LBAR();
      f32x4 acc0 = {0.f, 0.f, 0.f, 0.f}, acc1 = {0.f, 0.f, 0.f, 0.f};
#pragma unroll
      for (int ks = 0; ks < 4; ++ks) {
        const v4u a = *(const LAS v4u*)(c.L + L_VEC + (n & 1) * 256 + ks * 64 + g * 16);
        acc0 = mfma(a, ru[ks], acc0);
        acc1 = mfma(a, ru[4 + ks], acc1);
      }
      if (lane < 16) {
        const int o = OFF_X1 + (1 * 2 * 32 + h) * 1024 + (256 * ch + 32 * c.w + 2 * n) * 2;
        st32(c.rs, o, canon(pack2(acc0.x, acc1.x)));
        st32(c.rs, o + 32 * 1024, canon(pack2(acc0.y, acc1.y)));
      }
      STAMP(c, 6);
    }
  }
  gate<D_Q4>(c, OFF_X3 + (1 * 2 + (h & 1)) * XV_STR + h * 1024);
  if (c.w < 2) {
    const int qi = 2 * h + ch;
    const int off = OFF_X4 + (1 * 2 + c.w) * XV_STR + (c.lane < 33 ? c.lane : 32) * 1024 + qi * 16;
    v4u v;
    for (;;) {
      CBAR();
      v = ld128(c.rs, off);
      if (__all(max(umax3(v.x, v.y, v.z), v.w) != SENT) || !spin_ok(c)) break;
    }
    const unsigned wl = c.lane < 32 ? LO1 : 0u, wh = c.lane < 32 ? HI1 : 0u;
    v4u o;
#pragma unroll
    for (int q = 0; q < 4; ++q) {
      float lo = dot2(v[q], wl, 0.f), hi = dot2(v[q], wh, 0.f);
      lo = quad_sum(lo); hi = quad_sum(hi);
      lo += dppf<0x124>(lo); hi += dppf<0x124>(hi);
      lo += dppf<0x128>(lo); hi += dppf<0x128>(hi);
      lo += dppm<0x142, 0xa>(lo); hi += dppm<0x142, 0xa>(hi);
      const unsigned s2 = (unsigned)__builtin_amdgcn_readlane((int)pack2(lo, hi), 31);
      const unsigned r2 = (unsigned)__builtin_amdgcn_readlane((int)v[q], 32);
      o[q] = pack2(bf_lo(r2) + bf_lo(s2), bf_hi(r2) + bf_hi(s2));
    }
    if (c.lane == 0) *(GAS v4u*)((GAS unsigned char*)p.out + c.w * 1024 + qi * 16) = o;
    STAMP(c, 16);
  }
  __builtin_amdgcn_s_barrier();
  if (c.w == 0 && c.lane < 16) st128(c.rs, OFF_RDY + (c.blk + 64 * (c.lane >> 2)) * 64 + (c.lane & 3) * 16, v4u{~nonce.x, ~nonce.y, ~nonce.z, ~nonce.w});
}

DI void role_a(Ctx& c, const Params& p, v4u nonce) {
  asm volatile("; role A" ::: "memory");
  DRAIN();
  LBAR();
  STAMP(c, 20);
#pragma unroll 1
  for (int l = 0; l < 2; ++l) {
    attn_phase(c, l, NoHook());
    if (l == 0) { __builtin_amdgcn_s_sleep(100); fill_attn(c, c.w, 1); publish_ready_b(c, nonce, 2); wait_ready_b(c, nonce); gate<D_A1>(c, OFF_X4 + (0 * 2 + c.b) * XV_STR + (c.s & 31) * 1024); }
  }
}

DI void role_o(Ctx& c, const Params& p, v4u nonce, int h) {
  asm volatile("; role O" ::: "memory");
  const int n = c.lane & 15, g = c.lane >> 4;
  const v4u sv = {SENT, SENT, SENT, SENT};
  if (D_START > 0) __builtin_amdgcn_s_sleep(D_START);
  ckv_dma<16>(c, p, c.w * 16);
  fill_attn(c, c.w);
  if (c.w < 2) st128(c.rs, OFF_X3 + (c.w * 2 + c.b) * XV_STR + h * 1024 + c.lane * 16, sv);
  DRAIN();
  __builtin_amdgcn_sched_barrier(0);
  v4u Wv[16], Wo[16];
  {
    v4u rv[2][8];
    const GAS unsigned char* wv = (const GAS unsigned char*)p.wuv + ((size_t)(64 * c.w + 2 * g) * 4096 + h * 128 + 8 * n) * 2;
#pragma unroll
    for (int ks = 0; ks < 2; ++ks)
#pragma unroll
      for (int j = 0; j < 8; ++j) rv[ks][j] = *(const GAS v4u*)(wv + (size_t)(16 * (j & 3) + 8 * ks + (j >> 2)) * 8192);
    tr8x8(rv[0], Wv); tr8x8(rv[1], Wv + 8);
  }
  wait_ready_a(c, nonce, 16 * c.G + c.w, 8);
  DRAIN();
  LBAR();
  STAMP(c, 20);
  gate<0>(c, OFF_X1 + (c.b * 32 + 16 * c.G + (c.s & 15)) * 1024 + (c.s >> 4) * 256);
  auto o_phase = [&](int l) __attribute__((always_inline)) {
    RELANE(c);
    const int n = c.lane & 15, g = c.lane >> 4;
    float inv;
    {
      const int bs = OFF_ST + ((l * 2 + c.b) * 32 + h) * 512 + c.lane * 8;
      v2u st;
      for (;;) {
        CBAR();
        st = ld64(c.rs, bs);
        const unsigned bad = st.x | st.y;
        if (__all((bad & 1u) == 0u) || !spin_ok(c)) break;
      }
      const float kf = u2f(st.x & ~1u);
      float K = kf;
      K = fmaxf(K, dppf<0x128>(K)); K = fmaxf(K, dppf<0x124>(K)); K = fmaxf(K, dppf<0x122>(K)); K = fmaxf(K, dppf<0x121>(K));
      K = xg_max(K);
      const int d = (int)(K - kf);
      const unsigned eb = d < 127 ? (unsigned)(127 - d) << 7 : 0u;
      float Ls = u2f(st.y) * u2f(eb << 16);
      Ls += dppf<0x128>(Ls); Ls += dppf<0x124>(Ls); Ls += dppf<0x122>(Ls); Ls += dppf<0x121>(Ls);
      Ls = xg_sum(Ls);
      inv = 1.0f / Ls;
      *(LAS unsigned*)(c.L + L_EW + c.w * 256 + c.lane * 4) = eb;
    }
    unsigned el[16];
#pragma unroll
    for (int i = 0; i < 4; ++i) {
      const v4u e4 = *(const LAS v4u*)(c.L + L_EW + c.w * 256 + g * 64 + i * 16);
      el[4 * i] = e4.x; el[4 * i + 1] = e4.y; el[4 * i + 2] = e4.z; el[4 * i + 3] = e4.w;
    }
    const int bx = OFF_X2 + ((l * 2 + c.b) * 32 + h) * 65536 + c.w * 8192 + g * 2048 + n * 8;
    if (D_O2 > 0) __builtin_amdgcn_s_sleep(D_O2);
    v2u v[16];
    for (;;) {
      CBAR();
#pragma unroll
      for (int i = 0; i < 16; ++i) v[i] = ld64(c.rs, bx + i * 128);
      unsigned m = 0u;
#pragma unroll
      for (int i = 0; i < 16; ++i) m = m | v[i].x | v[i].y;
      if (__all((m & 1u) == 0u) || !spin_ok(c)) break;
    }
    STAMP(c, 5 + 8 * l);
    float s0 = 0.f, s1 = 0.f, s2 = 0.f, s3 = 0.f;
#pragma unroll
    for (int i = 0; i < 16; ++i) {
      const unsigned eh = el[i] << 16;
      s0 = dot2(v[i].x, el[i], s0); s1 = dot2(v[i].x, eh, s1); s2 = dot2(v[i].y, el[i], s2); s3 = dot2(v[i].y, eh, s3);
    }
    s0 = xg_sum(s0) * inv; s1 = xg_sum(s1) * inv; s2 = xg_sum(s2) * inv; s3 = xg_sum(s3) * inv;
    const unsigned h01 = pack2(s0, s1), h23 = pack2(s2, s3);
    const unsigned l01 = pack2(dot2(h01, NLO1, s0), dot2(h01, NHI1, s1)), l23 = pack2(dot2(h23, NLO1, s2), dot2(h23, NHI1, s3));
    *(LAS v2u*)(c.L + L_FR + c.w * 256 + (g & 1) * 128 + n * 8) = (g & 1) ? v2u{l01, l23} : v2u{h01, h23};
    const v4u a0 = *(const LAS v4u*)(c.L + L_FR + c.w * 256 + (n & 1) * 128 + g * 16);
    const v4u a1 = *(const LAS v4u*)(c.L + L_FR + c.w * 256 + (n & 1) * 128 + 64 + g * 16);
    asm volatile("" :: "v"(a0), "v"(a1));
    STAMP(c, 7 + 8 * l);
    gemv_ksplit<true>(c, a0, a1, Wv);
    STAMP(c, 22 + 2 * l);
    reduce128(c);
    STAMP(c, 23 + 2 * l);
    gemv_nsplit<2>(c, Wo, OFF_X3 + (l * 2 + c.b) * XV_STR + h * 1024);
    STAMP(c, 6 + 8 * l);
  };
  {
    v4u ro[2][8];
    attn_phase(c, 0, [&]() __attribute__((always_inline)) {
      const GAS unsigned char* wo = (const GAS unsigned char*)p.wo + ((size_t)(h * 128 + 32 * (n >> 3) + 8 * g) * 512 + 64 * c.w + 8 * (n & 7)) * 2;
#pragma unroll
      for (int kb = 0; kb < 2; ++kb)
#pragma unroll
        for (int j = 0; j < 8; ++j) ro[kb][j] = *(const GAS v4u*)(wo + (size_t)(64 * kb + j) * 1024);
      __builtin_amdgcn_sched_barrier(0);
    });
    tr8x8(ro[0], Wo); tr8x8(ro[1], Wo + 8);
  }
  wait_ready_b(c, nonce);
  o_phase(0);
  fill_attn(c, c.w, 1);
  publish_ready_b(c, nonce, 2);
  gate<0>(c, OFF_X4 + (0 * 2 + c.b) * XV_STR + h * 1024);
  wait_ready_b(c, nonce, 2);
  gate<D_A1 - 16>(c, -1);
  attn_phase(c, 1, NoHook());
  o_phase(1);
}

DI void role_f(Ctx& c, const Params& p, v4u nonce, int j) {
  asm volatile("; role F" ::: "memory");
  const int n = c.lane & 15, g = c.lane >> 4;
  const v4u sv = {SENT, SENT, SENT, SENT};
  if (D_START > 0) __builtin_amdgcn_s_sleep(D_START);
  ckv_dma<16>(c, p, c.w * 16);
  fill_attn(c, c.w);
  if (c.w < 2) st128(c.rs, OFF_X4 + (c.w * 2 + c.b) * XV_STR + j * 1024 + c.lane * 16, sv);
  if (j == 0 && (c.w == 2 || c.w == 3)) st128(c.rs, OFF_X4 + ((c.w - 2) * 2 + c.b) * XV_STR + 32 * 1024 + c.lane * 16, sv);
  v4u W13[16], W2[8];
  wait_ready_a(c, nonce, 16 * c.G + c.w, 8);
  DRAIN();
  LBAR();
  STAMP(c, 20);
  gate<0>(c, OFF_X1 + (c.b * 32 + 16 * c.G + (c.s & 15)) * 1024 + (c.s >> 4) * 256);
  auto f_phase = [&](int l) __attribute__((always_inline)) {
    RELANE(c);
    unsigned xp;
    if (l == 0) { xp = gather33<false>(c, OFF_X3 + (0 * 2 + c.b) * XV_STR, p.x + c.b * 512); }
    else xp = gather33<true>(c, OFF_X3 + (1 * 2 + c.b) * XV_STR, nullptr);
    STAMP(c, 5 + 8 * l);
    if (j == 0 && c.lane < 32) st32(c.rs, OFF_X4 + (l * 2 + c.b) * XV_STR + 32 * 1024 + 128 * c.w + 4 * c.lane, canon(xp));
    v4u a0, a1;
    layernorm(c, xp, a0, a1);
    STAMP(c, 7 + 8 * l);
    gemv_ksplit<false>(c, a0, a1, W13);
    STAMP(c, 22 + 2 * l);
    LBAR();
    STAMP(c, 8 + 8 * l);
    if (c.w == 0) {
      const int li = c.lane < 56 ? c.lane : 55;
      const unsigned gu = pack2(red_sum8(c, li), red_sum8(c, 56 + li));
      const float gt = bf_lo(gu);
      const float sg = rbf(gt * __builtin_amdgcn_rcpf(1.0f + __expf(-gt)));
      const float hv = c.lane < 56 ? sg * bf_hi(gu) : 0.f;
      *(LAS __bf16*)(c.L + L_VEC + c.lane * 2) = (__bf16)hv;
    }
    LBAR();
    STAMP(c, 23 + 2 * l);
    gemv_nsplit<1>(c, W2, OFF_X4 + (l * 2 + c.b) * XV_STR + j * 1024);
    STAMP(c, 6 + 8 * l);
  };
  attn_phase(c, 0, NoHook());
  {
  __builtin_amdgcn_sched_barrier(0);
      v4u r13[2][8], r2[8];
      {
        const int nn = n < 14 ? n : 13;
        const u16* wsel = nn < 7 ? p.w1 : p.w3;
        const int col = 56 * j + 8 * (nn < 7 ? nn : nn - 7);
        const GAS unsigned char* wa = (const GAS unsigned char*)wsel + ((size_t)(64 * c.w + 16 * g) * 1792 + col) * 2;
#pragma unroll
        for (int ks = 0; ks < 2; ++ks)
#pragma unroll
          for (int jj = 0; jj < 8; ++jj) r13[ks][jj] = *(const GAS v4u*)(wa + (size_t)(8 * ks + jj) * 3584);
#pragma unroll
        for (int jj = 0; jj < 8; ++jj) {
          int f = 32 * (n >> 3) + 8 * g + jj; f = f < 56 ? f : 55;
          r2[jj] = *(const GAS v4u*)((const GAS unsigned char*)p.w2 + ((size_t)(56 * j + f) * 512 + 64 * c.w + 8 * (n & 7)) * 2);
        }
      }
      __builtin_amdgcn_sched_barrier(0);
      tr8x8(r13[0], W13); tr8x8(r13[1], W13 + 8);
      tr8x8(r2, W2);
      fill_attn(c, c.w, 1);
      publish_ready_b(c, nonce, 2);
      wait_ready_b(c, nonce);
  }
  f_phase(0);
  gate<D_FA1>(c, -1);
  attn_phase(c, 1, NoHook());
  gate<D_F3>(c, -1);
  f_phase(1);
}

__global__ void __launch_bounds__(NT, 2) mega(Params p) {
  extern __shared__ __attribute__((aligned(16))) unsigned char smem[];
  Ctx c;
  c.rs = __builtin_amdgcn_make_buffer_rsrc((void*)p.ws, 0, WS_BYTES, 0x00020000);
  c.L = (LAS unsigned char*)smem;
  c.lane = threadIdx.x & 63;
  c.w = __builtin_amdgcn_readfirstlane(threadIdx.x >> 6);
  c.blk = blockIdx.x;
  const int x = c.blk & 7, slot = c.blk >> 3;
  c.b = (slot >> 3) & 1; c.G = slot >> 4; c.s = 8 * x + (slot & 7);
  const int r = 8 * c.G + (slot & 7);
  c.spins = 0; c.wsp = p.ws;
  c.kLO1 = sconst(0x00003F80u); c.kHI1 = sconst(0x3F800000u); c.kONES = sconst(0x3F803F80u); c.kNLO1 = sconst(0x0000BF80u); c.kNHI1 = sconst(0xBF800000u);
  STAMP(c, 0);
  const unsigned long long id = llvm_dispatch_id();
  const unsigned long long dp = (unsigned long long)__builtin_amdgcn_dispatch_ptr();
  const v4u nonce = {(unsigned)id ^ 0x5bd1e995u, (unsigned)(id >> 32) ^ 0x9e3779b9u, (unsigned)dp ^ 0x85ebca6bu, (unsigned)(dp >> 32) ^ 0xc2b2ae35u};
  c.nonce = nonce;
  if (slot >= 16 && slot < 28) {
    unsigned long long pc0;
    asm volatile("s_getpc_b64 %0" : "=s"(pc0));
    const GAS unsigned char* cp = (const GAS unsigned char*)(pc0 & ~63ull) + (slot - 16) * 4096 + threadIdx.x * 8;
    const v2u cv = *(const GAS v2u*)cp;
    asm volatile("" :: "v"(cv));
  }
  if (slot < 8) {
    const v4u sv = {SENT, SENT, SENT, SENT};
    const int h = 4 * x + (r >> 1), ch = r & 1;
    if (c.w == 7) st128(c.rs, OFF_X1 + ((1 * 2 + (c.lane >> 5)) * 32 + h) * 1024 + ch * 512 + (c.lane & 31) * 16, sv);
    if (h == 0 && ch == 0 && c.w >= 6) st128(c.rs, OFF_X3 + (1 * 2 + (c.w - 6)) * XV_STR + 32 * 1024 + c.lane * 16, sv);
  }
  if (slot < 16) prologue(c, p, nonce, 4 * x + (slot >> 2), slot & 3);
  if (r < 8) { if (c.b == 0) role_q(c, p, nonce, 4 * x + (r >> 1), r & 1); else role_a(c, p, nonce); }
  else if (r < 12) role_o(c, p, nonce, 4 * x + (r - 8));
  else role_f(c, p, nonce, 4 * x + (r - 12));
}
}

extern "C" void kernel_launch(void* const* d_in, const int* in_sizes, int n_in,
                              void* d_out, int out_size, void* d_ws, size_t ws_size,
                              hipStream_t stream) {
  mk::Params p;
  p.x = (const u16*)d_in[0]; p.ckv = (const u16*)d_in[1]; p.wq = (const u16*)d_in[2]; p.wuk = (const u16*)d_in[3];
  p.wuv = (const u16*)d_in[4]; p.wo = (const u16*)d_in[5]; p.w1 = (const u16*)d_in[7]; p.w3 = (const u16*)d_in[8];
  p.w2 = (const u16*)d_in[9]; p.out = (u16*)d_out; p.ws = (unsigned char*)d_ws;
  (void)hipFuncSetAttribute((const void*)mk::mega, hipFuncAttributeMaxDynamicSharedMemorySize, mk::L_BYTES + 2048);
  hipLaunchKernelGGL(mk::mega, dim3(256), dim3(mk::NT), mk::L_BYTES + 2048, stream, p);
}
```

```cpp
#include <hip/hip_runtime.h>
#include <hip/hip_bf16.h>
#include <stdint.h>

#define LAS __attribute__((address_space(3)))
#define GAS __attribute__((address_space(1)))
typedef unsigned short u16;
typedef unsigned v4u __attribute__((ext_vector_type(4)));
typedef unsigned v2u __attribute__((ext_vector_type(2)));
typedef float f32x4 __attribute__((ext_vector_type(4)));
typedef float f32x2 __attribute__((ext_vector_type(2)));
typedef short bf16x8 __attribute__((ext_vector_type(8)));
typedef short s16x4 __attribute__((ext_vector_type(4)));
typedef __bf16 bf16x2_t __attribute__((ext_vector_type(2)));
typedef unsigned short u16x2 __attribute__((ext_vector_type(2)));
#define DI __device__ __forceinline__

extern "C" __device__ unsigned long long llvm_dispatch_id() __asm("llvm.amdgcn.dispatch.id");

#define PROBE_T 900
namespace mk {
constexpr int NT = 512;
constexpr unsigned SENT = 0xFFFFFFFFu;
constexpr unsigned SPIN_CAP = 1u << 16;
constexpr int AUX_SC1 = 16;
#define LO1 (c.kLO1)
#define HI1 (c.kHI1)
#define ONES (c.kONES)
#define NLO1 (c.kNLO1)
#define NHI1 (c.kNHI1)

constexpr int OFF_RDY = 0;
constexpr int OFF_X1 = 16384;
constexpr int OFF_ST = OFF_X1 + 2 * 2 * 32 * 1024;
constexpr int OFF_X3 = OFF_ST + 2 * 2 * 32 * 512;
constexpr int XV_STR = 34 * 1024;
constexpr int OFF_X4 = OFF_X3 + 4 * XV_STR;
constexpr int OFF_X2 = OFF_X4 + 4 * XV_STR;
constexpr int OFF_XQ = OFF_X2 + 2 * 2 * 32 * 65536;
constexpr int WS_BYTES = OFF_XQ + 32 * 8192;

constexpr int L_IMG = 0;
constexpr int L_QA = 131072;
constexpr int L_PB = L_QA + 16384;
constexpr int L_SB = L_PB + 4096;
constexpr int L_LN = L_SB + 1024;
constexpr int L_VEC = L_LN + 128;
constexpr int L_EW = L_VEC + 512;
constexpr int L_FR = L_EW + 2048;
constexpr int L_CNT = L_FR + 2048;
constexpr int L_BYTES = L_CNT + 16;
constexpr int L_RED = L_QA;

struct Params {
  const u16* x; const u16* ckv; const u16* wq; const u16* wuk; const u16* wuv; const u16* wo;
  const u16* w1; const u16* w3; const u16* w2; u16* out; unsigned char* ws;
};

DI float u2f(unsigned u) { return __builtin_bit_cast(float, u); }
DI unsigned f2u(float f) { return __builtin_bit_cast(unsigned, f); }
DI float bf_lo(unsigned u) { return u2f(u << 16); }
DI float bf_hi(unsigned u) { return u2f(u & 0xFFFF0000u); }
DI float rbf(float f) { return (float)(__bf16)f; }
DI unsigned pack2(float lo, float hi) { bf16x2_t v = {(__bf16)lo, (__bf16)hi}; return __builtin_bit_cast(unsigned, v); }
DI unsigned canon(unsigned u) { return u < 0xFFFFFFFEu ? u : 0xFFFFFFFEu; }
DI unsigned par(unsigned u, int l) { return u & ~1u; }
DI float dot2(unsigned a, unsigned b, float c) { return __builtin_amdgcn_fdot2_f32_bf16(__builtin_bit_cast(bf16x2_t, a), __builtin_bit_cast(bf16x2_t, b), c, false); }
DI unsigned subsat(unsigned a, unsigned b) { return __builtin_bit_cast(unsigned, __builtin_elementwise_sub_sat(__builtin_bit_cast(u16x2, a), __builtin_bit_cast(u16x2, b))); }
DI unsigned umax3(unsigned a, unsigned b, unsigned c) { return max(max(a, b), c); }
DI f32x4 mfma(v4u a, v4u b, f32x4 c) { return __builtin_amdgcn_mfma_f32_16x16x32_bf16(__builtin_bit_cast(bf16x8, a), __builtin_bit_cast(bf16x8, b), c, 0, 0, 0); }
template <int CTRL> DI float dppf(float v) { return u2f((unsigned)__builtin_amdgcn_update_dpp(0, (int)f2u(v), CTRL, 0xf, 0xf, true)); }
template <int CTRL> DI unsigned dppu(unsigned v) { return (unsigned)__builtin_amdgcn_update_dpp(0, (int)v, CTRL, 0xf, 0xf, true); }
template <int CTRL, int RM> DI float dppm(float v) { return u2f((unsigned)__builtin_amdgcn_update_dpp(0, (int)f2u(v), CTRL, RM, 0xf, false)); }
DI float quad_sum(float v) { v += dppf<0xB1>(v); v += dppf<0x4E>(v); return v; }
DI float xg_sum(float v) {
  const auto a = __builtin_amdgcn_permlane16_swap(f2u(v), f2u(v), false, false);
  const unsigned a0 = a[0], a1 = a[1];
  v = u2f(a0) + u2f(a1);
  const auto b = __builtin_amdgcn_permlane32_swap(f2u(v), f2u(v), false, false);
  const unsigned b0 = b[0], b1 = b[1];
  return u2f(b0) + u2f(b1);
}
DI float x32_sum(float v) {
  const auto b = __builtin_amdgcn_permlane32_swap(f2u(v), f2u(v), false, false);
  const unsigned b0 = b[0], b1 = b[1];
  return u2f(b0) + u2f(b1);
}
DI float xg_max(float v) {
  const auto a = __builtin_amdgcn_permlane16_swap(f2u(v), f2u(v), false, false);
  const unsigned a0 = a[0], a1 = a[1];
  v = fmaxf(u2f(a0), u2f(a1));
  const auto b = __builtin_amdgcn_permlane32_swap(f2u(v), f2u(v), false, false);
  const unsigned b0 = b[0], b1 = b[1];
  return fmaxf(u2f(b0), u2f(b1));
}
DI unsigned swz(unsigned r) { return (((r ^ ((r >> 1) & 4u)) & 7u) << 1) | ((r >> 3) & 1u); }

typedef __amdgpu_buffer_rsrc_t rsrc_t;
DI v4u ld128(rsrc_t rs, int off) { return __builtin_amdgcn_raw_buffer_load_b128(rs, off, 0, AUX_SC1); }
DI v2u ld64(rsrc_t rs, int off) { return __builtin_amdgcn_raw_buffer_load_b64(rs, off, 0, AUX_SC1); }
DI unsigned ld32(rsrc_t rs, int off) { return __builtin_amdgcn_raw_buffer_load_b32(rs, off, 0, AUX_SC1); }
DI void st128(rsrc_t rs, int off, v4u v) { __builtin_amdgcn_raw_buffer_store_b128(v, rs, off, 0, AUX_SC1); }
DI void st64(rsrc_t rs, int off, v2u v) { __builtin_amdgcn_raw_buffer_store_b64(v, rs, off, 0, AUX_SC1); }
DI void st32(rsrc_t rs, int off, unsigned v) { __builtin_amdgcn_raw_buffer_store_b32(v, rs, off, 0, AUX_SC1); }
#define CBAR() asm volatile("" ::: "memory")
#define DRAIN() asm volatile("s_waitcnt vmcnt(0)" ::: "memory")
#define LBAR() do { asm volatile("s_waitcnt lgkmcnt(0)" ::: "memory"); __builtin_amdgcn_s_barrier(); asm volatile("" ::: "memory"); } while (0)

DI void tr8x8(const v4u (&r)[8], v4u* f) {
#pragma unroll
  for (int q = 0; q < 4; ++q)
#pragma unroll
    for (int d = 0; d < 4; ++d) {
      const unsigned a = r[2 * d][q], b = r[2 * d + 1][q];
      f[2 * q][d] = __builtin_amdgcn_perm(b, a, 0x05040100u);
      f[2 * q + 1][d] = __builtin_amdgcn_perm(b, a, 0x07060302u);
    }
}
DI void tr8x4(const v2u (&r)[8], v4u* f) {
#pragma unroll
  for (int q = 0; q < 2; ++q)
#pragma unroll
    for (int d = 0; d < 4; ++d) {
      const unsigned a = r[2 * d][q], b = r[2 * d + 1][q];
      f[2 * q][d] = __builtin_amdgcn_perm(b, a, 0x05040100u);
      f[2 * q + 1][d] = __builtin_amdgcn_perm(b, a, 0x07060302u);
    }
}

#define STAMP(c, i) do {} while (0)
struct Ctx {
  unsigned char* wsp;
  rsrc_t rs; LAS unsigned char* L;
  int lane, w, b, G, s, blk;
  unsigned spins;
  unsigned kLO1, kHI1, kONES, kNLO1, kNHI1;
  v4u nonce;
  unsigned long long t0;
};
DI unsigned sconst(unsigned v) { unsigned r; asm volatile("s_mov_b32 %0, %1" : "=s"(r) : "i"(v)); return r; }
#define RELANE(c) do { int l_ = (c).lane; asm volatile("" : "+v"(l_)); (c).lane = l_; } while (0)
DI bool spin_ok(Ctx& c) { return ++c.spins < SPIN_CAP; }

DI int p_blk(int h, int e) { return (h >> 2) + 8 * (4 * (h & 3) + e); }

DI bool nonce_eq(v4u a, v4u n) { return ((a.x ^ n.x) | (a.y ^ n.y) | (a.z ^ n.z) | (a.w ^ n.w)) == 0u; }
DI void wait_ready_a(Ctx& c, v4u nonce, int h0, int hstep) {
  const int q = c.lane & 7;
  const int off = OFF_RDY + p_blk(h0 + hstep * (q >> 2), q & 3) * 64;
  for (;;) {
    CBAR();
    const v4u a = ld128(c.rs, off);
    if (__all(nonce_eq(a, nonce)) || !spin_ok(c)) break;
  }
  CBAR();
}
DI void wait_ready_b(Ctx& c, v4u nonce, int which = 1) {
  const int o0 = OFF_RDY + c.lane * 64 + 16 * which;
  for (;;) {
    CBAR();
    const v4u a0 = ld128(c.rs, o0), a1 = ld128(c.rs, o0 + 4096), a2 = ld128(c.rs, o0 + 8192), a3 = ld128(c.rs, o0 + 12288);
    if (__all(nonce_eq(a0, nonce) & nonce_eq(a1, nonce) & nonce_eq(a2, nonce) & nonce_eq(a3, nonce)) || !spin_ok(c)) break;
  }
  CBAR();
}

template <int D>
DI void gate(Ctx& c, int off) {
  if (c.w == 0) {
    if (off >= 0) {
      for (;;) {
        CBAR();
        const unsigned v = __builtin_amdgcn_raw_buffer_load_b32(c.rs, off, 0, AUX_SC1);
        if (__builtin_amdgcn_readfirstlane(v) != SENT || !spin_ok(c)) break;
        __builtin_amdgcn_s_sleep(4);
      }
    }
    if (D > 0) __builtin_amdgcn_s_sleep(D);
  }
  __builtin_amdgcn_s_barrier();
}
#define PC_WAIT 4u
#define D_PC 16
#define D_O2 0
#define D_START 16
#define D_F3 40
#define D_Q4 30
#define D_A1 30
#define D_FA1 45

template <bool RES_WS>
DI unsigned gather33(Ctx& c, int base, const u16* xin) {
  const int co = 128 * c.w + 4 * (c.lane & 31);
  unsigned v[16], res;
  if (!RES_WS) res = *(const GAS unsigned*)((const GAS unsigned char*)xin + co);
  const int o = base + (c.lane >> 5) * 1024 + co;
  for (;;) {
    CBAR();
#pragma unroll
    for (int i = 0; i < 16; ++i) v[i] = ld32(c.rs, o + i * 2048);
    if (RES_WS) res = ld32(c.rs, base + 32 * 1024 + co);
    unsigned m = RES_WS ? res : 0u;
#pragma unroll
    for (int i = 0; i < 16; i += 2) m = umax3(m, v[i], v[i + 1]);
    if (__all(m != SENT) || !spin_ok(c)) break;
  }
  float s0 = 0.f, s1 = 0.f, s2 = 0.f, s3 = 0.f;
#pragma unroll
  for (int i = 0; i < 16; i += 2) { s0 = dot2(v[i], LO1, s0); s1 = dot2(v[i], HI1, s1); s2 = dot2(v[i + 1], LO1, s2); s3 = dot2(v[i + 1], HI1, s3); }
  const unsigned o01 = pack2(x32_sum(s0 + s2), x32_sum(s1 + s3));
  return pack2(dot2(o01, LO1, bf_lo(res)), dot2(o01, HI1, bf_hi(res)));
}

DI void layernorm(Ctx& c, unsigned xp, v4u& a0, v4u& a1) {
  float t1 = dot2(xp, ONES, 0.f), t2 = dot2(xp, xp, 0.f);
  t1 += dppf<0x121>(t1); t2 += dppf<0x121>(t2);
  t1 += dppf<0x122>(t1); t2 += dppf<0x122>(t2);
  t1 += dppf<0x124>(t1); t2 += dppf<0x124>(t2);
  t1 += dppf<0x128>(t1); t2 += dppf<0x128>(t2);
  t1 += dppm<0x142, 0xa>(t1); t2 += dppm<0x142, 0xa>(t2);
  if (c.lane == 31) *(LAS f32x2*)(c.L + L_LN + c.w * 8) = f32x2{t1, t2};
  LBAR();
  f32x4 t[4];
#pragma unroll
  for (int i = 0; i < 4; ++i) t[i] = *(const LAS f32x4*)(c.L + L_LN + i * 16);
  const f32x4 tt = (t[0] + t[1]) + (t[2] + t[3]);
  const float mean = (tt.x + tt.z) * (1.0f / 512.0f);
  const float var = fmaxf((tt.y + tt.w) * (1.0f / 512.0f) - mean * mean, 0.f);
  const unsigned mv = pack2(mean, var);
  const float nmb = -bf_lo(mv);
  const float sd = rbf(__builtin_amdgcn_sqrtf(rbf(bf_hi(mv) + 1e-5f)));
  const float ri = __builtin_amdgcn_rcpf(sd);
  const unsigned d01 = pack2(dot2(xp, LO1, nmb), dot2(xp, HI1, nmb));
  const unsigned n01 = pack2(bf_lo(d01) * ri, bf_hi(d01) * ri);
  *(LAS unsigned*)(c.L + L_FR + c.w * 256 + c.lane * 4) = n01;
  a0 = *(const LAS v4u*)(c.L + L_FR + c.w * 256 + (c.lane >> 4) * 32);
  a1 = *(const LAS v4u*)(c.L + L_FR + c.w * 256 + (c.lane >> 4) * 32 + 16);
}

template <bool HILO>
DI void gemv_ksplit(Ctx& c, v4u a0, v4u a1, const v4u* W) {
  f32x4 acc[8];
#pragma unroll
  for (int t = 0; t < 8; ++t) acc[t] = mfma(a0, W[t], f32x4{0.f, 0.f, 0.f, 0.f});
#pragma unroll
  for (int t = 0; t < 8; ++t) acc[t] = mfma(a1, W[8 + t], acc[t]);
  if (c.lane < 16) {
    f32x4 o0, o1;
#pragma unroll
    for (int t = 0; t < 4; ++t) { o0[t] = HILO ? acc[t].x + acc[t].y : acc[t].x; o1[t] = HILO ? acc[4 + t].x + acc[4 + t].y : acc[4 + t].x; }
    *(LAS f32x4*)(c.L + L_RED + c.w * 512 + c.lane * 32) = o0;
    *(LAS f32x4*)(c.L + L_RED + c.w * 512 + c.lane * 32 + 16) = o1;
  }
}
DI float red_sum8(Ctx& c, int idx) {
  const LAS float* R = (const LAS float*)(c.L + L_RED);
  return ((R[idx] + R[128 + idx]) + (R[256 + idx] + R[384 + idx])) + ((R[512 + idx] + R[640 + idx]) + (R[768 + idx] + R[896 + idx]));
}
DI void reduce128(Ctx& c) {
  LBAR();
  if (c.w < 2) { const int d = c.w * 64 + c.lane; *(LAS __bf16*)(c.L + L_VEC + d * 2) = (__bf16)red_sum8(c, d); }
  LBAR();
}

template <int KB>
DI void gemv_nsplit(Ctx& c, const v4u* W, int off) {
  const int n = c.lane & 15, g = c.lane >> 4;
  f32x4 acc[8];
#pragma unroll
  for (int kb = 0; kb < KB; ++kb) {
    const v4u a = *(const LAS v4u*)(c.L + L_VEC + kb * 128 + (n & 1) * 64 + g * 16);
#pragma unroll
    for (int t = 0; t < 8; ++t) acc[t] = mfma(a, W[8 * kb + t], kb == 0 ? f32x4{0.f, 0.f, 0.f, 0.f} : acc[t]);
  }
  float r[8];
#pragma unroll
  for (int t = 0; t < 8; ++t) r[t] = acc[t].x + dppf<0x108>(acc[t].y);
  if (c.lane < 8) st128(c.rs, off + 128 * c.w + 16 * c.lane, v4u{canon(pack2(r[0], r[1])), canon(pack2(r[2], r[3])), canon(pack2(r[4], r[5])), canon(pack2(r[6], r[7]))});
}

DI float score_round(float s) {
  const float SC = 0.08838834764831845f;
  const unsigned bits = f2u(s);
  const unsigned tr = bits & 0xFFFF0000u;
  const unsigned g2 = pack2(u2f(tr) * SC, u2f(tr + 0x10000u) * SC);
  const float ga = bf_lo(g2);
  const float wgt = fminf(fmaxf((float)(bits & 0xFFFFu) * (1.0f / (65536.0f * 0.6f)) + (0.5f - 0.5f / 0.6f), 0.f), 1.f);
  return ga + wgt * (bf_hi(g2) - ga);
}

struct NoHook { DI void operator()() const {} };
template <class Hook>
DI void attn_phase(Ctx& c, int l, Hook hook) {
  RELANE(c);
  const int tid = c.w * 64 + c.lane, n = c.lane & 15, g = c.lane >> 4;
  v4u kf[16];
  {
    const unsigned kb = L_IMG + (c.w * 16 + n) * 1024 + (((unsigned)g ^ swz(n)) << 4);
#pragma unroll
    for (int ks = 0; ks < 16; ++ks) kf[ks] = *(const LAS v4u*)(c.L + ((kb ^ (64u * (ks & 3))) + 256u * (ks >> 2)));
  }
  unsigned ab;
  {
    const unsigned q4 = (c.lane & 15) >> 2, p4 = c.lane & 3;
    const unsigned key = 4u * g + q4;
    const unsigned chunk = 8u * c.w + (p4 >> 1);
    ab = L_IMG + key * 1024 + ((chunk ^ swz(key)) << 4) + 8u * (p4 & 1);
    asm volatile("" : "+v"(ab));
  }
  {
    const int base = OFF_X1 + ((l * 2 + c.b) * 32 + 16 * c.G) * 1024 + tid * 16;
    v4u v0, v1;
    for (;;) {
      CBAR();
      v0 = ld128(c.rs, base);
      v1 = ld128(c.rs, base + 8192);
      const unsigned m = max(umax3(umax3(v0.x, v0.y, v0.z), v0.w, v1.x), umax3(v1.y, v1.z, v1.w));
      if (__all(m != SENT) || !spin_ok(c)) break;
    }
    STAMP(c, 2 + 8 * l);
    if (l == 0) { while (__builtin_amdgcn_s_memrealtime() - c.t0 < PROBE_T) {} }
    hook();
    *(LAS v4u*)(c.L + L_QA + c.w * 1024 + (((unsigned)c.lane ^ swz(c.w)) << 4)) = v0;
    *(LAS v4u*)(c.L + L_QA + (c.w + 8) * 1024 + (((unsigned)c.lane ^ swz(c.w + 8)) << 4)) = v1;
  }
  LBAR();
  if (l == 0 && c.w == 0 && c.lane == 0) st128(c.rs, OFF_RDY + c.blk * 64 + 16, c.nonce);
  STAMP(c, 26 + l);
  f32x4 sacc = {0.f, 0.f, 0.f, 0.f}, sacc2 = {0.f, 0.f, 0.f, 0.f};
  {
    const unsigned qb = L_QA + n * 1024 + (((unsigned)g ^ swz(n)) << 4);
    constexpr int QD = 4;
    v4u qf[QD];
#pragma unroll
    for (int ks = 0; ks < QD; ++ks) qf[ks] = *(const LAS v4u*)(c.L + ((qb ^ (64u * (ks & 3))) + 256u * (ks >> 2)));
    __builtin_amdgcn_sched_barrier(0);
#pragma unroll
    for (int ks = 0; ks < 16; ++ks) {
      if (ks & 1) sacc2 = mfma(kf[ks], qf[ks % QD], sacc2); else sacc = mfma(kf[ks], qf[ks % QD], sacc);
      if (ks + QD < 16) qf[ks % QD] = *(const LAS v4u*)(c.L + ((qb ^ (64u * ((ks + QD) & 3))) + 256u * ((ks + QD) >> 2)));
      __builtin_amdgcn_sched_barrier(0);
    }
  }
  if (l == 1) { asm volatile("" :: "v"(sacc), "v"(sacc2)); STAMP(c, 17); }
  v2u vf[4][4][2];
  auto vf_issue = [&](int u) __attribute__((always_inline)) {
#pragma unroll
    for (int t = 0; t < 4; ++t) {
      const unsigned ad = (ab ^ (32u * t)) + 32768u * u;
      const s16x4 lo = __builtin_amdgcn_ds_read_tr16_b64_v4i16((LAS s16x4*)(c.L + ad));
      const s16x4 hi = __builtin_amdgcn_ds_read_tr16_b64_v4i16((LAS s16x4*)(c.L + ad + 16384u));
      vf[u][t][0] = __builtin_bit_cast(v2u, lo); vf[u][t][1] = __builtin_bit_cast(v2u, hi);
    }
  };
  vf_issue(0); vf_issue(1);
  __builtin_amdgcn_sched_barrier(0);
  {
    const float LOG2E = 1.4426950408889634f;
    float sv[4];
#pragma unroll
    for (int q = 0; q < 4; ++q) sv[q] = score_round(sacc[q] + sacc2[q]) * LOG2E;
    const float kw = ceilf(xg_max(fmaxf(fmaxf(sv[0], sv[1]), fmaxf(sv[2], sv[3]))));
    float p[4];
#pragma unroll
    for (int q = 0; q < 4; ++q) p[q] = __builtin_amdgcn_exp2f(sv[q] - kw);
    const float lw = xg_sum((p[0] + p[1]) + (p[2] + p[3]));
    *(LAS v2u*)(c.L + L_PB + (c.w >> 1) * 1024 + c.lane * 16 + (c.w & 1) * 8) = v2u{pack2(p[0], p[1]), pack2(p[2], p[3])};
    if (g == 0) *(LAS v2u*)(c.L + L_SB + n * 64 + c.w * 8) = v2u{(unsigned)(int)kw, f2u(lw)};
  }
  if (l == 1) STAMP(c, 18);
  LBAR();
  STAMP(c, 3 + 8 * l);
  f32x4 acc[4];
  {
    v4u sb[4], pr[4];
#pragma unroll
    for (int i = 0; i < 4; ++i) sb[i] = *(const LAS v4u*)(c.L + L_SB + n * 64 + i * 16);
#pragma unroll
    for (int u = 0; u < 4; ++u) pr[u] = *(const LAS v4u*)(c.L + L_PB + u * 1024 + c.lane * 16);
    vf_issue(2); vf_issue(3);
    __builtin_amdgcn_sched_barrier(0);
    int K = max(max(max((int)sb[0].x, (int)sb[0].z), max((int)sb[1].x, (int)sb[1].z)), max(max((int)sb[2].x, (int)sb[2].z), max((int)sb[3].x, (int)sb[3].z)));
    if (c.w == 0) {
      float Ls = 0.f;
#pragma unroll
      for (int i = 0; i < 4; ++i) Ls += ldexpf(u2f(sb[i].y), (int)sb[i].x - K) + ldexpf(u2f(sb[i].w), (int)sb[i].z - K);
      if (g == 0) st64(c.rs, OFF_ST + (((l * 2 + c.b) * 32 + 16 * c.G + n) * 64 + c.s) * 8, v2u{par(f2u((float)K), l), par(f2u(Ls), l)});
    }
#pragma unroll
    for (int t = 0; t < 4; ++t) acc[t] = f32x4{0.f, 0.f, 0.f, 0.f};
#pragma unroll
    for (int u = 0; u < 4; ++u) {
      const unsigned d0 = (unsigned)min(K - (int)sb[u].x, 255) * 0x00800080u, d1 = (unsigned)min(K - (int)sb[u].z, 255) * 0x00800080u;
      const v4u pb = {subsat(pr[u].x, d0), subsat(pr[u].y, d0), subsat(pr[u].z, d1), subsat(pr[u].w, d1)};
#pragma unroll
      for (int t = 0; t < 4; ++t) {
        const v4u af = {vf[u][t][0].x, vf[u][t][0].y, vf[u][t][1].x, vf[u][t][1].y};
        acc[t] = mfma(pb, af, acc[t]);
      }
    }
  }
  if (l == 1) { asm volatile("" :: "v"(acc[0]), "v"(acc[1]), "v"(acc[2]), "v"(acc[3])); STAMP(c, 19); }
  {
    const int base = OFF_X2 + ((l * 2 + c.b) * 32 + 16 * c.G + 4 * g) * 65536 + c.w * 8192 + c.s * 128 + n * 8;
#pragma unroll
    for (int q = 0; q < 4; ++q)
      st64(c.rs, base + q * 65536, v2u{par(pack2(acc[0][q], acc[1][q]), l), par(pack2(acc[2][q], acc[3][q]), l)});
  }
  STAMP(c, 4 + 8 * l);
}

DI void fill_attn(Ctx& c, int wv, int l = 0) {
  const v4u sv = {SENT, SENT, SENT, SENT};
  const int base = OFF_X2 + ((l * 2 + c.b) * 32 + 16 * c.G + (c.lane >> 3)) * 65536 + wv * 8192 + c.s * 128 + (c.lane & 7) * 16;
  st128(c.rs, base, sv);
  st128(c.rs, base + 8 * 65536, sv);
  if (wv == 7 && c.lane < 16) st64(c.rs, OFF_ST + (((l * 2 + c.b) * 32 + 16 * c.G + c.lane) * 64 + c.s) * 8, v2u{SENT, SENT});
}

DI void ckv_issue_h(Ctx& c, const Params& p, v4u (&t)[8], int hf) {
  const GAS unsigned char* src = (const GAS unsigned char*)p.ckv + ((size_t)c.b * 8192 + c.s * 128 + 64 * hf + c.w * 8) * 1024 + c.lane * 16;
#pragma unroll
  for (int i = 0; i < 8; ++i) t[i] = *(const GAS v4u*)(src + i * 1024);
}
DI void ckv_stage_h(Ctx& c, const v4u (&t)[8], int hf) {
#pragma unroll
  for (int i = 0; i < 8; ++i)
    *(LAS v4u*)(c.L + L_IMG + (64 * hf + c.w * 8 + i) * 1024 + (((unsigned)c.lane ^ swz((c.w * 8 + i) & 15)) << 4)) = t[i];
}
template <int NR>
DI void ckv_dma(Ctx& c, const Params& p, int row0) {
  const GAS unsigned char* src = (const GAS unsigned char*)p.ckv + ((size_t)c.b * 8192 + c.s * 128 + row0) * 1024;
#pragma unroll
  for (int i = 0; i < NR; ++i)
    __builtin_amdgcn_global_load_lds((const GAS unsigned*)(src + i * 1024 + (((unsigned)c.lane ^ swz(i & 15)) << 4)),
                                     (LAS unsigned*)(c.L + L_IMG + (row0 + i) * 1024), 16, 0, 0);
}
DI void ckv_issue(Ctx& c, const Params& p, v4u (&t)[16]) {
  const GAS unsigned char* src = (const GAS unsigned char*)p.ckv + ((size_t)c.b * 8192 + c.s * 128 + c.w * 16) * 1024 + c.lane * 16;
#pragma unroll
  for (int i = 0; i < 16; ++i) t[i] = *(const GAS v4u*)(src + i * 1024);
}
DI void ckv_stage(Ctx& c, const v4u (&t)[16]) {
#pragma unroll
  for (int i = 0; i < 16; ++i)
    *(LAS v4u*)(c.L + L_IMG + (c.w * 16 + i) * 1024 + (((unsigned)c.lane ^ swz(i)) << 4)) = t[i];
}
DI void publish_ready_b(Ctx& c, v4u nonce, int which = 1) {
  DRAIN();
  LBAR();
  if (c.w == 0 && c.lane == 0) st128(c.rs, OFF_RDY + c.blk * 64 + 16 * which, nonce);
}

DI unsigned ln_apply(Ctx& c, unsigned xp, f32x4 tt) {
  const float mean = (tt.x + tt.z) * (1.0f / 512.0f);
  const float var = fmaxf((tt.y + tt.w) * (1.0f / 512.0f) - mean * mean, 0.f);
  const unsigned mv = pack2(mean, var);
  const float nmb = -bf_lo(mv);
  const float sd = rbf(__builtin_amdgcn_sqrtf(rbf(bf_hi(mv) + 1e-5f)));
  const float ri = __builtin_amdgcn_rcpf(sd);
  const unsigned d01 = pack2(dot2(xp, LO1, nmb), dot2(xp, HI1, nmb));
  return pack2(bf_lo(d01) * ri, bf_hi(d01) * ri);
}
DI void prologue(Ctx& c, const Params& p, v4u nonce, int h, int e) {
  asm volatile("; prologue" ::: "memory");
  const int n = c.lane & 15, g = c.lane >> 4;
  const v4u sv = {SENT, SENT, SENT, SENT};
  volatile LAS unsigned* cnt = (volatile LAS unsigned*)(c.L + L_CNT);
  if (c.w == 0 && c.lane < 2) cnt[c.lane] = 0u;
  LBAR();
  if (c.w < 4) {
    v4u ru[8];
    f32x4 acc[8];
    const GAS unsigned char* wu = (const GAS unsigned char*)p.wuk + ((size_t)(128 * e + 32 * c.w + g) * 4096 + h * 128 + 8 * n) * 2;
    if (c.w < 2) {
      if (c.w == 0) {
        st128(c.rs, OFF_XQ + h * 8192 + e * 2048 + c.lane * 16, sv);
        st128(c.rs, OFF_XQ + h * 8192 + e * 2048 + 1024 + c.lane * 16, sv);
        if (c.lane < 32) st128(c.rs, OFF_X1 + ((c.lane >> 4) * 32 + h) * 1024 + e * 256 + (c.lane & 15) * 16, sv);
      }
      const v4u xa = *(const GAS v4u*)((const GAS unsigned char*)p.x + c.lane * 16);
      const v4u xb = *(const GAS v4u*)((const GAS unsigned char*)p.x + 1024 + c.lane * 16);
      const int k0 = 128 * e + 64 * c.w + 16 * g;
      const v4u xk0 = *(const GAS v4u*)((const GAS unsigned char*)p.x + (n & 1) * 1024 + k0 * 2);
      const v4u xk1 = *(const GAS v4u*)((const GAS unsigned char*)p.x + (n & 1) * 1024 + k0 * 2 + 16);
      v4u rq[2][8];
      const GAS unsigned char* wq = (const GAS unsigned char*)p.wq + ((size_t)k0 * 4096 + h * 128 + 8 * n) * 2;
#pragma unroll
      for (int ks = 0; ks < 2; ++ks)
#pragma unroll
        for (int j = 0; j < 8; ++j) rq[ks][j] = *(const GAS v4u*)(wq + (size_t)(8 * ks + j) * 8192);
#pragma unroll
      for (int i = 0; i < 8; ++i) ru[i] = *(const GAS v4u*)(wu + (size_t)(4 * i) * 8192);
      __builtin_amdgcn_sched_barrier(0);
      float s1a = 0.f, s2a = 0.f, s1b = 0.f, s2b = 0.f;
#pragma unroll
      for (int q = 0; q < 4; ++q) { s1a = dot2(xa[q], ONES, s1a); s2a = dot2(xa[q], xa[q], s2a); s1b = dot2(xb[q], ONES, s1b); s2b = dot2(xb[q], xb[q], s2b); }
      if (c.w == 0 && c.lane == 0) { st128(c.rs, OFF_RDY + c.blk * 64, nonce); cnt[0] = 1u; }
      STAMP(c, 9);
      s1a += dppf<0x121>(s1a); s2a += dppf<0x121>(s2a); s1b += dppf<0x121>(s1b); s2b += dppf<0x121>(s2b);
      s1a += dppf<0x122>(s1a); s2a += dppf<0x122>(s2a); s1b += dppf<0x122>(s1b); s2b += dppf<0x122>(s2b);
      s1a += dppf<0x124>(s1a); s2a += dppf<0x124>(s2a); s1b += dppf<0x124>(s1b); s2b += dppf<0x124>(s2b);
      s1a += dppf<0x128>(s1a); s2a += dppf<0x128>(s2a); s1b += dppf<0x128>(s1b); s2b += dppf<0x128>(s2b);
      const float s1 = xg_sum((n & 1) ? s1b : s1a), s2 = xg_sum((n & 1) ? s2b : s2a);
      const f32x4 tt = {s1, s2, 0.f, 0.f};
      v4u a0, a1;
#pragma unroll
      for (int q = 0; q < 4; ++q) { a0[q] = ln_apply(c, xk0[q], tt); a1[q] = ln_apply(c, xk1[q], tt); }
      v4u W[16];
      tr8x8(rq[0], W); tr8x8(rq[1], W + 8);
#pragma unroll
      for (int t8 = 0; t8 < 8; ++t8) acc[t8] = mfma(a0, W[t8], f32x4{0.f, 0.f, 0.f, 0.f});
#pragma unroll
      for (int t8 = 0; t8 < 8; ++t8) acc[t8] = mfma(a1, W[8 + t8], acc[t8]);
    } else {
#pragma unroll
      for (int i = 0; i < 8; ++i) ru[i] = *(const GAS v4u*)(wu + (size_t)(4 * i) * 8192);
    }
    while (cnt[0] == 0u && spin_ok(c)) {}
    if (c.w < 2 && c.lane < 16) {
      const int o = OFF_XQ + h * 8192 + (2 * e + c.w) * 1024 + c.lane * 32;
      st128(c.rs, o, v4u{canon(f2u(acc[0].x)), canon(f2u(acc[1].x)), canon(f2u(acc[2].x)), canon(f2u(acc[3].x))});
      st128(c.rs, o + 16, v4u{canon(f2u(acc[4].x)), canon(f2u(acc[5].x)), canon(f2u(acc[6].x)), canon(f2u(acc[7].x))});
      st128(c.rs, o + 512, v4u{canon(f2u(acc[0].y)), canon(f2u(acc[1].y)), canon(f2u(acc[2].y)), canon(f2u(acc[3].y))});
      st128(c.rs, o + 528, v4u{canon(f2u(acc[4].y)), canon(f2u(acc[5].y)), canon(f2u(acc[6].y)), canon(f2u(acc[7].y))});
    }
    STAMP(c, 7);
    {
      const unsigned ib = L_QA + c.w * 4096;
#pragma unroll
      for (int hf = 0; hf < 2; ++hf) {
#pragma unroll
        for (int i = 0; i < 4; ++i) { const unsigned row = 4 * i + g; *(LAS v4u*)(c.L + ib + row * 256 + (((unsigned)n ^ swz(row)) << 4)) = ru[4 * hf + i]; }
        v4u tmp[4];
#pragma unroll
        for (int ks = 0; ks < 4; ++ks) tmp[ks] = *(const LAS v4u*)(c.L + ib + n * 256 + (((unsigned)(4 * ks + g) ^ swz(n)) << 4));
#pragma unroll
        for (int ks = 0; ks < 4; ++ks) ru[4 * hf + ks] = tmp[ks];
      }
    }
    wait_ready_a(c, nonce, h, 0);
    {
      const int o = OFF_XQ + h * 8192 + (c.w * 64 + c.lane) * 4;
      unsigned v[8];
      for (;;) {
        CBAR();
#pragma unroll
        for (int i = 0; i < 8; ++i) v[i] = ld32(c.rs, o + i * 1024);
        const unsigned m = max(umax3(umax3(v[0], v[1], v[2]), v[3], v[4]), umax3(v[5], v[6], v[7]));
        if (__all(m != SENT) || !spin_ok(c)) break;
      }
      const float sum = ((u2f(v[0]) + u2f(v[1])) + (u2f(v[2]) + u2f(v[3]))) + ((u2f(v[4]) + u2f(v[5])) + (u2f(v[6]) + u2f(v[7])));
      *(LAS __bf16*)(c.L + L_VEC + (c.w * 64 + c.lane) * 2) = (__bf16)sum;
      STAMP(c, 22);
    }
    asm volatile("s_waitcnt lgkmcnt(0)" ::: "memory");
    if (c.lane == 0) __hip_atomic_fetch_add((LAS unsigned*)(c.L + L_CNT + 4), 1u, __ATOMIC_RELAXED, __HIP_MEMORY_SCOPE_WORKGROUP);
    while (cnt[1] < 4u && spin_ok(c)) {}
    CBAR();
    f32x4 acc0 = {0.f, 0.f, 0.f, 0.f}, acc1 = {0.f, 0.f, 0.f, 0.f};
#pragma unroll
    for (int ks = 0; ks < 4; ++ks) {
      const v4u a = *(const LAS v4u*)(c.L + L_VEC + (n & 1) * 256 + ks * 64 + g * 16);
      acc0 = mfma(a, ru[ks], acc0);
      acc1 = mfma(a, ru[4 + ks], acc1);
    }
    const float n00 = dppf<0x101>(acc0.x), n01 = dppf<0x101>(acc0.y), n10 = dppf<0x101>(acc1.x), n11 = dppf<0x101>(acc1.y);
    if (c.lane < 16 && (c.lane & 1) == 0) {
      const int o = OFF_X1 + h * 1024 + (128 * e + 32 * c.w + n) * 2;
      st32(c.rs, o, canon(pack2(acc0.x, n00)));
      st32(c.rs, o + 32, canon(pack2(acc1.x, n10)));
      st32(c.rs, o + 32 * 1024, canon(pack2(acc0.y, n01)));
      st32(c.rs, o + 32 * 1024 + 32, canon(pack2(acc1.y, n11)));
    }
    STAMP(c, 21);
  } else {
    while (cnt[0] == 0u && spin_ok(c)) { __builtin_amdgcn_s_sleep(2); }
    __builtin_amdgcn_s_sleep(D_PC);
    fill_attn(c, 2 * (c.w - 4)); fill_attn(c, 2 * (c.w - 4) + 1);
    wait_ready_a(c, nonce, 16 * c.G + c.w, 8);
    while (cnt[1] < PC_WAIT && spin_ok(c)) { __builtin_amdgcn_s_sleep(2); }
    ckv_dma<32>(c, p, (c.w - 4) * 32);
    return;
  }
  wait_ready_a(c, nonce, 16 * c.G + c.w, 8);
}

DI void role_q(Ctx& c, const Params& p, v4u nonce, int h, int ch) {
  asm volatile("; role Q" ::: "memory");
  const bool xpub = (h == 0 && ch == 0);
  DRAIN();
  LBAR();
  STAMP(c, 20);
#pragma unroll 1
  for (int l = 0; l < 2; ++l) {
    attn_phase(c, l, NoHook());
    if (l == 0) {
      const int lane = c.lane, n = lane & 15, g = lane >> 4, hb = lane >> 5;
      v4u Wq[16], ru[8];
      {
        v4u rq[2][8];
        const GAS unsigned char* wq = (const GAS unsigned char*)p.wq + ((size_t)(64 * c.w + 16 * g) * 4096 + h * 128 + 8 * n) * 2;
#pragma unroll
        for (int ks = 0; ks < 2; ++ks)
#pragma unroll
          for (int j = 0; j < 8; ++j) rq[ks][j] = *(const GAS v4u*)(wq + (size_t)(8 * ks + j) * 8192);
        const GAS unsigned char* wu = (const GAS unsigned char*)p.wuk + ((size_t)(256 * ch + 32 * c.w + 2 * n) * 4096 + h * 128 + 8 * g) * 2;
#pragma unroll
        for (int t = 0; t < 2; ++t)
#pragma unroll
          for (int ks = 0; ks < 4; ++ks) ru[t * 4 + ks] = *(const GAS v4u*)(wu + (size_t)t * 8192 + ks * 64);
        tr8x8(rq[0], Wq); tr8x8(rq[1], Wq + 8);
      }
      asm volatile("" :: "v"(ru[0]), "v"(ru[1]), "v"(ru[2]), "v"(ru[3]), "v"(ru[4]), "v"(ru[5]), "v"(ru[6]), "v"(ru[7]));
      fill_attn(c, c.w, 1);
      publish_ready_b(c, nonce, 2);
      wait_ready_b(c, nonce);
      gate<D_Q4>(c, OFF_X3 + (0 * 2 + (h & 1)) * XV_STR + h * 1024);
      unsigned xp;
      {
        const int o = OFF_X4 + (0 * 2 + hb) * XV_STR + 128 * c.w + 4 * (lane & 31);
        unsigned v[33];
        for (;;) {
          CBAR();
#pragma unroll
          for (int i = 0; i < 33; ++i) v[i] = ld32(c.rs, o + i * 1024);
          unsigned m = v[32];
#pragma unroll
          for (int i = 0; i < 32; i += 2) m = umax3(m, v[i], v[i + 1]);
          if (__all(m != SENT) || !spin_ok(c)) break;
        }
        float s0 = 0.f, s1 = 0.f, s2 = 0.f, s3 = 0.f;
#pragma unroll
        for (int i = 0; i < 32; i += 2) { s0 = dot2(v[i], LO1, s0); s1 = dot2(v[i], HI1, s1); s2 = dot2(v[i + 1], LO1, s2); s3 = dot2(v[i + 1], HI1, s3); }
        const unsigned o01 = pack2(s0 + s2, s1 + s3);
        xp = pack2(dot2(o01, LO1, bf_lo(v[32])), dot2(o01, HI1, bf_hi(v[32])));
        STAMP(c, 5);
        if (xpub) st32(c.rs, OFF_X3 + (1 * 2 + hb) * XV_STR + 32 * 1024 + 128 * c.w + 4 * (lane & 31), canon(xp));
      }
      float t1 = dot2(xp, ONES, 0.f), t2 = dot2(xp, xp, 0.f);
      t1 += dppf<0x121>(t1); t2 += dppf<0x121>(t2);
      t1 += dppf<0x122>(t1); t2 += dppf<0x122>(t2);
      t1 += dppf<0x124>(t1); t2 += dppf<0x124>(t2);
      t1 += dppf<0x128>(t1); t2 += dppf<0x128>(t2);
      t1 += dppm<0x142, 0xa>(t1); t2 += dppm<0x142, 0xa>(t2);
      if ((lane & 31) == 31) *(LAS f32x2*)(c.L + L_LN + hb * 64 + c.w * 8) = f32x2{t1, t2};
      LBAR();
      f32x4 t[4];
#pragma unroll
      for (int i = 0; i < 4; ++i) t[i] = *(const LAS f32x4*)(c.L + L_LN + hb * 64 + i * 16);
      const unsigned n01 = ln_apply(c, xp, (t[0] + t[1]) + (t[2] + t[3]));
      *(LAS unsigned*)(c.L + L_FR + c.w * 256 + lane * 4) = n01;
      const v4u a0 = *(const LAS v4u*)(c.L + L_FR + c.w * 256 + (n & 1) * 128 + g * 32);
      const v4u a1 = *(const LAS v4u*)(c.L + L_FR + c.w * 256 + (n & 1) * 128 + g * 32 + 16);
      f32x4 acc[8];
#pragma unroll
      for (int t8 = 0; t8 < 8; ++t8) acc[t8] = mfma(a0, Wq[t8], f32x4{0.f, 0.f, 0.f, 0.f});
#pragma unroll
      for (int t8 = 0; t8 < 8; ++t8) acc[t8] = mfma(a1, Wq[8 + t8], acc[t8]);
      if (lane < 16) {
        *(LAS f32x4*)(c.L + L_RED + c.w * 1024 + lane * 32) = f32x4{acc[0].x, acc[1].x, acc[2].x, acc[3].x};
        *(LAS f32x4*)(c.L + L_RED + c.w * 1024 + lane * 32 + 16) = f32x4{acc[4].x, acc[5].x, acc[6].x, acc[7].x};
        *(LAS f32x4*)(c.L + L_RED + c.w * 1024 + 512 + lane * 32) = f32x4{acc[0].y, acc[1].y, acc[2].y, acc[3].y};
        *(LAS f32x4*)(c.L + L_RED + c.w * 1024 + 512 + lane * 32 + 16) = f32x4{acc[4].y, acc[5].y, acc[6].y, acc[7].y};
      }
      LBAR();
      if (c.w < 4) {
        const int d = c.w * 64 + lane;
        const LAS float* R = (const LAS float*)(c.L + L_RED);
        const float sum = ((R[d] + R[256 + d]) + (R[512 + d] + R[768 + d])) + ((R[1024 + d] + R[1280 + d]) + (R[1536 + d] + R[1792 + d]));
        *(LAS __bf16*)(c.L + L_VEC + d * 2) = (__bf16)sum;
      }
      LBAR();
      f32x4 acc0 = {0.f, 0.f, 0.f, 0.f}, acc1 = {0.f, 0.f, 0.f, 0.f};
#pragma unroll
      for (int ks = 0; ks < 4; ++ks) {
        const v4u a = *(const LAS v4u*)(c.L + L_VEC + (n & 1) * 256 + ks * 64 + g * 16);
        acc0 = mfma(a, ru[ks], acc0);
        acc1 = mfma(a, ru[4 + ks], acc1);
      }
      if (lane < 16) {
        const int o = OFF_X1 + (1 * 2 * 32 + h) * 1024 + (256 * ch + 32 * c.w + 2 * n) * 2;
        st32(c.rs, o, canon(pack2(acc0.x, acc1.x)));
        st32(c.rs, o + 32 * 1024, canon(pack2(acc0.y, acc1.y)));
      }
      STAMP(c, 6);
    }
  }
  gate<D_Q4>(c, OFF_X3 + (1 * 2 + (h & 1)) * XV_STR + h * 1024);
  if (c.w < 2) {
    const int qi = 2 * h + ch;
    const int off = OFF_X4 + (1 * 2 + c.w) * XV_STR + (c.lane < 33 ? c.lane : 32) * 1024 + qi * 16;
    v4u v;
    for (;;) {
      CBAR();
      v = ld128(c.rs, off);
      if (__all(max(umax3(v.x, v.y, v.z), v.w) != SENT) || !spin_ok(c)) break;
    }
    const unsigned wl = c.lane < 32 ? LO1 : 0u, wh = c.lane < 32 ? HI1 : 0u;
    v4u o;
#pragma unroll
    for (int q = 0; q < 4; ++q) {
      float lo = dot2(v[q], wl, 0.f), hi = dot2(v[q], wh, 0.f);
      lo = quad_sum(lo); hi = quad_sum(hi);
      lo += dppf<0x124>(lo); hi += dppf<0x124>(hi);
      lo += dppf<0x128>(lo); hi += dppf<0x128>(hi);
      lo += dppm<0x142, 0xa>(lo); hi += dppm<0x142, 0xa>(hi);
      const unsigned s2 = (unsigned)__builtin_amdgcn_readlane((int)pack2(lo, hi), 31);
      const unsigned r2 = (unsigned)__builtin_amdgcn_readlane((int)v[q], 32);
      o[q] = pack2(bf_lo(r2) + bf_lo(s2), bf_hi(r2) + bf_hi(s2));
    }
    if (c.lane == 0) *(GAS v4u*)((GAS unsigned char*)p.out + c.w * 1024 + qi * 16) = o;
    STAMP(c, 16);
  }
  __builtin_amdgcn_s_barrier();
  if (c.w == 0 && c.lane < 16) st128(c.rs, OFF_RDY + (c.blk + 64 * (c.lane >> 2)) * 64 + (c.lane & 3) * 16, v4u{~nonce.x, ~nonce.y, ~nonce.z, ~nonce.w});
}

DI void role_a(Ctx& c, const Params& p, v4u nonce) {
  asm volatile("; role A" ::: "memory");
  DRAIN();
  LBAR();
  STAMP(c, 20);
#pragma unroll 1
  for (int l = 0; l < 2; ++l) {
    attn_phase(c, l, NoHook());
    if (l == 0) { __builtin_amdgcn_s_sleep(100); fill_attn(c, c.w, 1); publish_ready_b(c, nonce, 2); wait_ready_b(c, nonce); gate<D_A1>(c, OFF_X4 + (0 * 2 + c.b) * XV_STR + (c.s & 31) * 1024); }
  }
}

DI void role_o(Ctx& c, const Params& p, v4u nonce, int h) {
  asm volatile("; role O" ::: "memory");
  const int n = c.lane & 15, g = c.lane >> 4;
  const v4u sv = {SENT, SENT, SENT, SENT};
  if (D_START > 0) __builtin_amdgcn_s_sleep(D_START);
  ckv_dma<16>(c, p, c.w * 16);
  fill_attn(c, c.w);
  if (c.w < 2) st128(c.rs, OFF_X3 + (c.w * 2 + c.b) * XV_STR + h * 1024 + c.lane * 16, sv);
  DRAIN();
  __builtin_amdgcn_sched_barrier(0);
  v4u Wv[16], Wo[16];
  {
    v4u rv[2][8];
    const GAS unsigned char* wv = (const GAS unsigned char*)p.wuv + ((size_t)(64 * c.w + 2 * g) * 4096 + h * 128 + 8 * n) * 2;
#pragma unroll
    for (int ks = 0; ks < 2; ++ks)
#pragma unroll
      for (int j = 0; j < 8; ++j) rv[ks][j] = *(const GAS v4u*)(wv + (size_t)(16 * (j & 3) + 8 * ks + (j >> 2)) * 8192);
    tr8x8(rv[0], Wv); tr8x8(rv[1], Wv + 8);
  }
  wait_ready_a(c, nonce, 16 * c.G + c.w, 8);
  DRAIN();
  LBAR();
  STAMP(c, 20);
  gate<0>(c, OFF_X1 + (c.b * 32 + 16 * c.G + (c.s & 15)) * 1024 + (c.s >> 4) * 256);
  auto o_phase = [&](int l) __attribute__((always_inline)) {
    RELANE(c);
    const int n = c.lane & 15, g = c.lane >> 4;
    float inv;
    {
      const int bs = OFF_ST + ((l * 2 + c.b) * 32 + h) * 512 + c.lane * 8;
      v2u st;
      for (;;) {
        CBAR();
        st = ld64(c.rs, bs);
        const unsigned bad = st.x | st.y;
        if (__all((bad & 1u) == 0u) || !spin_ok(c)) break;
      }
      const float kf = u2f(st.x & ~1u);
      float K = kf;
      K = fmaxf(K, dppf<0x128>(K)); K = fmaxf(K, dppf<0x124>(K)); K = fmaxf(K, dppf<0x122>(K)); K = fmaxf(K, dppf<0x121>(K));
      K = xg_max(K);
      const int d = (int)(K - kf);
      const unsigned eb = d < 127 ? (unsigned)(127 - d) << 7 : 0u;
      float Ls = u2f(st.y) * u2f(eb << 16);
      Ls += dppf<0x128>(Ls); Ls += dppf<0x124>(Ls); Ls += dppf<0x122>(Ls); Ls += dppf<0x121>(Ls);
      Ls = xg_sum(Ls);
      inv = 1.0f / Ls;
      *(LAS unsigned*)(c.L + L_EW + c.w * 256 + c.lane * 4) = eb;
    }
    unsigned el[16];
#pragma unroll
    for (int i = 0; i < 4; ++i) {
      const v4u e4 = *(const LAS v4u*)(c.L + L_EW + c.w * 256 + g * 64 + i * 16);
      el[4 * i] = e4.x; el[4 * i + 1] = e4.y; el[4 * i + 2] = e4.z; el[4 * i + 3] = e4.w;
    }
    const int bx = OFF_X2 + ((l * 2 + c.b) * 32 + h) * 65536 + c.w * 8192 + g * 2048 + n * 8;
    if (D_O2 > 0) __builtin_amdgcn_s_sleep(D_O2);
    v2u v[16];
    for (;;) {
      CBAR();
#pragma unroll
      for (int i = 0; i < 16; ++i) v[i] = ld64(c.rs, bx + i * 128);
      unsigned m = 0u;
#pragma unroll
      for (int i = 0; i < 16; ++i) m = m | v[i].x | v[i].y;
      if (__all((m & 1u) == 0u) || !spin_ok(c)) break;
    }
    STAMP(c, 5 + 8 * l);
    float s0 = 0.f, s1 = 0.f, s2 = 0.f, s3 = 0.f;
#pragma unroll
    for (int i = 0; i < 16; ++i) {
      const unsigned eh = el[i] << 16;
      s0 = dot2(v[i].x, el[i], s0); s1 = dot2(v[i].x, eh, s1); s2 = dot2(v[i].y, el[i], s2); s3 = dot2(v[i].y, eh, s3);
    }
    s0 = xg_sum(s0) * inv; s1 = xg_sum(s1) * inv; s2 = xg_sum(s2) * inv; s3 = xg_sum(s3) * inv;
    const unsigned h01 = pack2(s0, s1), h23 = pack2(s2, s3);
    const unsigned l01 = pack2(dot2(h01, NLO1, s0), dot2(h01, NHI1, s1)), l23 = pack2(dot2(h23, NLO1, s2), dot2(h23, NHI1, s3));
    *(LAS v2u*)(c.L + L_FR + c.w * 256 + (g & 1) * 128 + n * 8) = (g & 1) ? v2u{l01, l23} : v2u{h01, h23};
    const v4u a0 = *(const LAS v4u*)(c.L + L_FR + c.w * 256 + (n & 1) * 128 + g * 16);
    const v4u a1 = *(const LAS v4u*)(c.L + L_FR + c.w * 256 + (n & 1) * 128 + 64 + g * 16);
    asm volatile("" :: "v"(a0), "v"(a1));
    STAMP(c, 7 + 8 * l);
    gemv_ksplit<true>(c, a0, a1, Wv);
    STAMP(c, 22 + 2 * l);
    reduce128(c);
    STAMP(c, 23 + 2 * l);
    gemv_nsplit<2>(c, Wo, OFF_X3 + (l * 2 + c.b) * XV_STR + h * 1024);
    STAMP(c, 6 + 8 * l);
  };
  {
    v4u ro[2][8];
    attn_phase(c, 0, [&]() __attribute__((always_inline)) {
      const GAS unsigned char* wo = (const GAS unsigned char*)p.wo + ((size_t)(h * 128 + 32 * (n >> 3) + 8 * g) * 512 + 64 * c.w + 8 * (n & 7)) * 2;
#pragma unroll
      for (int kb = 0; kb < 2; ++kb)
#pragma unroll
        for (int j = 0; j < 8; ++j) ro[kb][j] = *(const GAS v4u*)(wo + (size_t)(64 * kb + j) * 1024);
      __builtin_amdgcn_sched_barrier(0);
    });
    tr8x8(ro[0], Wo); tr8x8(ro[1], Wo + 8);
  }
  wait_ready_b(c, nonce);
  o_phase(0);
  fill_attn(c, c.w, 1);
  publish_ready_b(c, nonce, 2);
  gate<0>(c, OFF_X4 + (0 * 2 + c.b) * XV_STR + h * 1024);
  wait_ready_b(c, nonce, 2);
  gate<D_A1 - 16>(c, -1);
  attn_phase(c, 1, NoHook());
  o_phase(1);
}

DI void role_f(Ctx& c, const Params& p, v4u nonce, int j) {
  asm volatile("; role F" ::: "memory");
  const int n = c.lane & 15, g = c.lane >> 4;
  const v4u sv = {SENT, SENT, SENT, SENT};
  if (D_START > 0) __builtin_amdgcn_s_sleep(D_START);
  ckv_dma<16>(c, p, c.w * 16);
  fill_attn(c, c.w);
  if (c.w < 2) st128(c.rs, OFF_X4 + (c.w * 2 + c.b) * XV_STR + j * 1024 + c.lane * 16, sv);
  if (j == 0 && (c.w == 2 || c.w == 3)) st128(c.rs, OFF_X4 + ((c.w - 2) * 2 + c.b) * XV_STR + 32 * 1024 + c.lane * 16, sv);
  v4u W13[16], W2[8];
  wait_ready_a(c, nonce, 16 * c.G + c.w, 8);
  DRAIN();
  LBAR();
  STAMP(c, 20);
  gate<0>(c, OFF_X1 + (c.b * 32 + 16 * c.G + (c.s & 15)) * 1024 + (c.s >> 4) * 256);
  auto f_phase = [&](int l) __attribute__((always_inline)) {
    RELANE(c);
    unsigned xp;
    if (l == 0) { xp = gather33<false>(c, OFF_X3 + (0 * 2 + c.b) * XV_STR, p.x + c.b * 512); }
    else xp = gather33<true>(c, OFF_X3 + (1 * 2 + c.b) * XV_STR, nullptr);
    STAMP(c, 5 + 8 * l);
    if (j == 0 && c.lane < 32) st32(c.rs, OFF_X4 + (l * 2 + c.b) * XV_STR + 32 * 1024 + 128 * c.w + 4 * c.lane, canon(xp));
    v4u a0, a1;
    layernorm(c, xp, a0, a1);
    STAMP(c, 7 + 8 * l);
    gemv_ksplit<false>(c, a0, a1, W13);
    STAMP(c, 22 + 2 * l);
    LBAR();
    STAMP(c, 8 + 8 * l);
    if (c.w == 0) {
      const int li = c.lane < 56 ? c.lane : 55;
      const unsigned gu = pack2(red_sum8(c, li), red_sum8(c, 56 + li));
      const float gt = bf_lo(gu);
      const float sg = rbf(gt * __builtin_amdgcn_rcpf(1.0f + __expf(-gt)));
      const float hv = c.lane < 56 ? sg * bf_hi(gu) : 0.f;
      *(LAS __bf16*)(c.L + L_VEC + c.lane * 2) = (__bf16)hv;
    }
    LBAR();
    STAMP(c, 23 + 2 * l);
    gemv_nsplit<1>(c, W2, OFF_X4 + (l * 2 + c.b) * XV_STR + j * 1024);
    STAMP(c, 6 + 8 * l);
  };
  attn_phase(c, 0, NoHook());
  {
  __builtin_amdgcn_sched_barrier(0);
      v4u r13[2][8], r2[8];
      {
        const int nn = n < 14 ? n : 13;
        const u16* wsel = nn < 7 ? p.w1 : p.w3;
        const int col = 56 * j + 8 * (nn < 7 ? nn : nn - 7);
        const GAS unsigned char* wa = (const GAS unsigned char*)wsel + ((size_t)(64 * c.w + 16 * g) * 1792 + col) * 2;
#pragma unroll
        for (int ks = 0; ks < 2; ++ks)
#pragma unroll
          for (int jj = 0; jj < 8; ++jj) r13[ks][jj] = *(const GAS v4u*)(wa + (size_t)(8 * ks + jj) * 3584);
#pragma unroll
        for (int jj = 0; jj < 8; ++jj) {
          int f = 32 * (n >> 3) + 8 * g + jj; f = f < 56 ? f : 55;
          r2[jj] = *(const GAS v4u*)((const GAS unsigned char*)p.w2 + ((size_t)(56 * j + f) * 512 + 64 * c.w + 8 * (n & 7)) * 2);
        }
      }
      __builtin_amdgcn_sched_barrier(0);
      tr8x8(r13[0], W13); tr8x8(r13[1], W13 + 8);
      tr8x8(r2, W2);
      fill_attn(c, c.w, 1);
      publish_ready_b(c, nonce, 2);
      wait_ready_b(c, nonce);
  }
  f_phase(0);
  gate<D_FA1>(c, -1);
  attn_phase(c, 1, NoHook());
  gate<D_F3>(c, -1);
  f_phase(1);
}

__global__ void __launch_bounds__(NT, 2) mega(Params p) {
  extern __shared__ __attribute__((aligned(16))) unsigned char smem[];
  Ctx c;
  c.rs = __builtin_amdgcn_make_buffer_rsrc((void*)p.ws, 0, WS_BYTES, 0x00020000);
  c.L = (LAS unsigned char*)smem;
  c.lane = threadIdx.x & 63;
  c.w = __builtin_amdgcn_readfirstlane(threadIdx.x >> 6);
  c.blk = blockIdx.x;
  const int x = c.blk & 7, slot = c.blk >> 3;
  c.b = (slot >> 3) & 1; c.G = slot >> 4; c.s = 8 * x + (slot & 7);
  const int r = 8 * c.G + (slot & 7);
  c.spins = 0; c.wsp = p.ws;
  c.kLO1 = sconst(0x00003F80u); c.kHI1 = sconst(0x3F800000u); c.kONES = sconst(0x3F803F80u); c.kNLO1 = sconst(0x0000BF80u); c.kNHI1 = sconst(0xBF800000u);
  STAMP(c, 0);
  const unsigned long long id = llvm_dispatch_id();
  const unsigned long long dp = (unsigned long long)__builtin_amdgcn_dispatch_ptr();
  const v4u nonce = {(unsigned)id ^ 0x5bd1e995u, (unsigned)(id >> 32) ^ 0x9e3779b9u, (unsigned)dp ^ 0x85ebca6bu, (unsigned)(dp >> 32) ^ 0xc2b2ae35u};
  c.nonce = nonce; c.t0 = __builtin_amdgcn_s_memrealtime();
  if (slot < 8) {
    const v4u sv = {SENT, SENT, SENT, SENT};
    const int h = 4 * x + (r >> 1), ch = r & 1;
    if (c.w == 7) st128(c.rs, OFF_X1 + ((1 * 2 + (c.lane >> 5)) * 32 + h) * 1024 + ch * 512 + (c.lane & 31) * 16, sv);
    if (h == 0 && ch == 0 && c.w >= 6) st128(c.rs, OFF_X3 + (1 * 2 + (c.w - 6)) * XV_STR + 32 * 1024 + c.lane * 16, sv);
  }
  if (slot < 16) prologue(c, p, nonce, 4 * x + (slot >> 2), slot & 3);
  if (r < 8) { if (c.b == 0) role_q(c, p, nonce, 4 * x + (r >> 1), r & 1); else role_a(c, p, nonce); }
  else if (r < 12) role_o(c, p, nonce, 4 * x + (r - 8));
  else role_f(c, p, nonce, 4 * x + (r - 12));
}
}

extern "C" void kernel_launch(void* const* d_in, const int* in_sizes, int n_in,
                              void* d_out, int out_size, void* d_ws, size_t ws_size,
                              hipStream_t stream) {
  mk::Params p;
  p.x = (const u16*)d_in[0]; p.ckv = (const u16*)d_in[1]; p.wq = (const u16*)d_in[2]; p.wuk = (const u16*)d_in[3];
  p.wuv = (const u16*)d_in[4]; p.wo = (const u16*)d_in[5]; p.w1 = (const u16*)d_in[7]; p.w3 = (const u16*)d_in[8];
  p.w2 = (const u16*)d_in[9]; p.out = (u16*)d_out; p.ws = (unsigned char*)d_ws;
  (void)hipFuncSetAttribute((const void*)mk::mega, hipFuncAttributeMaxDynamicSharedMemorySize, mk::L_BYTES + 2048);
  hipLaunchKernelGGL(mk::mega, dim3(256), dim3(mk::NT), mk::L_BYTES + 2048, stream, p);
}
```

```cpp
#include <hip/hip_runtime.h>
#include <hip/hip_bf16.h>
#include <stdint.h>

#define LAS __attribute__((address_space(3)))
#define GAS __attribute__((address_space(1)))
typedef unsigned short u16;
typedef unsigned v4u __attribute__((ext_vector_type(4)));
typedef unsigned v2u __attribute__((ext_vector_type(2)));
typedef float f32x4 __attribute__((ext_vector_type(4)));
typedef float f32x2 __attribute__((ext_vector_type(2)));
typedef short bf16x8 __attribute__((ext_vector_type(8)));
typedef short s16x4 __attribute__((ext_vector_type(4)));
typedef __bf16 bf16x2_t __attribute__((ext_vector_type(2)));
typedef unsigned short u16x2 __attribute__((ext_vector_type(2)));
#define DI __device__ __forceinline__

extern "C" __device__ unsigned long long llvm_dispatch_id() __asm("llvm.amdgcn.dispatch.id");

#define PROBE_T 4500
namespace mk {
constexpr int NT = 512;
constexpr unsigned SENT = 0xFFFFFFFFu;
constexpr unsigned SPIN_CAP = 1u << 14;
constexpr int AUX_SC1 = 16;
#define LO1 (c.kLO1)
#define HI1 (c.kHI1)
#define ONES (c.kONES)
#define NLO1 (c.kNLO1)
#define NHI1 (c.kNHI1)

constexpr int OFF_RDY = 0;
constexpr int OFF_X1 = 16384;
constexpr int OFF_ST = OFF_X1 + 2 * 2 * 32 * 1024;
constexpr int OFF_X3 = OFF_ST + 2 * 2 * 32 * 512;
constexpr int XV_STR = 34 * 1024;
constexpr int OFF_X4 = OFF_X3 + 4 * XV_STR;
constexpr int OFF_X2 = OFF_X4 + 4 * XV_STR;
constexpr int WS_BYTES = OFF_X2 + 2 * 2 * 32 * 65536;

constexpr int L_IMG = 0;
constexpr int L_QA = 131072;
constexpr int L_PB = L_QA + 16384;
constexpr int L_SB = L_PB + 4096;
constexpr int L_LN = L_SB + 1024;
constexpr int L_VEC = L_LN + 64;
constexpr int L_EW = L_VEC + 256;
constexpr int L_FR = L_EW + 2048;
constexpr int L_BYTES = L_FR + 2048;
constexpr int L_RED = L_QA;

struct Params {
  const u16* x; const u16* ckv; const u16* wq; const u16* wuk; const u16* wuv; const u16* wo;
  const u16* w1; const u16* w3; const u16* w2; u16* out; unsigned char* ws;
};

DI float u2f(unsigned u) { return __builtin_bit_cast(float, u); }
DI unsigned f2u(float f) { return __builtin_bit_cast(unsigned, f); }
DI float bf_lo(unsigned u) { return u2f(u << 16); }
DI float bf_hi(unsigned u) { return u2f(u & 0xFFFF0000u); }
DI float rbf(float f) { return (float)(__bf16)f; }
DI unsigned pack2(float lo, float hi) { bf16x2_t v = {(__bf16)lo, (__bf16)hi}; return __builtin_bit_cast(unsigned, v); }
DI unsigned canon(unsigned u) { return u < 0xFFFFFFFEu ? u : 0xFFFFFFFEu; }
DI float dot2(unsigned a, unsigned b, float c) { return __builtin_amdgcn_fdot2_f32_bf16(__builtin_bit_cast(bf16x2_t, a), __builtin_bit_cast(bf16x2_t, b), c, false); }
DI unsigned subsat(unsigned a, unsigned b) { return __builtin_bit_cast(unsigned, __builtin_elementwise_sub_sat(__builtin_bit_cast(u16x2, a), __builtin_bit_cast(u16x2, b))); }
DI unsigned umax3(unsigned a, unsigned b, unsigned c) { return max(max(a, b), c); }
DI f32x4 mfma(v4u a, v4u b, f32x4 c) { return __builtin_amdgcn_mfma_f32_16x16x32_bf16(__builtin_bit_cast(bf16x8, a), __builtin_bit_cast(bf16x8, b), c, 0, 0, 0); }
template <int CTRL> DI float dppf(float v) { return u2f((unsigned)__builtin_amdgcn_update_dpp(0, (int)f2u(v), CTRL, 0xf, 0xf, true)); }
template <int CTRL> DI unsigned dppu(unsigned v) { return (unsigned)__builtin_amdgcn_update_dpp(0, (int)v, CTRL, 0xf, 0xf, true); }
template <int CTRL, int RM> DI float dppm(float v) { return u2f((unsigned)__builtin_amdgcn_update_dpp(0, (int)f2u(v), CTRL, RM, 0xf, false)); }
DI float quad_sum(float v) { v += dppf<0xB1>(v); v += dppf<0x4E>(v); return v; }
DI float xg_sum(float v) {
  const auto a = __builtin_amdgcn_permlane16_swap(f2u(v), f2u(v), false, false);
  const unsigned a0 = a[0], a1 = a[1];
  v = u2f(a0) + u2f(a1);
  const auto b = __builtin_amdgcn_permlane32_swap(f2u(v), f2u(v), false, false);
  const unsigned b0 = b[0], b1 = b[1];
  return u2f(b0) + u2f(b1);
}
DI float x32_sum(float v) {
  const auto b = __builtin_amdgcn_permlane32_swap(f2u(v), f2u(v), false, false);
  const unsigned b0 = b[0], b1 = b[1];
  return u2f(b0) + u2f(b1);
}
DI float xg_max(float v) {
  const auto a = __builtin_amdgcn_permlane16_swap(f2u(v), f2u(v), false, false);
  const unsigned a0 = a[0], a1 = a[1];
  v = fmaxf(u2f(a0), u2f(a1));
  const auto b = __builtin_amdgcn_permlane32_swap(f2u(v), f2u(v), false, false);
  const unsigned b0 = b[0], b1 = b[1];
  return fmaxf(u2f(b0), u2f(b1));
}
DI unsigned swz(unsigned r) { return (((r ^ ((r >> 1) & 4u)) & 7u) << 1) | ((r >> 3) & 1u); }

typedef __amdgpu_buffer_rsrc_t rsrc_t;
DI v4u ld128(rsrc_t rs, int off) { return __builtin_amdgcn_raw_buffer_load_b128(rs, off, 0, AUX_SC1); }
DI v2u ld64(rsrc_t rs, int off) { return __builtin_amdgcn_raw_buffer_load_b64(rs, off, 0, AUX_SC1); }
DI unsigned ld32(rsrc_t rs, int off) { return __builtin_amdgcn_raw_buffer_load_b32(rs, off, 0, AUX_SC1); }
DI void st128(rsrc_t rs, int off, v4u v) { __builtin_amdgcn_raw_buffer_store_b128(v, rs, off, 0, AUX_SC1); }
DI void st64(rsrc_t rs, int off, v2u v) { __builtin_amdgcn_raw_buffer_store_b64(v, rs, off, 0, AUX_SC1); }
DI void st32(rsrc_t rs, int off, unsigned v) { __builtin_amdgcn_raw_buffer_store_b32(v, rs, off, 0, AUX_SC1); }
#define CBAR() asm volatile("" ::: "memory")
#define DRAIN() asm volatile("s_waitcnt vmcnt(0)" ::: "memory")
#define LBAR() do { asm volatile("s_waitcnt lgkmcnt(0)" ::: "memory"); __builtin_amdgcn_s_barrier(); asm volatile("" ::: "memory"); } while (0)

DI void tr8x8(const v4u (&r)[8], v4u* f) {
#pragma unroll
  for (int q = 0; q < 4; ++q)
#pragma unroll
    for (int d = 0; d < 4; ++d) {
      const unsigned a = r[2 * d][q], b = r[2 * d + 1][q];
      f[2 * q][d] = __builtin_amdgcn_perm(b, a, 0x05040100u);
      f[2 * q + 1][d] = __builtin_amdgcn_perm(b, a, 0x07060302u);
    }
}
DI void tr8x4(const v2u (&r)[8], v4u* f) {
#pragma unroll
  for (int q = 0; q < 2; ++q)
#pragma unroll
    for (int d = 0; d < 4; ++d) {
      const unsigned a = r[2 * d][q], b = r[2 * d + 1][q];
      f[2 * q][d] = __builtin_amdgcn_perm(b, a, 0x05040100u);
      f[2 * q + 1][d] = __builtin_amdgcn_perm(b, a, 0x07060302u);
    }
}

#define STAMP(c, i) do {} while (0)
struct Ctx {
  unsigned char* wsp;
  rsrc_t rs; LAS unsigned char* L;
  int lane, w, b, G, s, blk;
  unsigned spins;
  unsigned kLO1, kHI1, kONES, kNLO1, kNHI1;
  unsigned long long t0;
};
DI unsigned sconst(unsigned v) { unsigned r; asm volatile("s_mov_b32 %0, %1" : "=s"(r) : "i"(v)); return r; }
#define RELANE(c) do { int l_ = (c).lane; asm volatile("" : "+v"(l_)); (c).lane = l_; } while (0)
DI bool spin_ok(Ctx& c) { return ++c.spins < SPIN_CAP; }

DI int q_blk(int b, int h, int ch) { return (h >> 2) + 8 * (8 * b + 2 * (h & 3) + ch); }

DI bool nonce_eq(v4u a, v4u n) { return ((a.x ^ n.x) | (a.y ^ n.y) | (a.z ^ n.z) | (a.w ^ n.w)) == 0u; }
DI void wait_ready_a(Ctx& c, v4u nonce) {
  const int q = c.lane & 3;
  const int off = OFF_RDY + q_blk(c.b, 16 * c.G + c.w + 8 * (q >> 1), q & 1) * 64;
  for (;;) {
    CBAR();
    const v4u a = ld128(c.rs, off);
    if (__all(nonce_eq(a, nonce)) || !spin_ok(c)) break;
  }
  CBAR();
}
DI void wait_ready_b(Ctx& c, v4u nonce) {
  const int o0 = OFF_RDY + (c.b * 64 + c.lane) * 64 + 16, o1 = o0 + 128 * 64;
  for (;;) {
    CBAR();
    const v4u a = ld128(c.rs, o0), bq = ld128(c.rs, o1);
    if (__all(nonce_eq(a, nonce) & nonce_eq(bq, nonce)) || !spin_ok(c)) break;
  }
  CBAR();
}

template <int D>
DI void gate(Ctx& c, int off) {
  if (c.w == 0) {
    if (off >= 0) {
      for (;;) {
        CBAR();
        const unsigned v = __builtin_amdgcn_raw_buffer_load_b32(c.rs, off, 0, AUX_SC1);
        if (__builtin_amdgcn_readfirstlane(v) != SENT || !spin_ok(c)) break;
        __builtin_amdgcn_s_sleep(4);
      }
    }
    if (D > 0) __builtin_amdgcn_s_sleep(D);
  }
  __builtin_amdgcn_s_barrier();
}
#define D_F3 40
#define D_Q4 30
#define D_A1 30
#define D_FA1 45

template <bool RES_WS>
DI unsigned gather33(Ctx& c, int base, const u16* xin) {
  const int co = 128 * c.w + 4 * (c.lane & 31);
  unsigned v[16], res;
  if (!RES_WS) res = *(const GAS unsigned*)((const GAS unsigned char*)xin + co);
  const int o = base + (c.lane >> 5) * 1024 + co;
  for (;;) {
    CBAR();
#pragma unroll
    for (int i = 0; i < 16; ++i) v[i] = ld32(c.rs, o + i * 2048);
    if (RES_WS) res = ld32(c.rs, base + 32 * 1024 + co);
    unsigned m = RES_WS ? res : 0u;
#pragma unroll
    for (int i = 0; i < 16; i += 2) m = umax3(m, v[i], v[i + 1]);
    if (__all(m != SENT) || !spin_ok(c)) break;
  }
  float s0 = 0.f, s1 = 0.f, s2 = 0.f, s3 = 0.f;
#pragma unroll
  for (int i = 0; i < 16; i += 2) { s0 = dot2(v[i], LO1, s0); s1 = dot2(v[i], HI1, s1); s2 = dot2(v[i + 1], LO1, s2); s3 = dot2(v[i + 1], HI1, s3); }
  const unsigned o01 = pack2(x32_sum(s0 + s2), x32_sum(s1 + s3));
  return pack2(dot2(o01, LO1, bf_lo(res)), dot2(o01, HI1, bf_hi(res)));
}

DI void layernorm(Ctx& c, unsigned xp, v4u& a0, v4u& a1) {
  float t1 = dot2(xp, ONES, 0.f), t2 = dot2(xp, xp, 0.f);
  t1 += dppf<0x121>(t1); t2 += dppf<0x121>(t2);
  t1 += dppf<0x122>(t1); t2 += dppf<0x122>(t2);
  t1 += dppf<0x124>(t1); t2 += dppf<0x124>(t2);
  t1 += dppf<0x128>(t1); t2 += dppf<0x128>(t2);
  t1 += dppm<0x142, 0xa>(t1); t2 += dppm<0x142, 0xa>(t2);
  if (c.lane == 31) *(LAS f32x2*)(c.L + L_LN + c.w * 8) = f32x2{t1, t2};
  LBAR();
  f32x4 t[4];
#pragma unroll
  for (int i = 0; i < 4; ++i) t[i] = *(const LAS f32x4*)(c.L + L_LN + i * 16);
  const f32x4 tt = (t[0] + t[1]) + (t[2] + t[3]);
  const float mean = (tt.x + tt.z) * (1.0f / 512.0f);
  const float var = fmaxf((tt.y + tt.w) * (1.0f / 512.0f) - mean * mean, 0.f);
  const unsigned mv = pack2(mean, var);
  const float nmb = -bf_lo(mv);
  const float sd = rbf(__builtin_amdgcn_sqrtf(rbf(bf_hi(mv) + 1e-5f)));
  const float ri = __builtin_amdgcn_rcpf(sd);
  const unsigned d01 = pack2(dot2(xp, LO1, nmb), dot2(xp, HI1, nmb));
  const unsigned n01 = pack2(bf_lo(d01) * ri, bf_hi(d01) * ri);
  *(LAS unsigned*)(c.L + L_FR + c.w * 256 + c.lane * 4) = n01;
  a0 = *(const LAS v4u*)(c.L + L_FR + c.w * 256 + (c.lane >> 4) * 32);
  a1 = *(const LAS v4u*)(c.L + L_FR + c.w * 256 + (c.lane >> 4) * 32 + 16);
}

template <bool HILO>
DI void gemv_ksplit(Ctx& c, v4u a0, v4u a1, const v4u* W) {
  f32x4 acc[8];
#pragma unroll
  for (int t = 0; t < 8; ++t) acc[t] = mfma(a0, W[t], f32x4{0.f, 0.f, 0.f, 0.f});
#pragma unroll
  for (int t = 0; t < 8; ++t) acc[t] = mfma(a1, W[8 + t], acc[t]);
  if (c.lane < 16) {
    f32x4 o0, o1;
#pragma unroll
    for (int t = 0; t < 4; ++t) { o0[t] = HILO ? acc[t].x + acc[t].y : acc[t].x; o1[t] = HILO ? acc[4 + t].x + acc[4 + t].y : acc[4 + t].x; }
    *(LAS f32x4*)(c.L + L_RED + c.w * 512 + c.lane * 32) = o0;
    *(LAS f32x4*)(c.L + L_RED + c.w * 512 + c.lane * 32 + 16) = o1;
  }
}
DI float red_sum8(Ctx& c, int idx) {
  const LAS float* R = (const LAS float*)(c.L + L_RED);
  return ((R[idx] + R[128 + idx]) + (R[256 + idx] + R[384 + idx])) + ((R[512 + idx] + R[640 + idx]) + (R[768 + idx] + R[896 + idx]));
}
DI void reduce128(Ctx& c) {
  LBAR();
  if (c.w < 2) { const int d = c.w * 64 + c.lane; *(LAS __bf16*)(c.L + L_VEC + d * 2) = (__bf16)red_sum8(c, d); }
  LBAR();
}

DI float score_round(float s) {
  const float SC = 0.08838834764831845f;
  const unsigned bits = f2u(s);
  const unsigned tr = bits & 0xFFFF0000u;
  const unsigned g2 = pack2(u2f(tr) * SC, u2f(tr + 0x10000u) * SC);
  const float ga = bf_lo(g2);
  const float wgt = fminf(fmaxf((float)(bits & 0xFFFFu) * (1.0f / (65536.0f * 0.6f)) + (0.5f - 0.5f / 0.6f), 0.f), 1.f);
  return ga + wgt * (bf_hi(g2) - ga);
}

DI void attn_phase(Ctx& c, int l) {
  RELANE(c);
  const int tid = c.w * 64 + c.lane, n = c.lane & 15, g = c.lane >> 4;
  v4u kf[16];
  {
    const unsigned kb = L_IMG + (c.w * 16 + n) * 1024 + (((unsigned)g ^ swz(n)) << 4);
#pragma unroll
    for (int ks = 0; ks < 16; ++ks) kf[ks] = *(const LAS v4u*)(c.L + ((kb ^ (64u * (ks & 3))) + 256u * (ks >> 2)));
  }
  unsigned ab;
  {
    const unsigned q4 = (c.lane & 15) >> 2, p4 = c.lane & 3;
    const unsigned key = 4u * g + q4;
    const unsigned chunk = 8u * c.w + (p4 >> 1);
    ab = L_IMG + key * 1024 + ((chunk ^ swz(key)) << 4) + 8u * (p4 & 1);
    asm volatile("" : "+v"(ab));
  }
  {
    const int base = OFF_X1 + ((l * 2 + c.b) * 32 + 16 * c.G) * 1024 + tid * 16;
    v4u v0, v1;
    for (;;) {
      CBAR();
      v0 = ld128(c.rs, base);
      v1 = ld128(c.rs, base + 8192);
      const unsigned m = max(umax3(umax3(v0.x, v0.y, v0.z), v0.w, v1.x), umax3(v1.y, v1.z, v1.w));
      if (__all(m != SENT) || !spin_ok(c)) break;
    }
    STAMP(c, 2 + 8 * l);
    *(LAS v4u*)(c.L + L_QA + c.w * 1024 + (((unsigned)c.lane ^ swz(c.w)) << 4)) = v0;
    *(LAS v4u*)(c.L + L_QA + (c.w + 8) * 1024 + (((unsigned)c.lane ^ swz(c.w + 8)) << 4)) = v1;
  }
  LBAR();
  STAMP(c, 26 + l);
  f32x4 sacc = {0.f, 0.f, 0.f, 0.f}, sacc2 = {0.f, 0.f, 0.f, 0.f};
  {
    const unsigned qb = L_QA + n * 1024 + (((unsigned)g ^ swz(n)) << 4);
    constexpr int QD = 4;
    v4u qf[QD];
#pragma unroll
    for (int ks = 0; ks < QD; ++ks) qf[ks] = *(const LAS v4u*)(c.L + ((qb ^ (64u * (ks & 3))) + 256u * (ks >> 2)));
    __builtin_amdgcn_sched_barrier(0);
#pragma unroll
    for (int ks = 0; ks < 16; ++ks) {
      if (ks & 1) sacc2 = mfma(kf[ks], qf[ks % QD], sacc2); else sacc = mfma(kf[ks], qf[ks % QD], sacc);
      if (ks + QD < 16) qf[ks % QD] = *(const LAS v4u*)(c.L + ((qb ^ (64u * ((ks + QD) & 3))) + 256u * ((ks + QD) >> 2)));
      __builtin_amdgcn_sched_barrier(0);
    }
  }
  if (l == 1) { asm volatile("" :: "v"(sacc), "v"(sacc2)); STAMP(c, 17); }
  v2u vf[4][4][2];
  auto vf_issue = [&](int u) __attribute__((always_inline)) {
#pragma unroll
    for (int t = 0; t < 4; ++t) {
      const unsigned ad = (ab ^ (32u * t)) + 32768u * u;
      const s16x4 lo = __builtin_amdgcn_ds_read_tr16_b64_v4i16((LAS s16x4*)(c.L + ad));
      const s16x4 hi = __builtin_amdgcn_ds_read_tr16_b64_v4i16((LAS s16x4*)(c.L + ad + 16384u));
      vf[u][t][0] = __builtin_bit_cast(v2u, lo); vf[u][t][1] = __builtin_bit_cast(v2u, hi);
    }
  };
  vf_issue(0); vf_issue(1);
  __builtin_amdgcn_sched_barrier(0);
  {
    const float LOG2E = 1.4426950408889634f;
    float sv[4];
#pragma unroll
    for (int q = 0; q < 4; ++q) sv[q] = score_round(sacc[q] + sacc2[q]) * LOG2E;
    const float kw = ceilf(xg_max(fmaxf(fmaxf(sv[0], sv[1]), fmaxf(sv[2], sv[3]))));
    float p[4];
#pragma unroll
    for (int q = 0; q < 4; ++q) p[q] = __builtin_amdgcn_exp2f(sv[q] - kw);
    const float lw = xg_sum((p[0] + p[1]) + (p[2] + p[3]));
    *(LAS v2u*)(c.L + L_PB + (c.w >> 1) * 1024 + c.lane * 16 + (c.w & 1) * 8) = v2u{pack2(p[0], p[1]), pack2(p[2], p[3])};
    if (g == 0) *(LAS v2u*)(c.L + L_SB + n * 64 + c.w * 8) = v2u{(unsigned)(int)kw, f2u(lw)};
  }
  if (l == 1) STAMP(c, 18);
  LBAR();
  STAMP(c, 3 + 8 * l);
  f32x4 acc[4];
  {
    v4u sb[4], pr[4];
#pragma unroll
    for (int i = 0; i < 4; ++i) sb[i] = *(const LAS v4u*)(c.L + L_SB + n * 64 + i * 16);
#pragma unroll
    for (int u = 0; u < 4; ++u) pr[u] = *(const LAS v4u*)(c.L + L_PB + u * 1024 + c.lane * 16);
    vf_issue(2); vf_issue(3);
    __builtin_amdgcn_sched_barrier(0);
    int K = max(max(max((int)sb[0].x, (int)sb[0].z), max((int)sb[1].x, (int)sb[1].z)), max(max((int)sb[2].x, (int)sb[2].z), max((int)sb[3].x, (int)sb[3].z)));
    if (c.w == 0) {
      float Ls = 0.f;
#pragma unroll
      for (int i = 0; i < 4; ++i) Ls += ldexpf(u2f(sb[i].y), (int)sb[i].x - K) + ldexpf(u2f(sb[i].w), (int)sb[i].z - K);
      if (g == 0) st64(c.rs, OFF_ST + (((l * 2 + c.b) * 32 + 16 * c.G + n) * 64 + c.s) * 8, v2u{canon(f2u((float)K)), canon(f2u(Ls))});
    }
#pragma unroll
    for (int t = 0; t < 4; ++t) acc[t] = f32x4{0.f, 0.f, 0.f, 0.f};
#pragma unroll
    for (int u = 0; u < 4; ++u) {
      const unsigned d0 = (unsigned)min(K - (int)sb[u].x, 255) * 0x00800080u, d1 = (unsigned)min(K - (int)sb[u].z, 255) * 0x00800080u;
      const v4u pb = {subsat(pr[u].x, d0), subsat(pr[u].y, d0), subsat(pr[u].z, d1), subsat(pr[u].w, d1)};
#pragma unroll
      for (int t = 0; t < 4; ++t) {
        const v4u af = {vf[u][t][0].x, vf[u][t][0].y, vf[u][t][1].x, vf[u][t][1].y};
        acc[t] = mfma(pb, af, acc[t]);
      }
    }
  }
  if (l == 1) { asm volatile("" :: "v"(acc[0]), "v"(acc[1]), "v"(acc[2]), "v"(acc[3])); STAMP(c, 19); }
  {
    const int base = OFF_X2 + ((l * 2 + c.b) * 32 + 16 * c.G + 4 * g) * 65536 + c.w * 8192 + c.s * 128 + n * 8;
#pragma unroll
    for (int q = 0; q < 4; ++q)
      st64(c.rs, base + q * 65536, v2u{canon(pack2(acc[0][q], acc[1][q])), canon(pack2(acc[2][q], acc[3][q]))});
  }
  STAMP(c, 4 + 8 * l);
}

DI void fill_attn(Ctx& c) {
  const int n = c.lane & 15, g = c.lane >> 4;
  const v4u sv = {SENT, SENT, SENT, SENT};
#pragma unroll
  for (int l = 0; l < 2; ++l) {
    const int base = OFF_X2 + ((l * 2 + c.b) * 32 + 16 * c.G + (c.lane >> 3)) * 65536 + c.w * 8192 + c.s * 128 + (c.lane & 7) * 16;
    st128(c.rs, base, sv);
    st128(c.rs, base + 8 * 65536, sv);
    if (c.w == 0 && g == 0) st64(c.rs, OFF_ST + (((l * 2 + c.b) * 32 + 16 * c.G + n) * 64 + c.s) * 8, v2u{SENT, SENT});
  }
}

DI void ckv_issue(Ctx& c, const Params& p, v4u (&t)[16]) {
  const GAS unsigned char* src = (const GAS unsigned char*)p.ckv + ((size_t)c.b * 8192 + c.s * 128 + c.w * 16) * 1024 + c.lane * 16;
#pragma unroll
  for (int i = 0; i < 16; ++i) t[i] = *(const GAS v4u*)(src + i * 1024);
}
DI void ckv_stage(Ctx& c, const v4u (&t)[16]) {
#pragma unroll
  for (int i = 0; i < 16; ++i)
    *(LAS v4u*)(c.L + L_IMG + (c.w * 16 + i) * 1024 + (((unsigned)c.lane ^ swz(i)) << 4)) = t[i];
}
DI void publish_ready_b(Ctx& c, v4u nonce) {
  DRAIN();
  LBAR();
  if (c.w == 0 && c.lane == 0) st128(c.rs, OFF_RDY + c.blk * 64 + 16, nonce);
}

DI void role_q(Ctx& c, const Params& p, v4u nonce, int h, int ch) {
  const int n = c.lane & 15, g = c.lane >> 4;
  const v4u sv = {SENT, SENT, SENT, SENT};
  if (c.w == 0) st128(c.rs, OFF_X1 + (((c.lane >> 5) * 2 + c.b) * 32 + h) * 1024 + ch * 512 + (c.lane & 31) * 16, sv);
  unsigned xp = *(const GAS unsigned*)((const GAS unsigned char*)p.x + c.b * 1024 + 128 * c.w + 4 * (c.lane & 31));
  v4u rq[2][8], ru[8];
  {
    const GAS unsigned char* wq = (const GAS unsigned char*)p.wq + ((size_t)(64 * c.w + 16 * g) * 4096 + h * 128 + 8 * n) * 2;
#pragma unroll
    for (int ks = 0; ks < 2; ++ks)
#pragma unroll
      for (int j = 0; j < 8; ++j) rq[ks][j] = *(const GAS v4u*)(wq + (size_t)(8 * ks + j) * 8192);
    const GAS unsigned char* wu = (const GAS unsigned char*)p.wuk + ((size_t)(256 * ch + 32 * c.w + 2 * n) * 4096 + h * 128 + 8 * g) * 2;
#pragma unroll
    for (int t = 0; t < 2; ++t)
#pragma unroll
      for (int ks = 0; ks < 4; ++ks) ru[t * 4 + ks] = *(const GAS v4u*)(wu + (size_t)t * 8192 + ks * 64);
  }
  v4u ct[16];
  __builtin_amdgcn_sched_barrier(0);
  asm volatile("" : "+v"(xp));
  STAMP(c, 9);
  if (c.w == 0 && c.lane == 0) st128(c.rs, OFF_RDY + c.blk * 64, nonce);
  v4u Wq[16];
  const bool xpub = (h == 0 && ch == 0);
  auto q_phase = [&](int l) __attribute__((always_inline)) {
    v4u a0, a1;
    layernorm(c, xp, a0, a1);
    if (l == 0) { ckv_issue(c, p, ct); __builtin_amdgcn_sched_barrier(0); STAMP(c, 7); tr8x8(rq[0], Wq); tr8x8(rq[1], Wq + 8); asm volatile("" :: "v"(Wq[15])); STAMP(c, 22); }
    gemv_ksplit<false>(c, a0, a1, Wq);
    reduce128(c);
    if (l == 0) STAMP(c, 23);
    f32x4 acc0 = {0.f, 0.f, 0.f, 0.f}, acc1 = {0.f, 0.f, 0.f, 0.f};
#pragma unroll
    for (int ks = 0; ks < 4; ++ks) {
      const v4u a = *(const LAS v4u*)(c.L + L_VEC + ks * 64 + g * 16);
      acc0 = mfma(a, ru[ks], acc0);
      acc1 = mfma(a, ru[4 + ks], acc1);
    }
    if (c.lane < 16) st32(c.rs, OFF_X1 + ((l * 2 + c.b) * 32 + h) * 1024 + (256 * ch + 32 * c.w + 2 * n) * 2, canon(pack2(acc0.x, acc1.x)));
  };
  q_phase(0);
  STAMP(c, 21);
  fill_attn(c);
  if (xpub && c.w == 1) st128(c.rs, OFF_X3 + (1 * 2 + c.b) * XV_STR + 32 * 1024 + c.lane * 16, sv);
  ckv_stage(c, ct);
  publish_ready_b(c, nonce);
  STAMP(c, 20);
  wait_ready_a(c, nonce);
  STAMP(c, 1);
#pragma unroll 1
  for (int l = 0; l < 2; ++l) {
    attn_phase(c, l);
    if (l == 0) {
      wait_ready_b(c, nonce);
      gate<D_Q4>(c, OFF_X3 + (0 * 2 + c.b) * XV_STR + h * 1024);
      xp = gather33<true>(c, OFF_X4 + (0 * 2 + c.b) * XV_STR, nullptr);
      STAMP(c, 5);
      if (xpub && c.lane < 32) st32(c.rs, OFF_X3 + (1 * 2 + c.b) * XV_STR + 32 * 1024 + 128 * c.w + 4 * c.lane, canon(xp));
      q_phase(1);
      STAMP(c, 6);
    }
  }
  gate<D_Q4>(c, OFF_X3 + (1 * 2 + c.b) * XV_STR + h * 1024);
  if (c.w == 0) {
    const int qi = 2 * h + ch;
    const int off = OFF_X4 + (1 * 2 + c.b) * XV_STR + (c.lane < 33 ? c.lane : 32) * 1024 + qi * 16;
    v4u v;
    for (;;) {
      CBAR();
      v = ld128(c.rs, off);
      if (__all(max(umax3(v.x, v.y, v.z), v.w) != SENT) || !spin_ok(c)) break;
    }
    const unsigned wl = c.lane < 32 ? LO1 : 0u, wh = c.lane < 32 ? HI1 : 0u;
    v4u o;
#pragma unroll
    for (int q = 0; q < 4; ++q) {
      float lo = dot2(v[q], wl, 0.f), hi = dot2(v[q], wh, 0.f);
      lo = quad_sum(lo); hi = quad_sum(hi);
      lo += dppf<0x124>(lo); hi += dppf<0x124>(hi);
      lo += dppf<0x128>(lo); hi += dppf<0x128>(hi);
      lo += dppm<0x142, 0xa>(lo); hi += dppm<0x142, 0xa>(hi);
      const unsigned s2 = (unsigned)__builtin_amdgcn_readlane((int)pack2(lo, hi), 31);
      const unsigned r2 = (unsigned)__builtin_amdgcn_readlane((int)v[q], 32);
      o[q] = pack2(bf_lo(r2) + bf_lo(s2), bf_hi(r2) + bf_hi(s2));
    }
    while (__builtin_amdgcn_s_memrealtime() - c.t0 < PROBE_T) {}
    if (c.lane == 0) *(GAS v4u*)((GAS unsigned char*)p.out + c.b * 1024 + qi * 16) = o;
    STAMP(c, 16);
  }
}

DI void role_o(Ctx& c, const Params& p, v4u nonce, int h) {
  const int n = c.lane & 15, g = c.lane >> 4;
  const v4u sv = {SENT, SENT, SENT, SENT};
  {
    v4u ct[16];
    ckv_issue(c, p, ct);
    ckv_stage(c, ct);
  }
  __builtin_amdgcn_sched_barrier(0);
  v4u rv[2][8]; v2u ro[4][8];
  {
    const GAS unsigned char* wv = (const GAS unsigned char*)p.wuv + ((size_t)(64 * c.w + 2 * g) * 4096 + h * 128 + 8 * n) * 2;
#pragma unroll
    for (int ks = 0; ks < 2; ++ks)
#pragma unroll
      for (int j = 0; j < 8; ++j) rv[ks][j] = *(const GAS v4u*)(wv + (size_t)(16 * (j & 3) + 8 * ks + (j >> 2)) * 8192);
    const GAS unsigned char* wo = (const GAS unsigned char*)p.wo + ((size_t)(h * 128 + 8 * g) * 512 + 64 * c.w + 4 * n) * 2;
#pragma unroll
    for (int ks = 0; ks < 4; ++ks)
#pragma unroll
      for (int j = 0; j < 8; ++j) ro[ks][j] = *(const GAS v2u*)(wo + (size_t)(32 * ks + j) * 1024);
  }
  __builtin_amdgcn_sched_barrier(0);
  fill_attn(c);
  if (c.w < 2) st128(c.rs, OFF_X3 + (c.w * 2 + c.b) * XV_STR + h * 1024 + c.lane * 16, sv);
  v4u Wv[16], Wo[16];
  tr8x8(rv[0], Wv); tr8x8(rv[1], Wv + 8);
#pragma unroll
  for (int ks = 0; ks < 4; ++ks) tr8x4(ro[ks], Wo + 4 * ks);
  publish_ready_b(c, nonce);
  STAMP(c, 20);
  wait_ready_a(c, nonce);
  STAMP(c, 1);
#pragma unroll 1
  for (int l = 0; l < 2; ++l) {
    attn_phase(c, l);
    if (l == 0) wait_ready_b(c, nonce);
    float inv;
    {
      const int bs = OFF_ST + ((l * 2 + c.b) * 32 + h) * 512 + c.lane * 8;
      v2u st;
      for (;;) {
        CBAR();
        st = ld64(c.rs, bs);
        if (__all(max(st.x, st.y) != SENT) || !spin_ok(c)) break;
      }
      const float kf = u2f(st.x);
      float K = kf;
      K = fmaxf(K, dppf<0x128>(K)); K = fmaxf(K, dppf<0x124>(K)); K = fmaxf(K, dppf<0x122>(K)); K = fmaxf(K, dppf<0x121>(K));
      K = xg_max(K);
      const int d = (int)(K - kf);
      const unsigned eb = d < 127 ? (unsigned)(127 - d) << 7 : 0u;
      float Ls = u2f(st.y) * u2f(eb << 16);
      Ls += dppf<0x128>(Ls); Ls += dppf<0x124>(Ls); Ls += dppf<0x122>(Ls); Ls += dppf<0x121>(Ls);
      Ls = xg_sum(Ls);
      inv = 1.0f / Ls;
      *(LAS unsigned*)(c.L + L_EW + c.w * 256 + c.lane * 4) = eb;
    }
    unsigned el[16];
#pragma unroll
    for (int i = 0; i < 4; ++i) {
      const v4u e4 = *(const LAS v4u*)(c.L + L_EW + c.w * 256 + g * 64 + i * 16);
      el[4 * i] = e4.x; el[4 * i + 1] = e4.y; el[4 * i + 2] = e4.z; el[4 * i + 3] = e4.w;
    }
    const int bx = OFF_X2 + ((l * 2 + c.b) * 32 + h) * 65536 + c.w * 8192 + g * 2048 + n * 8;
    v2u v[16];
    for (;;) {
      CBAR();
#pragma unroll
      for (int i = 0; i < 16; ++i) v[i] = ld64(c.rs, bx + i * 128);
      unsigned m = 0u;
#pragma unroll
      for (int i = 0; i < 16; ++i) m = umax3(m, v[i].x, v[i].y);
      if (__all(m != SENT) || !spin_ok(c)) break;
    }
    STAMP(c, 5 + 8 * l);
    float s0 = 0.f, s1 = 0.f, s2 = 0.f, s3 = 0.f;
#pragma unroll
    for (int i = 0; i < 16; ++i) {
      const unsigned eh = el[i] << 16;
      s0 = dot2(v[i].x, el[i], s0); s1 = dot2(v[i].x, eh, s1); s2 = dot2(v[i].y, el[i], s2); s3 = dot2(v[i].y, eh, s3);
    }
    s0 = xg_sum(s0) * inv; s1 = xg_sum(s1) * inv; s2 = xg_sum(s2) * inv; s3 = xg_sum(s3) * inv;
    const unsigned h01 = pack2(s0, s1), h23 = pack2(s2, s3);
    const unsigned l01 = pack2(dot2(h01, NLO1, s0), dot2(h01, NHI1, s1)), l23 = pack2(dot2(h23, NLO1, s2), dot2(h23, NHI1, s3));
    *(LAS v2u*)(c.L + L_FR + c.w * 256 + (g & 1) * 128 + n * 8) = (g & 1) ? v2u{l01, l23} : v2u{h01, h23};
    const v4u a0 = *(const LAS v4u*)(c.L + L_FR + c.w * 256 + (n & 1) * 128 + g * 16);
    const v4u a1 = *(const LAS v4u*)(c.L + L_FR + c.w * 256 + (n & 1) * 128 + 64 + g * 16);
    asm volatile("" :: "v"(a0), "v"(a1));
    STAMP(c, 7 + 8 * l);
    gemv_ksplit<true>(c, a0, a1, Wv);
    STAMP(c, 22 + 2 * l);
    reduce128(c);
    STAMP(c, 23 + 2 * l);
    f32x4 acc[4];
#pragma unroll
    for (int t = 0; t < 4; ++t) acc[t] = f32x4{0.f, 0.f, 0.f, 0.f};
#pragma unroll
    for (int k4 = 0; k4 < 4; ++k4) {
      const v4u aa = *(const LAS v4u*)(c.L + L_VEC + k4 * 64 + g * 16);
#pragma unroll
      for (int t = 0; t < 4; ++t) acc[t] = mfma(aa, Wo[4 * k4 + t], acc[t]);
    }
    if (c.lane < 16) {
      v2u o = {canon(pack2(acc[0].x, acc[1].x)), canon(pack2(acc[2].x, acc[3].x))};
      st64(c.rs, OFF_X3 + (l * 2 + c.b) * XV_STR + h * 1024 + (64 * c.w + 4 * n) * 2, o);
    }
    STAMP(c, 6 + 8 * l);
    if (l == 0) gate<D_A1>(c, OFF_X4 + (0 * 2 + c.b) * XV_STR + h * 1024);
  }
}

DI void role_f(Ctx& c, const Params& p, v4u nonce, int j) {
  const int n = c.lane & 15, g = c.lane >> 4;
  const v4u sv = {SENT, SENT, SENT, SENT};
  {
    v4u ct[16];
    ckv_issue(c, p, ct);
    ckv_stage(c, ct);
  }
  __builtin_amdgcn_sched_barrier(0);
  v4u r13[2][8]; v2u r2[2][8];
  {
    const int nn = n < 14 ? n : 13;
    const u16* wsel = nn < 7 ? p.w1 : p.w3;
    const int col = 56 * j + 8 * (nn < 7 ? nn : nn - 7);
    const GAS unsigned char* wa = (const GAS unsigned char*)wsel + ((size_t)(64 * c.w + 16 * g) * 1792 + col) * 2;
#pragma unroll
    for (int ks = 0; ks < 2; ++ks)
#pragma unroll
      for (int jj = 0; jj < 8; ++jj) r13[ks][jj] = *(const GAS v4u*)(wa + (size_t)(8 * ks + jj) * 3584);
#pragma unroll
    for (int ks = 0; ks < 2; ++ks)
#pragma unroll
      for (int jj = 0; jj < 8; ++jj) {
        int f = 32 * ks + 8 * g + jj; f = f < 56 ? f : 55;
        r2[ks][jj] = *(const GAS v2u*)((const GAS unsigned char*)p.w2 + ((size_t)(56 * j + f) * 512 + 64 * c.w + 4 * n) * 2);
      }
  }
  __builtin_amdgcn_sched_barrier(0);
  fill_attn(c);
  if (c.w < 2) st128(c.rs, OFF_X4 + (c.w * 2 + c.b) * XV_STR + j * 1024 + c.lane * 16, sv);
  if (j == 0 && (c.w == 2 || c.w == 3)) st128(c.rs, OFF_X4 + ((c.w - 2) * 2 + c.b) * XV_STR + 32 * 1024 + c.lane * 16, sv);
  v4u W13[16], W2[8];
  tr8x8(r13[0], W13); tr8x8(r13[1], W13 + 8);
  tr8x4(r2[0], W2); tr8x4(r2[1], W2 + 4);
  publish_ready_b(c, nonce);
  STAMP(c, 20);
  wait_ready_a(c, nonce);
  STAMP(c, 1);
#pragma unroll 1
  for (int l = 0; l < 2; ++l) {
    attn_phase(c, l);
    unsigned xp;
    if (l == 0) wait_ready_b(c, nonce);
    gate<D_F3>(c, -1);
    if (l == 0) { xp = gather33<false>(c, OFF_X3 + (0 * 2 + c.b) * XV_STR, p.x + c.b * 512); }
    else xp = gather33<true>(c, OFF_X3 + (1 * 2 + c.b) * XV_STR, nullptr);
    STAMP(c, 5 + 8 * l);
    if (j == 0 && c.lane < 32) st32(c.rs, OFF_X4 + (l * 2 + c.b) * XV_STR + 32 * 1024 + 128 * c.w + 4 * c.lane, canon(xp));
    v4u a0, a1;
    layernorm(c, xp, a0, a1);
    STAMP(c, 7 + 8 * l);
    gemv_ksplit<false>(c, a0, a1, W13);
    STAMP(c, 22 + 2 * l);
    LBAR();
    STAMP(c, 8 + 8 * l);
    if (c.w == 0) {
      const int li = c.lane < 56 ? c.lane : 55;
      const unsigned gu = pack2(red_sum8(c, li), red_sum8(c, 56 + li));
      const float gt = bf_lo(gu);
      const float sg = rbf(gt * __builtin_amdgcn_rcpf(1.0f + __expf(-gt)));
      const float hv = c.lane < 56 ? sg * bf_hi(gu) : 0.f;
      *(LAS __bf16*)(c.L + L_VEC + c.lane * 2) = (__bf16)hv;
    }
    LBAR();
    STAMP(c, 23 + 2 * l);
    f32x4 acc[4];
#pragma unroll
    for (int t = 0; t < 4; ++t) acc[t] = f32x4{0.f, 0.f, 0.f, 0.f};
#pragma unroll
    for (int k2 = 0; k2 < 2; ++k2) {
      const v4u a = *(const LAS v4u*)(c.L + L_VEC + k2 * 64 + g * 16);
#pragma unroll
      for (int t = 0; t < 4; ++t) acc[t] = mfma(a, W2[4 * k2 + t], acc[t]);
    }
    if (c.lane < 16) {
      v2u o = {canon(pack2(acc[0].x, acc[1].x)), canon(pack2(acc[2].x, acc[3].x))};
      st64(c.rs, OFF_X4 + (l * 2 + c.b) * XV_STR + j * 1024 + (64 * c.w + 4 * n) * 2, o);
    }
    STAMP(c, 6 + 8 * l);
    if (l == 0) gate<D_FA1>(c, -1);
  }
}

__global__ void __launch_bounds__(NT, 2) mega(Params p) {
  extern __shared__ __attribute__((aligned(16))) unsigned char smem[];
  unsigned long long tt0;
  asm volatile("s_memrealtime %0\n\ts_waitcnt lgkmcnt(0)" : "=s"(tt0) :: "memory");
  Ctx c; c.t0 = tt0;
  c.rs = __builtin_amdgcn_make_buffer_rsrc((void*)p.ws, 0, WS_BYTES, 0x00020000);
  c.L = (LAS unsigned char*)smem;
  c.lane = threadIdx.x & 63;
  c.w = __builtin_amdgcn_readfirstlane(threadIdx.x >> 6);
  c.blk = blockIdx.x;
  const int x = c.blk & 7, slot = c.blk >> 3;
  c.b = (slot >> 3) & 1; c.G = slot >> 4; c.s = 8 * x + (slot & 7);
  const int r = 8 * c.G + (slot & 7);
  c.spins = 0; c.wsp = p.ws;
  c.kLO1 = sconst(0x00003F80u); c.kHI1 = sconst(0x3F800000u); c.kONES = sconst(0x3F803F80u); c.kNLO1 = sconst(0x0000BF80u); c.kNHI1 = sconst(0xBF800000u);
  STAMP(c, 0);
  const unsigned long long id = llvm_dispatch_id();
  const unsigned long long dp = (unsigned long long)__builtin_amdgcn_dispatch_ptr();
  const v4u nonce = {(unsigned)id ^ 0x5bd1e995u, (unsigned)(id >> 32) ^ 0x9e3779b9u, (unsigned)dp ^ 0x85ebca6bu, (unsigned)(dp >> 32) ^ 0xc2b2ae35u};
  if (r < 8) role_q(c, p, nonce, 4 * x + (r >> 1), r & 1);
  else if (r < 12) role_o(c, p, nonce, 4 * x + (r - 8));
  else role_f(c, p, nonce, 4 * x + (r - 12));
  if (c.w == 0 && c.lane < 2) st128(c.rs, OFF_RDY + c.blk * 64 + c.lane * 16, v4u{~nonce.x, ~nonce.y, ~nonce.z, ~nonce.w});
}
}

extern "C" void kernel_launch(void* const* d_in, const int* in_sizes, int n_in,
                              void* d_out, int out_size, void* d_ws, size_t ws_size,
                              hipStream_t stream) {
  mk::Params p;
  p.x = (const u16*)d_in[0]; p.ckv = (const u16*)d_in[1]; p.wq = (const u16*)d_in[2]; p.wuk = (const u16*)d_in[3];
  p.wuv = (const u16*)d_in[4]; p.wo = (const u16*)d_in[5]; p.w1 = (const u16*)d_in[7]; p.w3 = (const u16*)d_in[8];
  p.w2 = (const u16*)d_in[9]; p.out = (u16*)d_out; p.ws = (unsigned char*)d_ws;
  (void)hipFuncSetAttribute((const void*)mk::mega, hipFuncAttributeMaxDynamicSharedMemorySize, mk::L_BYTES + 2048);
  hipLaunchKernelGGL(mk::mega, dim3(256), dim3(mk::NT), mk::L_BYTES + 2048, stream, p);
}
```

```cpp
#include <hip/hip_runtime.h>
#include <hip/hip_bf16.h>
#include <stdint.h>

#define LAS __attribute__((address_space(3)))
#define GAS __attribute__((address_space(1)))
typedef unsigned short u16;
typedef unsigned v4u __attribute__((ext_vector_type(4)));
typedef unsigned v2u __attribute__((ext_vector_type(2)));
typedef float f32x4 __attribute__((ext_vector_type(4)));
typedef float f32x2 __attribute__((ext_vector_type(2)));
typedef short bf16x8 __attribute__((ext_vector_type(8)));
typedef short s16x4 __attribute__((ext_vector_type(4)));
typedef __bf16 bf16x2_t __attribute__((ext_vector_type(2)));
typedef unsigned short u16x2 __attribute__((ext_vector_type(2)));
#define DI __device__ __forceinline__

extern "C" __device__ unsigned long long llvm_dispatch_id() __asm("llvm.amdgcn.dispatch.id");

#define OPT_PF 1
#define OPT_DMA 1
#define OPT_CO 1
#define OPT_RDY 1
namespace mk {
constexpr int NT = 512;
constexpr unsigned SENT = 0xFFFFFFFFu;
constexpr unsigned SPIN_CAP = 1u << 14;
constexpr int AUX_SC1 = 16;
#define LO1 (c.kLO1)
#define HI1 (c.kHI1)
#define ONES (c.kONES)
#define NLO1 (c.kNLO1)
#define NHI1 (c.kNHI1)

constexpr int OFF_RDY = 0;
constexpr int OFF_X1 = 16384;
constexpr int OFF_ST = OFF_X1 + 2 * 2 * 32 * 1024;
constexpr int OFF_X3 = OFF_ST + 2 * 2 * 32 * 512;
constexpr int XV_STR = 34 * 1024;
constexpr int OFF_X4 = OFF_X3 + 4 * XV_STR;
constexpr int OFF_X2 = OFF_X4 + 4 * XV_STR;
constexpr int WS_BYTES = OFF_X2 + 2 * 2 * 32 * 65536;

constexpr int L_IMG = 0;
constexpr int L_QA = 131072;
constexpr int L_PB = L_QA + 16384;
constexpr int L_SB = L_PB + 4096;
constexpr int L_LN = L_SB + 1024;
constexpr int L_VEC = L_LN + 64;
constexpr int L_EW = L_VEC + 256;
constexpr int L_FR = L_EW + 2048;
constexpr int L_BYTES = L_FR + 2048;
constexpr int L_RED = L_QA;

struct Params {
  const u16* x; const u16* ckv; const u16* wq; const u16* wuk; const u16* wuv; const u16* wo;
  const u16* w1; const u16* w3; const u16* w2; u16* out; unsigned char* ws;
};

DI float u2f(unsigned u) { return __builtin_bit_cast(float, u); }
DI unsigned f2u(float f) { return __builtin_bit_cast(unsigned, f); }
DI float bf_lo(unsigned u) { return u2f(u << 16); }
DI float bf_hi(unsigned u) { return u2f(u & 0xFFFF0000u); }
DI float rbf(float f) { return (float)(__bf16)f; }
DI unsigned pack2(float lo, float hi) { bf16x2_t v = {(__bf16)lo, (__bf16)hi}; return __builtin_bit_cast(unsigned, v); }
DI unsigned canon(unsigned u) { return u < 0xFFFFFFFEu ? u : 0xFFFFFFFEu; }
DI float dot2(unsigned a, unsigned b, float c) { return __builtin_amdgcn_fdot2_f32_bf16(__builtin_bit_cast(bf16x2_t, a), __builtin_bit_cast(bf16x2_t, b), c, false); }
DI unsigned subsat(unsigned a, unsigned b) { return __builtin_bit_cast(unsigned, __builtin_elementwise_sub_sat(__builtin_bit_cast(u16x2, a), __builtin_bit_cast(u16x2, b))); }
DI unsigned umax3(unsigned a, unsigned b, unsigned c) { return max(max(a, b), c); }
DI f32x4 mfma(v4u a, v4u b, f32x4 c) { return __builtin_amdgcn_mfma_f32_16x16x32_bf16(__builtin_bit_cast(bf16x8, a), __builtin_bit_cast(bf16x8, b), c, 0, 0, 0); }
template <int CTRL> DI float dppf(float v) { return u2f((unsigned)__builtin_amdgcn_update_dpp(0, (int)f2u(v), CTRL, 0xf, 0xf, true)); }
template <int CTRL> DI unsigned dppu(unsigned v) { return (unsigned)__builtin_amdgcn_update_dpp(0, (int)v, CTRL, 0xf, 0xf, true); }
template <int CTRL, int RM> DI float dppm(float v) { return u2f((unsigned)__builtin_amdgcn_update_dpp(0, (int)f2u(v), CTRL, RM, 0xf, false)); }
DI float quad_sum(float v) { v += dppf<0xB1>(v); v += dppf<0x4E>(v); return v; }
DI float xg_sum(float v) {
  const auto a = __builtin_amdgcn_permlane16_swap(f2u(v), f2u(v), false, false);
  const unsigned a0 = a[0], a1 = a[1];
  v = u2f(a0) + u2f(a1);
  const auto b = __builtin_amdgcn_permlane32_swap(f2u(v), f2u(v), false, false);
  const unsigned b0 = b[0], b1 = b[1];
  return u2f(b0) + u2f(b1);
}
DI float x32_sum(float v) {
  const auto b = __builtin_amdgcn_permlane32_swap(f2u(v), f2u(v), false, false);
  const unsigned b0 = b[0], b1 = b[1];
  return u2f(b0) + u2f(b1);
}
DI float xg_max(float v) {
  const auto a = __builtin_amdgcn_permlane16_swap(f2u(v), f2u(v), false, false);
  const unsigned a0 = a[0], a1 = a[1];
  v = fmaxf(u2f(a0), u2f(a1));
  const auto b = __builtin_amdgcn_permlane32_swap(f2u(v), f2u(v), false, false);
  const unsigned b0 = b[0], b1 = b[1];
  return fmaxf(u2f(b0), u2f(b1));
}
DI unsigned swz(unsigned r) { return (((r ^ ((r >> 1) & 4u)) & 7u) << 1) | ((r >> 3) & 1u); }

typedef __amdgpu_buffer_rsrc_t rsrc_t;
DI v4u ld128(rsrc_t rs, int off) { return __builtin_amdgcn_raw_buffer_load_b128(rs, off, 0, AUX_SC1); }
DI v2u ld64(rsrc_t rs, int off) { return __builtin_amdgcn_raw_buffer_load_b64(rs, off, 0, AUX_SC1); }
DI unsigned ld32(rsrc_t rs, int off) { return __builtin_amdgcn_raw_buffer_load_b32(rs, off, 0, AUX_SC1); }
DI void st128(rsrc_t rs, int off, v4u v) { __builtin_amdgcn_raw_buffer_store_b128(v, rs, off, 0, AUX_SC1); }
DI void st64(rsrc_t rs, int off, v2u v) { __builtin_amdgcn_raw_buffer_store_b64(v, rs, off, 0, AUX_SC1); }
DI void st32(rsrc_t rs, int off, unsigned v) { __builtin_amdgcn_raw_buffer_store_b32(v, rs, off, 0, AUX_SC1); }
#define CBAR() asm volatile("" ::: "memory")
#define DRAIN() asm volatile("s_waitcnt vmcnt(0)" ::: "memory")
#define LBAR() do { asm volatile("s_waitcnt lgkmcnt(0)" ::: "memory"); __builtin_amdgcn_s_barrier(); asm volatile("" ::: "memory"); } while (0)

DI void tr8x8(const v4u (&r)[8], v4u* f) {
#pragma unroll
  for (int q = 0; q < 4; ++q)
#pragma unroll
    for (int d = 0; d < 4; ++d) {
      const unsigned a = r[2 * d][q], b = r[2 * d + 1][q];
      f[2 * q][d] = __builtin_amdgcn_perm(b, a, 0x05040100u);
      f[2 * q + 1][d] = __builtin_amdgcn_perm(b, a, 0x07060302u);
    }
}
DI void tr8x4(const v2u (&r)[8], v4u* f) {
#pragma unroll
  for (int q = 0; q < 2; ++q)
#pragma unroll
    for (int d = 0; d < 4; ++d) {
      const unsigned a = r[2 * d][q], b = r[2 * d + 1][q];
      f[2 * q][d] = __builtin_amdgcn_perm(b, a, 0x05040100u);
      f[2 * q + 1][d] = __builtin_amdgcn_perm(b, a, 0x07060302u);
    }
}

#define STAMP(c, i) do {} while (0)
struct Ctx {
  unsigned char* wsp;
  rsrc_t rs; LAS unsigned char* L;
  int lane, w, b, G, s, blk;
  unsigned spins;
  unsigned kLO1, kHI1, kONES, kNLO1, kNHI1;
  unsigned cpf;
  v4u nonce;
};
DI unsigned sconst(unsigned v) { unsigned r; asm volatile("s_mov_b32 %0, %1" : "=s"(r) : "i"(v)); return r; }
#define RELANE(c) do { int l_ = (c).lane; asm volatile("" : "+v"(l_)); (c).lane = l_; } while (0)
DI bool spin_ok(Ctx& c) { return ++c.spins < SPIN_CAP; }

DI int q_blk(int b, int h, int ch) { return (h >> 2) + 8 * (8 * b + 2 * (h & 3) + ch); }

DI bool nonce_eq(v4u a, v4u n) { return ((a.x ^ n.x) | (a.y ^ n.y) | (a.z ^ n.z) | (a.w ^ n.w)) == 0u; }
DI void wait_ready_a(Ctx& c, v4u nonce) {
  const int q = c.lane & 3;
  const int off = OFF_RDY + q_blk(c.b, 16 * c.G + c.w + 8 * (q >> 1), q & 1) * 64;
  for (;;) {
    CBAR();
    const v4u a = ld128(c.rs, off);
    if (__all(nonce_eq(a, nonce)) || !spin_ok(c)) break;
  }
  CBAR();
}
DI void wait_ready_b(Ctx& c, v4u nonce) {
  const int o0 = OFF_RDY + (c.b * 64 + c.lane) * 64 + 16, o1 = o0 + 128 * 64;
  for (;;) {
    CBAR();
    const v4u a = ld128(c.rs, o0), bq = ld128(c.rs, o1);
    if (__all(nonce_eq(a, nonce) & nonce_eq(bq, nonce)) || !spin_ok(c)) break;
  }
  CBAR();
}

template <int D>
DI void gate(Ctx& c, int off) {
  if (c.w == 0) {
    if (off >= 0) {
      for (;;) {
        CBAR();
        const unsigned v = __builtin_amdgcn_raw_buffer_load_b32(c.rs, off, 0, AUX_SC1);
        if (__builtin_amdgcn_readfirstlane(v) != SENT || !spin_ok(c)) break;
        __builtin_amdgcn_s_sleep(4);
      }
    }
    if (D > 0) __builtin_amdgcn_s_sleep(D);
  }
  __builtin_amdgcn_s_barrier();
}
#define D_F3 40
#define D_Q4 30
#define D_A1 30
#define D_FA1 45

template <bool RES_WS>
DI unsigned gather33(Ctx& c, int base, const u16* xin) {
  const int co = 128 * c.w + 4 * (c.lane & 31);
  unsigned v[16], res;
  if (!RES_WS) res = *(const GAS unsigned*)((const GAS unsigned char*)xin + co);
  const int o = base + (c.lane >> 5) * 1024 + co;
  for (;;) {
    CBAR();
#pragma unroll
    for (int i = 0; i < 16; ++i) v[i] = ld32(c.rs, o + i * 2048);
    if (RES_WS) res = ld32(c.rs, base + 32 * 1024 + co);
    unsigned m = RES_WS ? res : 0u;
#pragma unroll
    for (int i = 0; i < 16; i += 2) m = umax3(m, v[i], v[i + 1]);
    if (__all(m != SENT) || !spin_ok(c)) break;
  }
  float s0 = 0.f, s1 = 0.f, s2 = 0.f, s3 = 0.f;
#pragma unroll
  for (int i = 0; i < 16; i += 2) { s0 = dot2(v[i], LO1, s0); s1 = dot2(v[i], HI1, s1); s2 = dot2(v[i + 1], LO1, s2); s3 = dot2(v[i + 1], HI1, s3); }
  const unsigned o01 = pack2(x32_sum(s0 + s2), x32_sum(s1 + s3));
  return pack2(dot2(o01, LO1, bf_lo(res)), dot2(o01, HI1, bf_hi(res)));
}

DI void layernorm(Ctx& c, unsigned xp, v4u& a0, v4u& a1) {
  float t1 = dot2(xp, ONES, 0.f), t2 = dot2(xp, xp, 0.f);
  t1 += dppf<0x121>(t1); t2 += dppf<0x121>(t2);
  t1 += dppf<0x122>(t1); t2 += dppf<0x122>(t2);
  t1 += dppf<0x124>(t1); t2 += dppf<0x124>(t2);
  t1 += dppf<0x128>(t1); t2 += dppf<0x128>(t2);
  t1 += dppm<0x142, 0xa>(t1); t2 += dppm<0x142, 0xa>(t2);
  if (c.lane == 31) *(LAS f32x2*)(c.L + L_LN + c.w * 8) = f32x2{t1, t2};
  LBAR();
  f32x4 t[4];
#pragma unroll
  for (int i = 0; i < 4; ++i) t[i] = *(const LAS f32x4*)(c.L + L_LN + i * 16);
  const f32x4 tt = (t[0] + t[1]) + (t[2] + t[3]);
  const float mean = (tt.x + tt.z) * (1.0f / 512.0f);
  const float var = fmaxf((tt.y + tt.w) * (1.0f / 512.0f) - mean * mean, 0.f);
  const unsigned mv = pack2(mean, var);
  const float nmb = -bf_lo(mv);
  const float sd = rbf(__builtin_amdgcn_sqrtf(rbf(bf_hi(mv) + 1e-5f)));
  const float ri = __builtin_amdgcn_rcpf(sd);
  const unsigned d01 = pack2(dot2(xp, LO1, nmb), dot2(xp, HI1, nmb));
  const unsigned n01 = pack2(bf_lo(d01) * ri, bf_hi(d01) * ri);
  *(LAS unsigned*)(c.L + L_FR + c.w * 256 + c.lane * 4) = n01;
  a0 = *(const LAS v4u*)(c.L + L_FR + c.w * 256 + (c.lane >> 4) * 32);
  a1 = *(const LAS v4u*)(c.L + L_FR + c.w * 256 + (c.lane >> 4) * 32 + 16);
}

template <bool HILO>
DI void gemv_ksplit(Ctx& c, v4u a0, v4u a1, const v4u* W) {
  f32x4 acc[8];
#pragma unroll
  for (int t = 0; t < 8; ++t) acc[t] = mfma(a0, W[t], f32x4{0.f, 0.f, 0.f, 0.f});
#pragma unroll
  for (int t = 0; t < 8; ++t) acc[t] = mfma(a1, W[8 + t], acc[t]);
  if (c.lane < 16) {
    f32x4 o0, o1;
#pragma unroll
    for (int t = 0; t < 4; ++t) { o0[t] = HILO ? acc[t].x + acc[t].y : acc[t].x; o1[t] = HILO ? acc[4 + t].x + acc[4 + t].y : acc[4 + t].x; }
    *(LAS f32x4*)(c.L + L_RED + c.w * 512 + c.lane * 32) = o0;
    *(LAS f32x4*)(c.L + L_RED + c.w * 512 + c.lane * 32 + 16) = o1;
  }
}
DI float red_sum8(Ctx& c, int idx) {
  const LAS float* R = (const LAS float*)(c.L + L_RED);
  return ((R[idx] + R[128 + idx]) + (R[256 + idx] + R[384 + idx])) + ((R[512 + idx] + R[640 + idx]) + (R[768 + idx] + R[896 + idx]));
}
DI void reduce128(Ctx& c) {
  LBAR();
  if (c.w < 2) { const int d = c.w * 64 + c.lane; *(LAS __bf16*)(c.L + L_VEC + d * 2) = (__bf16)red_sum8(c, d); }
  LBAR();
}

template <int KB>
DI void gemv_nsplit(Ctx& c, const v4u* W, int off) {
  const int n = c.lane & 15, g = c.lane >> 4;
  f32x4 acc[8];
#pragma unroll
  for (int kb = 0; kb < KB; ++kb) {
    const v4u a = *(const LAS v4u*)(c.L + L_VEC + kb * 128 + (n & 1) * 64 + g * 16);
#pragma unroll
    for (int t = 0; t < 8; ++t) acc[t] = mfma(a, W[8 * kb + t], kb == 0 ? f32x4{0.f, 0.f, 0.f, 0.f} : acc[t]);
  }
  float r[8];
#pragma unroll
  for (int t = 0; t < 8; ++t) r[t] = acc[t].x + dppf<0x108>(acc[t].y);
  if (c.lane < 8) st128(c.rs, off + 128 * c.w + 16 * c.lane, v4u{canon(pack2(r[0], r[1])), canon(pack2(r[2], r[3])), canon(pack2(r[4], r[5])), canon(pack2(r[6], r[7]))});
}

DI float score_round(float s) {
  const float SC = 0.08838834764831845f;
  const unsigned bits = f2u(s);
  const unsigned tr = bits & 0xFFFF0000u;
  const unsigned g2 = pack2(u2f(tr) * SC, u2f(tr + 0x10000u) * SC);
  const float ga = bf_lo(g2);
  const float wgt = fminf(fmaxf((float)(bits & 0xFFFFu) * (1.0f / (65536.0f * 0.6f)) + (0.5f - 0.5f / 0.6f), 0.f), 1.f);
  return ga + wgt * (bf_hi(g2) - ga);
}

DI void attn_phase(Ctx& c, int l) {
  RELANE(c);
  const int tid = c.w * 64 + c.lane, n = c.lane & 15, g = c.lane >> 4;
  v4u kf[16];
  {
    const unsigned kb = L_IMG + (c.w * 16 + n) * 1024 + (((unsigned)g ^ swz(n)) << 4);
#pragma unroll
    for (int ks = 0; ks < 16; ++ks) kf[ks] = *(const LAS v4u*)(c.L + ((kb ^ (64u * (ks & 3))) + 256u * (ks >> 2)));
  }
  unsigned ab;
  {
    const unsigned q4 = (c.lane & 15) >> 2, p4 = c.lane & 3;
    const unsigned key = 4u * g + q4;
    const unsigned chunk = 8u * c.w + (p4 >> 1);
    ab = L_IMG + key * 1024 + ((chunk ^ swz(key)) << 4) + 8u * (p4 & 1);
    asm volatile("" : "+v"(ab));
  }
  {
    const int base = OFF_X1 + ((l * 2 + c.b) * 32 + 16 * c.G) * 1024 + tid * 16;
    v4u v0, v1;
    for (;;) {
      CBAR();
      v0 = ld128(c.rs, base);
      v1 = ld128(c.rs, base + 8192);
      const unsigned m = max(umax3(umax3(v0.x, v0.y, v0.z), v0.w, v1.x), umax3(v1.y, v1.z, v1.w));
      if (__all(m != SENT) || !spin_ok(c)) break;
    }
    STAMP(c, 2 + 8 * l);
    asm volatile("" :: "v"(c.cpf));
    *(LAS v4u*)(c.L + L_QA + c.w * 1024 + (((unsigned)c.lane ^ swz(c.w)) << 4)) = v0;
    *(LAS v4u*)(c.L + L_QA + (c.w + 8) * 1024 + (((unsigned)c.lane ^ swz(c.w + 8)) << 4)) = v1;
  }
  LBAR();
  if (l == 0 && c.w == 0 && c.lane == 0) st128(c.rs, OFF_RDY + c.blk * 64 + 16, c.nonce);
  STAMP(c, 26 + l);
  f32x4 sacc = {0.f, 0.f, 0.f, 0.f}, sacc2 = {0.f, 0.f, 0.f, 0.f};
  {
    const unsigned qb = L_QA + n * 1024 + (((unsigned)g ^ swz(n)) << 4);
    constexpr int QD = 4;
    v4u qf[QD];
#pragma unroll
    for (int ks = 0; ks < QD; ++ks) qf[ks] = *(const LAS v4u*)(c.L + ((qb ^ (64u * (ks & 3))) + 256u * (ks >> 2)));
    __builtin_amdgcn_sched_barrier(0);
#pragma unroll
    for (int ks = 0; ks < 16; ++ks) {
      if (ks & 1) sacc2 = mfma(kf[ks], qf[ks % QD], sacc2); else sacc = mfma(kf[ks], qf[ks % QD], sacc);
      if (ks + QD < 16) qf[ks % QD] = *(const LAS v4u*)(c.L + ((qb ^ (64u * ((ks + QD) & 3))) + 256u * ((ks + QD) >> 2)));
      __builtin_amdgcn_sched_barrier(0);
    }
  }
  if (l == 1) { asm volatile("" :: "v"(sacc), "v"(sacc2)); STAMP(c, 17); }
  v2u vf[4][4][2];
  auto vf_issue = [&](int u) __attribute__((always_inline)) {
#pragma unroll
    for (int t = 0; t < 4; ++t) {
      const unsigned ad = (ab ^ (32u * t)) + 32768u * u;
      const s16x4 lo = __builtin_amdgcn_ds_read_tr16_b64_v4i16((LAS s16x4*)(c.L + ad));
      const s16x4 hi = __builtin_amdgcn_ds_read_tr16_b64_v4i16((LAS s16x4*)(c.L + ad + 16384u));
      vf[u][t][0] = __builtin_bit_cast(v2u, lo); vf[u][t][1] = __builtin_bit_cast(v2u, hi);
    }
  };
  vf_issue(0); vf_issue(1);
  __builtin_amdgcn_sched_barrier(0);
  {
    const float LOG2E = 1.4426950408889634f;
    float sv[4];
#pragma unroll
    for (int q = 0; q < 4; ++q) sv[q] = score_round(sacc[q] + sacc2[q]) * LOG2E;
    const float kw = ceilf(xg_max(fmaxf(fmaxf(sv[0], sv[1]), fmaxf(sv[2], sv[3]))));
    float p[4];
#pragma unroll
    for (int q = 0; q < 4; ++q) p[q] = __builtin_amdgcn_exp2f(sv[q] - kw);
    const float lw = xg_sum((p[0] + p[1]) + (p[2] + p[3]));
    *(LAS v2u*)(c.L + L_PB + (c.w >> 1) * 1024 + c.lane * 16 + (c.w & 1) * 8) = v2u{pack2(p[0], p[1]), pack2(p[2], p[3])};
    if (g == 0) *(LAS v2u*)(c.L + L_SB + n * 64 + c.w * 8) = v2u{(unsigned)(int)kw, f2u(lw)};
  }
  if (l == 1) STAMP(c, 18);
  LBAR();
  STAMP(c, 3 + 8 * l);
  f32x4 acc[4];
  {
    v4u sb[4], pr[4];
#pragma unroll
    for (int i = 0; i < 4; ++i) sb[i] = *(const LAS v4u*)(c.L + L_SB + n * 64 + i * 16);
#pragma unroll
    for (int u = 0; u < 4; ++u) pr[u] = *(const LAS v4u*)(c.L + L_PB + u * 1024 + c.lane * 16);
    vf_issue(2); vf_issue(3);
    __builtin_amdgcn_sched_barrier(0);
    int K = max(max(max((int)sb[0].x, (int)sb[0].z), max((int)sb[1].x, (int)sb[1].z)), max(max((int)sb[2].x, (int)sb[2].z), max((int)sb[3].x, (int)sb[3].z)));
    if (c.w == 0) {
      float Ls = 0.f;
#pragma unroll
      for (int i = 0; i < 4; ++i) Ls += ldexpf(u2f(sb[i].y), (int)sb[i].x - K) + ldexpf(u2f(sb[i].w), (int)sb[i].z - K);
      if (g == 0) st64(c.rs, OFF_ST + (((l * 2 + c.b) * 32 + 16 * c.G + n) * 64 + c.s) * 8, v2u{canon(f2u((float)K)), canon(f2u(Ls))});
    }
#pragma unroll
    for (int t = 0; t < 4; ++t) acc[t] = f32x4{0.f, 0.f, 0.f, 0.f};
#pragma unroll
    for (int u = 0; u < 4; ++u) {
      const unsigned d0 = __umul24((unsigned)min(K - (int)sb[u].x, 255), 0x00800080u), d1 = __umul24((unsigned)min(K - (int)sb[u].z, 255), 0x00800080u);
      const v4u pb = {subsat(pr[u].x, d0), subsat(pr[u].y, d0), subsat(pr[u].z, d1), subsat(pr[u].w, d1)};
#pragma unroll
      for (int t = 0; t < 4; ++t) {
        const v4u af = {vf[u][t][0].x, vf[u][t][0].y, vf[u][t][1].x, vf[u][t][1].y};
        acc[t] = mfma(pb, af, acc[t]);
      }
    }
  }
  if (l == 1) { asm volatile("" :: "v"(acc[0]), "v"(acc[1]), "v"(acc[2]), "v"(acc[3])); STAMP(c, 19); }
  {
    const int base = OFF_X2 + ((l * 2 + c.b) * 32 + 16 * c.G + 4 * g) * 65536 + c.w * 8192 + c.s * 128 + n * 8;
#pragma unroll
    for (int q = 0; q < 4; ++q)
      st64(c.rs, base + q * 65536, v2u{canon(pack2(acc[0][q], acc[1][q])), canon(pack2(acc[2][q], acc[3][q]))});
  }
  STAMP(c, 4 + 8 * l);
}

DI void fill_attn(Ctx& c) {
  const int n = c.lane & 15, g = c.lane >> 4;
  const v4u sv = {SENT, SENT, SENT, SENT};
#pragma unroll
  for (int l = 0; l < 2; ++l) {
    const int base = OFF_X2 + ((l * 2 + c.b) * 32 + 16 * c.G + (c.lane >> 3)) * 65536 + c.w * 8192 + c.s * 128 + (c.lane & 7) * 16;
    st128(c.rs, base, sv);
    st128(c.rs, base + 8 * 65536, sv);
    if (c.w == 0 && g == 0) st64(c.rs, OFF_ST + (((l * 2 + c.b) * 32 + 16 * c.G + n) * 64 + c.s) * 8, v2u{SENT, SENT});
  }
}

DI void ckv_dma(Ctx& c, const Params& p) {
  const GAS unsigned char* src = (const GAS unsigned char*)p.ckv + ((size_t)c.b * 8192 + c.s * 128 + c.w * 16) * 1024;
#pragma unroll
  for (int i = 0; i < 16; ++i)
    __builtin_amdgcn_global_load_lds((const GAS unsigned*)(src + i * 1024 + (((unsigned)c.lane ^ swz(i)) << 4)),
                                     (LAS unsigned*)(c.L + L_IMG + (c.w * 16 + i) * 1024), 16, 0, 0);
}
DI void ckv_issue(Ctx& c, const Params& p, v4u (&t)[16]) {
  const GAS unsigned char* src = (const GAS unsigned char*)p.ckv + ((size_t)c.b * 8192 + c.s * 128 + c.w * 16) * 1024 + c.lane * 16;
#pragma unroll
  for (int i = 0; i < 16; ++i) t[i] = *(const GAS v4u*)(src + i * 1024);
}
DI void ckv_stage(Ctx& c, const v4u (&t)[16]) {
#pragma unroll
  for (int i = 0; i < 16; ++i)
    *(LAS v4u*)(c.L + L_IMG + (c.w * 16 + i) * 1024 + (((unsigned)c.lane ^ swz(i)) << 4)) = t[i];
}
DI void publish_ready_b(Ctx& c, v4u nonce) {
  LBAR();
}

DI void role_q(Ctx& c, const Params& p, v4u nonce, int h, int ch) {
  asm volatile("; role Q" ::: "memory");
  const int n = c.lane & 15, g = c.lane >> 4;
  const v4u sv = {SENT, SENT, SENT, SENT};
  if (c.w == 0) st128(c.rs, OFF_X1 + (((c.lane >> 5) * 2 + c.b) * 32 + h) * 1024 + ch * 512 + (c.lane & 31) * 16, sv);
  unsigned xp = *(const GAS unsigned*)((const GAS unsigned char*)p.x + c.b * 1024 + 128 * c.w + 4 * (c.lane & 31));
  v4u rq[2][8], ru[8];
  {
    const GAS unsigned char* wq = (const GAS unsigned char*)p.wq + ((size_t)(64 * c.w + 16 * g) * 4096 + h * 128 + 8 * n) * 2;
#pragma unroll
    for (int ks = 0; ks < 2; ++ks)
#pragma unroll
      for (int j = 0; j < 8; ++j) rq[ks][j] = *(const GAS v4u*)(wq + (size_t)(8 * ks + j) * 8192);
    const GAS unsigned char* wu = (const GAS unsigned char*)p.wuk + ((size_t)(256 * ch + 32 * c.w + g) * 4096 + h * 128 + 8 * n) * 2;
#pragma unroll
    for (int i = 0; i < 8; ++i) ru[i] = *(const GAS v4u*)(wu + (size_t)(4 * i) * 8192);
  }
  v4u ct[16];
  __builtin_amdgcn_sched_barrier(0);
  asm volatile("" : "+v"(xp));
  STAMP(c, 9);
  if (c.w == 0 && c.lane == 0) st128(c.rs, OFF_RDY + c.blk * 64, nonce);
  v4u Wq[16];
  const bool xpub = (h == 0 && ch == 0);
  auto q_phase = [&](int l) __attribute__((always_inline)) {
    v4u a0, a1;
    layernorm(c, xp, a0, a1);
    if (l == 0) { ckv_issue(c, p, ct); __builtin_amdgcn_sched_barrier(0); STAMP(c, 7); tr8x8(rq[0], Wq); tr8x8(rq[1], Wq + 8); asm volatile("" :: "v"(Wq[15])); STAMP(c, 22); }
    gemv_ksplit<false>(c, a0, a1, Wq);
    if (l == 0) {
#pragma unroll
      for (int i = 0; i < 8; ++i) { const unsigned row = 4 * i + g; *(LAS v4u*)(c.L + L_IMG + c.w * 8192 + row * 256 + (((unsigned)n ^ swz(row >> 1)) << 4)) = ru[i]; }
#pragma unroll
      for (int t = 0; t < 2; ++t)
#pragma unroll
        for (int ks = 0; ks < 4; ++ks) ru[t * 4 + ks] = *(const LAS v4u*)(c.L + L_IMG + c.w * 8192 + (2 * n + t) * 256 + (((unsigned)(4 * ks + g) ^ swz(n)) << 4));
    }
    reduce128(c);
    if (l == 0) STAMP(c, 23);
    f32x4 acc0 = {0.f, 0.f, 0.f, 0.f}, acc1 = {0.f, 0.f, 0.f, 0.f};
#pragma unroll
    for (int ks = 0; ks < 4; ++ks) {
      const v4u a = *(const LAS v4u*)(c.L + L_VEC + ks * 64 + g * 16);
      acc0 = mfma(a, ru[ks], acc0);
      acc1 = mfma(a, ru[4 + ks], acc1);
    }
    if (c.lane < 16) st32(c.rs, OFF_X1 + ((l * 2 + c.b) * 32 + h) * 1024 + (256 * ch + 32 * c.w + 2 * n) * 2, canon(pack2(acc0.x, acc1.x)));
  };
  q_phase(0);
  STAMP(c, 21);
  wait_ready_a(c, nonce);
  fill_attn(c);
  if (xpub && c.w == 1) st128(c.rs, OFF_X3 + (1 * 2 + c.b) * XV_STR + 32 * 1024 + c.lane * 16, sv);
  ckv_stage(c, ct);
  publish_ready_b(c, nonce);
  STAMP(c, 20);
  STAMP(c, 1);
#pragma unroll 1
  for (int l = 0; l < 2; ++l) {
    attn_phase(c, l);
    if (l == 0) {
      wait_ready_b(c, nonce);
      gate<D_Q4>(c, OFF_X3 + (0 * 2 + c.b) * XV_STR + h * 1024);
      xp = gather33<true>(c, OFF_X4 + (0 * 2 + c.b) * XV_STR, nullptr);
      STAMP(c, 5);
      if (xpub && c.lane < 32) st32(c.rs, OFF_X3 + (1 * 2 + c.b) * XV_STR + 32 * 1024 + 128 * c.w + 4 * c.lane, canon(xp));
      q_phase(1);
      STAMP(c, 6);
    }
  }
  gate<D_Q4>(c, OFF_X3 + (1 * 2 + c.b) * XV_STR + h * 1024);
  if (c.w == 0) {
    const int qi = 2 * h + ch;
    const int off = OFF_X4 + (1 * 2 + c.b) * XV_STR + (c.lane < 33 ? c.lane : 32) * 1024 + qi * 16;
    v4u v;
    for (;;) {
      CBAR();
      v = ld128(c.rs, off);
      if (__all(max(umax3(v.x, v.y, v.z), v.w) != SENT) || !spin_ok(c)) break;
    }
    const unsigned wl = c.lane < 32 ? LO1 : 0u, wh = c.lane < 32 ? HI1 : 0u;
    v4u o;
#pragma unroll
    for (int q = 0; q < 4; ++q) {
      float lo = dot2(v[q], wl, 0.f), hi = dot2(v[q], wh, 0.f);
      lo = quad_sum(lo); hi = quad_sum(hi);
      lo += dppf<0x124>(lo); hi += dppf<0x124>(hi);
      lo += dppf<0x128>(lo); hi += dppf<0x128>(hi);
      lo += dppm<0x142, 0xa>(lo); hi += dppm<0x142, 0xa>(hi);
      const unsigned s2 = (unsigned)__builtin_amdgcn_readlane((int)pack2(lo, hi), 31);
      const unsigned r2 = (unsigned)__builtin_amdgcn_readlane((int)v[q], 32);
      o[q] = pack2(bf_lo(r2) + bf_lo(s2), bf_hi(r2) + bf_hi(s2));
    }
    if (c.lane == 0) *(GAS v4u*)((GAS unsigned char*)p.out + c.b * 1024 + qi * 16) = o;
    STAMP(c, 16);
  }
}

DI void role_o(Ctx& c, const Params& p, v4u nonce, int h) {
  asm volatile("; role O" ::: "memory");
  const int n = c.lane & 15, g = c.lane >> 4;
  const v4u sv = {SENT, SENT, SENT, SENT};
  ckv_dma(c, p);
  DRAIN();
  __builtin_amdgcn_sched_barrier(0);
  v4u rv[2][8], ro[2][8];
  {
    const GAS unsigned char* wv = (const GAS unsigned char*)p.wuv + ((size_t)(64 * c.w + 2 * g) * 4096 + h * 128 + 8 * n) * 2;
#pragma unroll
    for (int ks = 0; ks < 2; ++ks)
#pragma unroll
      for (int j = 0; j < 8; ++j) rv[ks][j] = *(const GAS v4u*)(wv + (size_t)(16 * (j & 3) + 8 * ks + (j >> 2)) * 8192);
    const GAS unsigned char* wo = (const GAS unsigned char*)p.wo + ((size_t)(h * 128 + 32 * (n >> 3) + 8 * g) * 512 + 64 * c.w + 8 * (n & 7)) * 2;
#pragma unroll
    for (int kb = 0; kb < 2; ++kb)
#pragma unroll
      for (int j = 0; j < 8; ++j) ro[kb][j] = *(const GAS v4u*)(wo + (size_t)(64 * kb + j) * 1024);
  }
  __builtin_amdgcn_sched_barrier(0);
  fill_attn(c);
  if (c.w < 2) st128(c.rs, OFF_X3 + (c.w * 2 + c.b) * XV_STR + h * 1024 + c.lane * 16, sv);
  v4u Wv[16], Wo[16];
  tr8x8(rv[0], Wv); tr8x8(rv[1], Wv + 8);
  tr8x8(ro[0], Wo); tr8x8(ro[1], Wo + 8);
  publish_ready_b(c, nonce);
  STAMP(c, 20);
  wait_ready_a(c, nonce);
  STAMP(c, 1);
#pragma unroll 1
  for (int l = 0; l < 2; ++l) {
    attn_phase(c, l);
    if (l == 0) wait_ready_b(c, nonce);
    float inv;
    {
      const int bs = OFF_ST + ((l * 2 + c.b) * 32 + h) * 512 + c.lane * 8;
      v2u st;
      for (;;) {
        CBAR();
        st = ld64(c.rs, bs);
        if (__all(max(st.x, st.y) != SENT) || !spin_ok(c)) break;
      }
      const float kf = u2f(st.x);
      float K = kf;
      K = fmaxf(K, dppf<0x128>(K)); K = fmaxf(K, dppf<0x124>(K)); K = fmaxf(K, dppf<0x122>(K)); K = fmaxf(K, dppf<0x121>(K));
      K = xg_max(K);
      const int d = (int)(K - kf);
      const unsigned eb = d < 127 ? (unsigned)(127 - d) << 7 : 0u;
      float Ls = u2f(st.y) * u2f(eb << 16);
      Ls += dppf<0x128>(Ls); Ls += dppf<0x124>(Ls); Ls += dppf<0x122>(Ls); Ls += dppf<0x121>(Ls);
      Ls = xg_sum(Ls);
      inv = 1.0f / Ls;
      *(LAS unsigned*)(c.L + L_EW + c.w * 256 + c.lane * 4) = eb;
    }
    unsigned el[16];
#pragma unroll
    for (int i = 0; i < 4; ++i) {
      const v4u e4 = *(const LAS v4u*)(c.L + L_EW + c.w * 256 + g * 64 + i * 16);
      el[4 * i] = e4.x; el[4 * i + 1] = e4.y; el[4 * i + 2] = e4.z; el[4 * i + 3] = e4.w;
    }
    const int bx = OFF_X2 + ((l * 2 + c.b) * 32 + h) * 65536 + c.w * 8192 + g * 2048 + n * 8;
    v2u v[16];
    for (;;) {
      CBAR();
#pragma unroll
      for (int i = 0; i < 16; ++i) v[i] = ld64(c.rs, bx + i * 128);
      unsigned m = 0u;
#pragma unroll
      for (int i = 0; i < 16; ++i) m = umax3(m, v[i].x, v[i].y);
      if (__all(m != SENT) || !spin_ok(c)) break;
    }
    STAMP(c, 5 + 8 * l);
    float s0 = 0.f, s1 = 0.f, s2 = 0.f, s3 = 0.f;
#pragma unroll
    for (int i = 0; i < 16; ++i) {
      const unsigned eh = el[i] << 16;
      s0 = dot2(v[i].x, el[i], s0); s1 = dot2(v[i].x, eh, s1); s2 = dot2(v[i].y, el[i], s2); s3 = dot2(v[i].y, eh, s3);
    }
    s0 = xg_sum(s0) * inv; s1 = xg_sum(s1) * inv; s2 = xg_sum(s2) * inv; s3 = xg_sum(s3) * inv;
    const unsigned h01 = pack2(s0, s1), h23 = pack2(s2, s3);
    const unsigned l01 = pack2(dot2(h01, NLO1, s0), dot2(h01, NHI1, s1)), l23 = pack2(dot2(h23, NLO1, s2), dot2(h23, NHI1, s3));
    *(LAS v2u*)(c.L + L_FR + c.w * 256 + (g & 1) * 128 + n * 8) = (g & 1) ? v2u{l01, l23} : v2u{h01, h23};
    const v4u a0 = *(const LAS v4u*)(c.L + L_FR + c.w * 256 + (n & 1) * 128 + g * 16);
    const v4u a1 = *(const LAS v4u*)(c.L + L_FR + c.w * 256 + (n & 1) * 128 + 64 + g * 16);
    asm volatile("" :: "v"(a0), "v"(a1));
    STAMP(c, 7 + 8 * l);
    gemv_ksplit<true>(c, a0, a1, Wv);
    STAMP(c, 22 + 2 * l);
    reduce128(c);
    STAMP(c, 23 + 2 * l);
    gemv_nsplit<2>(c, Wo, OFF_X3 + (l * 2 + c.b) * XV_STR + h * 1024);
    STAMP(c, 6 + 8 * l);
    if (l == 0) gate<D_A1>(c, OFF_X4 + (0 * 2 + c.b) * XV_STR + h * 1024);
  }
}

DI void role_f(Ctx& c, const Params& p, v4u nonce, int j) {
  asm volatile("; role F" ::: "memory");
  const int n = c.lane & 15, g = c.lane >> 4;
  const v4u sv = {SENT, SENT, SENT, SENT};
  ckv_dma(c, p);
  DRAIN();
  __builtin_amdgcn_sched_barrier(0);
  v4u r13[2][8], r2[8];
  {
    const int nn = n < 14 ? n : 13;
    const u16* wsel = nn < 7 ? p.w1 : p.w3;
    const int col = 56 * j + 8 * (nn < 7 ? nn : nn - 7);
    const GAS unsigned char* wa = (const GAS unsigned char*)wsel + ((size_t)(64 * c.w + 16 * g) * 1792 + col) * 2;
#pragma unroll
    for (int ks = 0; ks < 2; ++ks)
#pragma unroll
      for (int jj = 0; jj < 8; ++jj) r13[ks][jj] = *(const GAS v4u*)(wa + (size_t)(8 * ks + jj) * 3584);
#pragma unroll
    for (int jj = 0; jj < 8; ++jj) {
      int f = 32 * (n >> 3) + 8 * g + jj; f = f < 56 ? f : 55;
      r2[jj] = *(const GAS v4u*)((const GAS unsigned char*)p.w2 + ((size_t)(56 * j + f) * 512 + 64 * c.w + 8 * (n & 7)) * 2);
    }
  }
  __builtin_amdgcn_sched_barrier(0);
  fill_attn(c);
  if (c.w < 2) st128(c.rs, OFF_X4 + (c.w * 2 + c.b) * XV_STR + j * 1024 + c.lane * 16, sv);
  if (j == 0 && (c.w == 2 || c.w == 3)) st128(c.rs, OFF_X4 + ((c.w - 2) * 2 + c.b) * XV_STR + 32 * 1024 + c.lane * 16, sv);
  v4u W13[16], W2[8];
  tr8x8(r13[0], W13); tr8x8(r13[1], W13 + 8);
  tr8x8(r2, W2);
  publish_ready_b(c, nonce);
  STAMP(c, 20);
  wait_ready_a(c, nonce);
  STAMP(c, 1);
#pragma unroll 1
  for (int l = 0; l < 2; ++l) {
    attn_phase(c, l);
    unsigned xp;
    if (l == 0) wait_ready_b(c, nonce);
    gate<D_F3>(c, -1);
    if (l == 0) { xp = gather33<false>(c, OFF_X3 + (0 * 2 + c.b) * XV_STR, p.x + c.b * 512); }
    else xp = gather33<true>(c, OFF_X3 + (1 * 2 + c.b) * XV_STR, nullptr);
    STAMP(c, 5 + 8 * l);
    if (j == 0 && c.lane < 32) st32(c.rs, OFF_X4 + (l * 2 + c.b) * XV_STR + 32 * 1024 + 128 * c.w + 4 * c.lane, canon(xp));
    v4u a0, a1;
    layernorm(c, xp, a0, a1);
    STAMP(c, 7 + 8 * l);
    gemv_ksplit<false>(c, a0, a1, W13);
    STAMP(c, 22 + 2 * l);
    LBAR();
    STAMP(c, 8 + 8 * l);
    if (c.w == 0) {
      const int li = c.lane < 56 ? c.lane : 55;
      const unsigned gu = pack2(red_sum8(c, li), red_sum8(c, 56 + li));
      const float gt = bf_lo(gu);
      const float sg = rbf(gt * __builtin_amdgcn_rcpf(1.0f + __expf(-gt)));
      const float hv = c.lane < 56 ? sg * bf_hi(gu) : 0.f;
      *(LAS __bf16*)(c.L + L_VEC + c.lane * 2) = (__bf16)hv;
    }
    LBAR();
    STAMP(c, 23 + 2 * l);
    gemv_nsplit<1>(c, W2, OFF_X4 + (l * 2 + c.b) * XV_STR + j * 1024);
    STAMP(c, 6 + 8 * l);
    if (l == 0) gate<D_FA1>(c, -1);
  }
}

__global__ void __launch_bounds__(NT, 2) mega(Params p) {
  extern __shared__ __attribute__((aligned(16))) unsigned char smem[];
  Ctx c;
  {
    unsigned long long pc0; unsigned clen, cpf;
    asm volatile("s_getpc_b64 %0\n.Lmk_here:\n\ts_mov_b32 %1, .Lmk_end-.Lmk_here" : "=s"(pc0), "=s"(clen));
    const unsigned o = min(threadIdx.x * 128u, clen);
    asm volatile("global_load_dword %0, %1, %2" : "=v"(cpf) : "v"(o), "s"(pc0) : "memory");
    c.cpf = cpf;
  }
  c.rs = __builtin_amdgcn_make_buffer_rsrc((void*)p.ws, 0, WS_BYTES, 0x00020000);
  c.L = (LAS unsigned char*)smem;
  c.lane = threadIdx.x & 63;
  c.w = __builtin_amdgcn_readfirstlane(threadIdx.x >> 6);
  c.blk = blockIdx.x;
  const int x = c.blk & 7, slot = c.blk >> 3;
  c.b = (slot >> 3) & 1; c.G = slot >> 4; c.s = 8 * x + (slot & 7);
  const int r = 8 * c.G + (slot & 7);
  c.spins = 0; c.wsp = p.ws;
  c.kLO1 = sconst(0x00003F80u); c.kHI1 = sconst(0x3F800000u); c.kONES = sconst(0x3F803F80u); c.kNLO1 = sconst(0x0000BF80u); c.kNHI1 = sconst(0xBF800000u);
  STAMP(c, 0);
  const unsigned long long id = llvm_dispatch_id();
  const unsigned long long dp = (unsigned long long)__builtin_amdgcn_dispatch_ptr();
  const v4u nonce = {(unsigned)id ^ 0x5bd1e995u, (unsigned)(id >> 32) ^ 0x9e3779b9u, (unsigned)dp ^ 0x85ebca6bu, (unsigned)(dp >> 32) ^ 0xc2b2ae35u};
  c.nonce = nonce;
  if (r < 8) role_q(c, p, nonce, 4 * x + (r >> 1), r & 1);
  else if (r < 12) role_o(c, p, nonce, 4 * x + (r - 8));
  else role_f(c, p, nonce, 4 * x + (r - 12));
  asm volatile(".Lmk_end:" ::: "memory");
  if (c.w == 0 && c.lane < 2) st128(c.rs, OFF_RDY + c.blk * 64 + c.lane * 16, v4u{~nonce.x, ~nonce.y, ~nonce.z, ~nonce.w});
}
}

extern "C" void kernel_launch(void* const* d_in, const int* in_sizes, int n_in,
                              void* d_out, int out_size, void* d_ws, size_t ws_size,
                              hipStream_t stream) {
  mk::Params p;
  p.x = (const u16*)d_in[0]; p.ckv = (const u16*)d_in[1]; p.wq = (const u16*)d_in[2]; p.wuk = (const u16*)d_in[3];
  p.wuv = (const u16*)d_in[4]; p.wo = (const u16*)d_in[5]; p.w1 = (const u16*)d_in[7]; p.w3 = (const u16*)d_in[8];
  p.w2 = (const u16*)d_in[9]; p.out = (u16*)d_out; p.ws = (unsigned char*)d_ws;
  (void)hipFuncSetAttribute((const void*)mk::mega, hipFuncAttributeMaxDynamicSharedMemorySize, mk::L_BYTES + 2048);
  hipLaunchKernelGGL(mk::mega, dim3(256), dim3(mk::NT), mk::L_BYTES + 2048, stream, p);
}
```

```cpp
#include <hip/hip_runtime.h>
#include <hip/hip_bf16.h>
#include <stdint.h>

#define LAS __attribute__((address_space(3)))
#define GAS __attribute__((address_space(1)))
typedef unsigned short u16;
typedef unsigned v4u __attribute__((ext_vector_type(4)));
typedef unsigned v2u __attribute__((ext_vector_type(2)));
typedef float f32x4 __attribute__((ext_vector_type(4)));
typedef float f32x2 __attribute__((ext_vector_type(2)));
typedef short bf16x8 __attribute__((ext_vector_type(8)));
typedef short s16x4 __attribute__((ext_vector_type(4)));
typedef __bf16 bf16x2_t __attribute__((ext_vector_type(2)));
typedef unsigned short u16x2 __attribute__((ext_vector_type(2)));
#define DI __device__ __forceinline__

extern "C" __device__ unsigned long long llvm_dispatch_id() __asm("llvm.amdgcn.dispatch.id");

#define OPT_PF 1
#define OPT_DMA 1
#define OPT_CO 1
#define OPT_RDY 1
#define PROBE_T 750
namespace mk {
constexpr int NT = 512;
constexpr unsigned SENT = 0xFFFFFFFFu;
constexpr unsigned SPIN_CAP = 1u << 14;
constexpr int AUX_SC1 = 16;
#define LO1 (c.kLO1)
#define HI1 (c.kHI1)
#define ONES (c.kONES)
#define NLO1 (c.kNLO1)
#define NHI1 (c.kNHI1)

constexpr int OFF_RDY = 0;
constexpr int OFF_X1 = 16384;
constexpr int OFF_ST = OFF_X1 + 2 * 2 * 32 * 1024;
constexpr int OFF_X3 = OFF_ST + 2 * 2 * 32 * 512;
constexpr int XV_STR = 34 * 1024;
constexpr int OFF_X4 = OFF_X3 + 4 * XV_STR;
constexpr int OFF_X2 = OFF_X4 + 4 * XV_STR;
constexpr int WS_BYTES = OFF_X2 + 2 * 2 * 32 * 65536;

constexpr int L_IMG = 0;
constexpr int L_QA = 131072;
constexpr int L_PB = L_QA + 16384;
constexpr int L_SB = L_PB + 4096;
constexpr int L_LN = L_SB + 1024;
constexpr int L_VEC = L_LN + 64;
constexpr int L_EW = L_VEC + 256;
constexpr int L_FR = L_EW + 2048;
constexpr int L_BYTES = L_FR + 2048;
constexpr int L_RED = L_QA;

struct Params {
  const u16* x; const u16* ckv; const u16* wq; const u16* wuk; const u16* wuv; const u16* wo;
  const u16* w1; const u16* w3; const u16* w2; u16* out; unsigned char* ws;
};

DI float u2f(unsigned u) { return __builtin_bit_cast(float, u); }
DI unsigned f2u(float f) { return __builtin_bit_cast(unsigned, f); }
DI float bf_lo(unsigned u) { return u2f(u << 16); }
DI float bf_hi(unsigned u) { return u2f(u & 0xFFFF0000u); }
DI float rbf(float f) { return (float)(__bf16)f; }
DI unsigned pack2(float lo, float hi) { bf16x2_t v = {(__bf16)lo, (__bf16)hi}; return __builtin_bit_cast(unsigned, v); }
DI unsigned canon(unsigned u) { return u < 0xFFFFFFFEu ? u : 0xFFFFFFFEu; }
DI float dot2(unsigned a, unsigned b, float c) { return __builtin_amdgcn_fdot2_f32_bf16(__builtin_bit_cast(bf16x2_t, a), __builtin_bit_cast(bf16x2_t, b), c, false); }
DI unsigned subsat(unsigned a, unsigned b) { return __builtin_bit_cast(unsigned, __builtin_elementwise_sub_sat(__builtin_bit_cast(u16x2, a), __builtin_bit_cast(u16x2, b))); }
DI unsigned umax3(unsigned a, unsigned b, unsigned c) { return max(max(a, b), c); }
DI f32x4 mfma(v4u a, v4u b, f32x4 c) { return __builtin_amdgcn_mfma_f32_16x16x32_bf16(__builtin_bit_cast(bf16x8, a), __builtin_bit_cast(bf16x8, b), c, 0, 0, 0); }
template <int CTRL> DI float dppf(float v) { return u2f((unsigned)__builtin_amdgcn_update_dpp(0, (int)f2u(v), CTRL, 0xf, 0xf, true)); }
template <int CTRL> DI unsigned dppu(unsigned v) { return (unsigned)__builtin_amdgcn_update_dpp(0, (int)v, CTRL, 0xf, 0xf, true); }
template <int CTRL, int RM> DI float dppm(float v) { return u2f((unsigned)__builtin_amdgcn_update_dpp(0, (int)f2u(v), CTRL, RM, 0xf, false)); }
DI float quad_sum(float v) { v += dppf<0xB1>(v); v += dppf<0x4E>(v); return v; }
DI float xg_sum(float v) {
  const auto a = __builtin_amdgcn_permlane16_swap(f2u(v), f2u(v), false, false);
  const unsigned a0 = a[0], a1 = a[1];
  v = u2f(a0) + u2f(a1);
  const auto b = __builtin_amdgcn_permlane32_swap(f2u(v), f2u(v), false, false);
  const unsigned b0 = b[0], b1 = b[1];
  return u2f(b0) + u2f(b1);
}
DI float x32_sum(float v) {
  const auto b = __builtin_amdgcn_permlane32_swap(f2u(v), f2u(v), false, false);
  const unsigned b0 = b[0], b1 = b[1];
  return u2f(b0) + u2f(b1);
}
DI float xg_max(float v) {
  const auto a = __builtin_amdgcn_permlane16_swap(f2u(v), f2u(v), false, false);
  const unsigned a0 = a[0], a1 = a[1];
  v = fmaxf(u2f(a0), u2f(a1));
  const auto b = __builtin_amdgcn_permlane32_swap(f2u(v), f2u(v), false, false);
  const unsigned b0 = b[0], b1 = b[1];
  return fmaxf(u2f(b0), u2f(b1));
}
DI unsigned swz(unsigned r) { return (((r ^ ((r >> 1) & 4u)) & 7u) << 1) | ((r >> 3) & 1u); }

typedef __amdgpu_buffer_rsrc_t rsrc_t;
DI v4u ld128(rsrc_t rs, int off) { return __builtin_amdgcn_raw_buffer_load_b128(rs, off, 0, AUX_SC1); }
DI v2u ld64(rsrc_t rs, int off) { return __builtin_amdgcn_raw_buffer_load_b64(rs, off, 0, AUX_SC1); }
DI unsigned ld32(rsrc_t rs, int off) { return __builtin_amdgcn_raw_buffer_load_b32(rs, off, 0, AUX_SC1); }
DI void st128(rsrc_t rs, int off, v4u v) { __builtin_amdgcn_raw_buffer_store_b128(v, rs, off, 0, AUX_SC1); }
DI void st64(rsrc_t rs, int off, v2u v) { __builtin_amdgcn_raw_buffer_store_b64(v, rs, off, 0, AUX_SC1); }
DI void st32(rsrc_t rs, int off, unsigned v) { __builtin_amdgcn_raw_buffer_store_b32(v, rs, off, 0, AUX_SC1); }
#define CBAR() asm volatile("" ::: "memory")
#define DRAIN() asm volatile("s_waitcnt vmcnt(0)" ::: "memory")
#define LBAR() do { asm volatile("s_waitcnt lgkmcnt(0)" ::: "memory"); __builtin_amdgcn_s_barrier(); asm volatile("" ::: "memory"); } while (0)

DI void tr8x8(const v4u (&r)[8], v4u* f) {
#pragma unroll
  for (int q = 0; q < 4; ++q)
#pragma unroll
    for (int d = 0; d < 4; ++d) {
      const unsigned a = r[2 * d][q], b = r[2 * d + 1][q];
      f[2 * q][d] = __builtin_amdgcn_perm(b, a, 0x05040100u);
      f[2 * q + 1][d] = __builtin_amdgcn_perm(b, a, 0x07060302u);
    }
}
DI void tr8x4(const v2u (&r)[8], v4u* f) {
#pragma unroll
  for (int q = 0; q < 2; ++q)
#pragma unroll
    for (int d = 0; d < 4; ++d) {
      const unsigned a = r[2 * d][q], b = r[2 * d + 1][q];
      f[2 * q][d] = __builtin_amdgcn_perm(b, a, 0x05040100u);
      f[2 * q + 1][d] = __builtin_amdgcn_perm(b, a, 0x07060302u);
    }
}

#define STAMP(c, i) do {} while (0)
struct Ctx {
  unsigned char* wsp;
  rsrc_t rs; LAS unsigned char* L;
  int lane, w, b, G, s, blk;
  unsigned spins;
  unsigned kLO1, kHI1, kONES, kNLO1, kNHI1;
  unsigned cpf;
  v4u nonce;
  unsigned long long t0;
};
DI unsigned sconst(unsigned v) { unsigned r; asm volatile("s_mov_b32 %0, %1" : "=s"(r) : "i"(v)); return r; }
#define RELANE(c) do { int l_ = (c).lane; asm volatile("" : "+v"(l_)); (c).lane = l_; } while (0)
DI bool spin_ok(Ctx& c) { return ++c.spins < SPIN_CAP; }

DI int q_blk(int b, int h, int ch) { return (h >> 2) + 8 * (8 * b + 2 * (h & 3) + ch); }

DI bool nonce_eq(v4u a, v4u n) { return ((a.x ^ n.x) | (a.y ^ n.y) | (a.z ^ n.z) | (a.w ^ n.w)) == 0u; }
DI void wait_ready_a(Ctx& c, v4u nonce) {
  const int q = c.lane & 3;
  const int off = OFF_RDY + q_blk(c.b, 16 * c.G + c.w + 8 * (q >> 1), q & 1) * 64;
  for (;;) {
    CBAR();
    const v4u a = ld128(c.rs, off);
    if (__all(nonce_eq(a, nonce)) || !spin_ok(c)) break;
  }
  CBAR();
}
DI void wait_ready_b(Ctx& c, v4u nonce) {
  const int o0 = OFF_RDY + (c.b * 64 + c.lane) * 64 + 16, o1 = o0 + 128 * 64;
  for (;;) {
    CBAR();
    const v4u a = ld128(c.rs, o0), bq = ld128(c.rs, o1);
    if (__all(nonce_eq(a, nonce) & nonce_eq(bq, nonce)) || !spin_ok(c)) break;
  }
  CBAR();
}

template <int D>
DI void gate(Ctx& c, int off) {
  if (c.w == 0) {
    if (off >= 0) {
      for (;;) {
        CBAR();
        const unsigned v = __builtin_amdgcn_raw_buffer_load_b32(c.rs, off, 0, AUX_SC1);
        if (__builtin_amdgcn_readfirstlane(v) != SENT || !spin_ok(c)) break;
        __builtin_amdgcn_s_sleep(4);
      }
    }
    if (D > 0) __builtin_amdgcn_s_sleep(D);
  }
  __builtin_amdgcn_s_barrier();
}
#define D_F3 40
#define D_Q4 30
#define D_A1 30
#define D_FA1 45

template <bool RES_WS>
DI unsigned gather33(Ctx& c, int base, const u16* xin) {
  const int co = 128 * c.w + 4 * (c.lane & 31);
  unsigned v[16], res;
  if (!RES_WS) res = *(const GAS unsigned*)((const GAS unsigned char*)xin + co);
  const int o = base + (c.lane >> 5) * 1024 + co;
  for (;;) {
    CBAR();
#pragma unroll
    for (int i = 0; i < 16; ++i) v[i] = ld32(c.rs, o + i * 2048);
    if (RES_WS) res = ld32(c.rs, base + 32 * 1024 + co);
    unsigned m = RES_WS ? res : 0u;
#pragma unroll
    for (int i = 0; i < 16; i += 2) m = umax3(m, v[i], v[i + 1]);
    if (__all(m != SENT) || !spin_ok(c)) break;
  }
  float s0 = 0.f, s1 = 0.f, s2 = 0.f, s3 = 0.f;
#pragma unroll
  for (int i = 0; i < 16; i += 2) { s0 = dot2(v[i], LO1, s0); s1 = dot2(v[i], HI1, s1); s2 = dot2(v[i + 1], LO1, s2); s3 = dot2(v[i + 1], HI1, s3); }
  const unsigned o01 = pack2(x32_sum(s0 + s2), x32_sum(s1 + s3));
  return pack2(dot2(o01, LO1, bf_lo(res)), dot2(o01, HI1, bf_hi(res)));
}

DI void layernorm(Ctx& c, unsigned xp, v4u& a0, v4u& a1) {
  float t1 = dot2(xp, ONES, 0.f), t2 = dot2(xp, xp, 0.f);
  t1 += dppf<0x121>(t1); t2 += dppf<0x121>(t2);
  t1 += dppf<0x122>(t1); t2 += dppf<0x122>(t2);
  t1 += dppf<0x124>(t1); t2 += dppf<0x124>(t2);
  t1 += dppf<0x128>(t1); t2 += dppf<0x128>(t2);
  t1 += dppm<0x142, 0xa>(t1); t2 += dppm<0x142, 0xa>(t2);
  if (c.lane == 31) *(LAS f32x2*)(c.L + L_LN + c.w * 8) = f32x2{t1, t2};
  LBAR();
  f32x4 t[4];
#pragma unroll
  for (int i = 0; i < 4; ++i) t[i] = *(const LAS f32x4*)(c.L + L_LN + i * 16);
  const f32x4 tt = (t[0] + t[1]) + (t[2] + t[3]);
  const float mean = (tt.x + tt.z) * (1.0f / 512.0f);
  const float var = fmaxf((tt.y + tt.w) * (1.0f / 512.0f) - mean * mean, 0.f);
  const unsigned mv = pack2(mean, var);
  const float nmb = -bf_lo(mv);
  const float sd = rbf(__builtin_amdgcn_sqrtf(rbf(bf_hi(mv) + 1e-5f)));
  const float ri = __builtin_amdgcn_rcpf(sd);
  const unsigned d01 = pack2(dot2(xp, LO1, nmb), dot2(xp, HI1, nmb));
  const unsigned n01 = pack2(bf_lo(d01) * ri, bf_hi(d01) * ri);
  *(LAS unsigned*)(c.L + L_FR + c.w * 256 + c.lane * 4) = n01;
  a0 = *(const LAS v4u*)(c.L + L_FR + c.w * 256 + (c.lane >> 4) * 32);
  a1 = *(const LAS v4u*)(c.L + L_FR + c.w * 256 + (c.lane >> 4) * 32 + 16);
}

template <bool HILO>
DI void gemv_ksplit(Ctx& c, v4u a0, v4u a1, const v4u* W) {
  f32x4 acc[8];
#pragma unroll
  for (int t = 0; t < 8; ++t) acc[t] = mfma(a0, W[t], f32x4{0.f, 0.f, 0.f, 0.f});
#pragma unroll
  for (int t = 0; t < 8; ++t) acc[t] = mfma(a1, W[8 + t], acc[t]);
  if (c.lane < 16) {
    f32x4 o0, o1;
#pragma unroll
    for (int t = 0; t < 4; ++t) { o0[t] = HILO ? acc[t].x + acc[t].y : acc[t].x; o1[t] = HILO ? acc[4 + t].x + acc[4 + t].y : acc[4 + t].x; }
    *(LAS f32x4*)(c.L + L_RED + c.w * 512 + c.lane * 32) = o0;
    *(LAS f32x4*)(c.L + L_RED + c.w * 512 + c.lane * 32 + 16) = o1;
  }
}
DI float red_sum8(Ctx& c, int idx) {
  const LAS float* R = (const LAS float*)(c.L + L_RED);
  return ((R[idx] + R[128 + idx]) + (R[256 + idx] + R[384 + idx])) + ((R[512 + idx] + R[640 + idx]) + (R[768 + idx] + R[896 + idx]));
}
DI void reduce128(Ctx& c) {
  LBAR();
  if (c.w < 2) { const int d = c.w * 64 + c.lane; *(LAS __bf16*)(c.L + L_VEC + d * 2) = (__bf16)red_sum8(c, d); }
  LBAR();
}

template <int KB>
DI void gemv_nsplit(Ctx& c, const v4u* W, int off) {
  const int n = c.lane & 15, g = c.lane >> 4;
  f32x4 acc[8];
#pragma unroll
  for (int kb = 0; kb < KB; ++kb) {
    const v4u a = *(const LAS v4u*)(c.L + L_VEC + kb * 128 + (n & 1) * 64 + g * 16);
#pragma unroll
    for (int t = 0; t < 8; ++t) acc[t] = mfma(a, W[8 * kb + t], kb == 0 ? f32x4{0.f, 0.f, 0.f, 0.f} : acc[t]);
  }
  float r[8];
#pragma unroll
  for (int t = 0; t < 8; ++t) r[t] = acc[t].x + dppf<0x108>(acc[t].y);
  if (c.lane < 8) st128(c.rs, off + 128 * c.w + 16 * c.lane, v4u{canon(pack2(r[0], r[1])), canon(pack2(r[2], r[3])), canon(pack2(r[4], r[5])), canon(pack2(r[6], r[7]))});
}

DI float score_round(float s) {
  const float SC = 0.08838834764831845f;
  const unsigned bits = f2u(s);
  const unsigned tr = bits & 0xFFFF0000u;
  const unsigned g2 = pack2(u2f(tr) * SC, u2f(tr + 0x10000u) * SC);
  const float ga = bf_lo(g2);
  const float wgt = fminf(fmaxf((float)(bits & 0xFFFFu) * (1.0f / (65536.0f * 0.6f)) + (0.5f - 0.5f / 0.6f), 0.f), 1.f);
  return ga + wgt * (bf_hi(g2) - ga);
}

DI void attn_phase(Ctx& c, int l) {
  RELANE(c);
  const int tid = c.w * 64 + c.lane, n = c.lane & 15, g = c.lane >> 4;
  v4u kf[16];
  {
    const unsigned kb = L_IMG + (c.w * 16 + n) * 1024 + (((unsigned)g ^ swz(n)) << 4);
#pragma unroll
    for (int ks = 0; ks < 16; ++ks) kf[ks] = *(const LAS v4u*)(c.L + ((kb ^ (64u * (ks & 3))) + 256u * (ks >> 2)));
  }
  unsigned ab;
  {
    const unsigned q4 = (c.lane & 15) >> 2, p4 = c.lane & 3;
    const unsigned key = 4u * g + q4;
    const unsigned chunk = 8u * c.w + (p4 >> 1);
    ab = L_IMG + key * 1024 + ((chunk ^ swz(key)) << 4) + 8u * (p4 & 1);
    asm volatile("" : "+v"(ab));
  }
  {
    const int base = OFF_X1 + ((l * 2 + c.b) * 32 + 16 * c.G) * 1024 + tid * 16;
    v4u v0, v1;
    for (;;) {
      CBAR();
      v0 = ld128(c.rs, base);
      v1 = ld128(c.rs, base + 8192);
      const unsigned m = max(umax3(umax3(v0.x, v0.y, v0.z), v0.w, v1.x), umax3(v1.y, v1.z, v1.w));
      if (__all(m != SENT) || !spin_ok(c)) break;
    }
    STAMP(c, 2 + 8 * l);
    asm volatile("" :: "v"(c.cpf));
    *(LAS v4u*)(c.L + L_QA + c.w * 1024 + (((unsigned)c.lane ^ swz(c.w)) << 4)) = v0;
    *(LAS v4u*)(c.L + L_QA + (c.w + 8) * 1024 + (((unsigned)c.lane ^ swz(c.w + 8)) << 4)) = v1;
  }
  LBAR();
  if (l == 0 && c.w == 0 && c.lane == 0) st128(c.rs, OFF_RDY + c.blk * 64 + 16, c.nonce);
  STAMP(c, 26 + l);
  f32x4 sacc = {0.f, 0.f, 0.f, 0.f}, sacc2 = {0.f, 0.f, 0.f, 0.f};
  {
    const unsigned qb = L_QA + n * 1024 + (((unsigned)g ^ swz(n)) << 4);
    constexpr int QD = 4;
    v4u qf[QD];
#pragma unroll
    for (int ks = 0; ks < QD; ++ks) qf[ks] = *(const LAS v4u*)(c.L + ((qb ^ (64u * (ks & 3))) + 256u * (ks >> 2)));
    __builtin_amdgcn_sched_barrier(0);
#pragma unroll
    for (int ks = 0; ks < 16; ++ks) {
      if (ks & 1) sacc2 = mfma(kf[ks], qf[ks % QD], sacc2); else sacc = mfma(kf[ks], qf[ks % QD], sacc);
      if (ks + QD < 16) qf[ks % QD] = *(const LAS v4u*)(c.L + ((qb ^ (64u * ((ks + QD) & 3))) + 256u * ((ks + QD) >> 2)));
      __builtin_amdgcn_sched_barrier(0);
    }
  }
  if (l == 1) { asm volatile("" :: "v"(sacc), "v"(sacc2)); STAMP(c, 17); }
  v2u vf[4][4][2];
  auto vf_issue = [&](int u) __attribute__((always_inline)) {
#pragma unroll
    for (int t = 0; t < 4; ++t) {
      const unsigned ad = (ab ^ (32u * t)) + 32768u * u;
      const s16x4 lo = __builtin_amdgcn_ds_read_tr16_b64_v4i16((LAS s16x4*)(c.L + ad));
      const s16x4 hi = __builtin_amdgcn_ds_read_tr16_b64_v4i16((LAS s16x4*)(c.L + ad + 16384u));
      vf[u][t][0] = __builtin_bit_cast(v2u, lo); vf[u][t][1] = __builtin_bit_cast(v2u, hi);
    }
  };
  vf_issue(0); vf_issue(1);
  __builtin_amdgcn_sched_barrier(0);
  {
    const float LOG2E = 1.4426950408889634f;
    float sv[4];
#pragma unroll
    for (int q = 0; q < 4; ++q) sv[q] = score_round(sacc[q] + sacc2[q]) * LOG2E;
    const float kw = ceilf(xg_max(fmaxf(fmaxf(sv[0], sv[1]), fmaxf(sv[2], sv[3]))));
    float p[4];
#pragma unroll
    for (int q = 0; q < 4; ++q) p[q] = __builtin_amdgcn_exp2f(sv[q] - kw);
    const float lw = xg_sum((p[0] + p[1]) + (p[2] + p[3]));
    *(LAS v2u*)(c.L + L_PB + (c.w >> 1) * 1024 + c.lane * 16 + (c.w & 1) * 8) = v2u{pack2(p[0], p[1]), pack2(p[2], p[3])};
    if (g == 0) *(LAS v2u*)(c.L + L_SB + n * 64 + c.w * 8) = v2u{(unsigned)(int)kw, f2u(lw)};
  }
  if (l == 1) STAMP(c, 18);
  LBAR();
  STAMP(c, 3 + 8 * l);
  f32x4 acc[4];
  {
    v4u sb[4], pr[4];
#pragma unroll
    for (int i = 0; i < 4; ++i) sb[i] = *(const LAS v4u*)(c.L + L_SB + n * 64 + i * 16);
#pragma unroll
    for (int u = 0; u < 4; ++u) pr[u] = *(const LAS v4u*)(c.L + L_PB + u * 1024 + c.lane * 16);
    vf_issue(2); vf_issue(3);
    __builtin_amdgcn_sched_barrier(0);
    int K = max(max(max((int)sb[0].x, (int)sb[0].z), max((int)sb[1].x, (int)sb[1].z)), max(max((int)sb[2].x, (int)sb[2].z), max((int)sb[3].x, (int)sb[3].z)));
    if (c.w == 0) {
      float Ls = 0.f;
#pragma unroll
      for (int i = 0; i < 4; ++i) Ls += ldexpf(u2f(sb[i].y), (int)sb[i].x - K) + ldexpf(u2f(sb[i].w), (int)sb[i].z - K);
      if (g == 0) st64(c.rs, OFF_ST + (((l * 2 + c.b) * 32 + 16 * c.G + n) * 64 + c.s) * 8, v2u{canon(f2u((float)K)), canon(f2u(Ls))});
    }
#pragma unroll
    for (int t = 0; t < 4; ++t) acc[t] = f32x4{0.f, 0.f, 0.f, 0.f};
#pragma unroll
    for (int u = 0; u < 4; ++u) {
      const unsigned d0 = __umul24((unsigned)min(K - (int)sb[u].x, 255), 0x00800080u), d1 = __umul24((unsigned)min(K - (int)sb[u].z, 255), 0x00800080u);
      const v4u pb = {subsat(pr[u].x, d0), subsat(pr[u].y, d0), subsat(pr[u].z, d1), subsat(pr[u].w, d1)};
#pragma unroll
      for (int t = 0; t < 4; ++t) {
        const v4u af = {vf[u][t][0].x, vf[u][t][0].y, vf[u][t][1].x, vf[u][t][1].y};
        acc[t] = mfma(pb, af, acc[t]);
      }
    }
  }
  if (l == 1) { asm volatile("" :: "v"(acc[0]), "v"(acc[1]), "v"(acc[2]), "v"(acc[3])); STAMP(c, 19); }
  {
    const int base = OFF_X2 + ((l * 2 + c.b) * 32 + 16 * c.G + 4 * g) * 65536 + c.w * 8192 + c.s * 128 + n * 8;
#pragma unroll
    for (int q = 0; q < 4; ++q)
      st64(c.rs, base + q * 65536, v2u{canon(pack2(acc[0][q], acc[1][q])), canon(pack2(acc[2][q], acc[3][q]))});
  }
  STAMP(c, 4 + 8 * l);
}

DI void fill_attn(Ctx& c) {
  const int n = c.lane & 15, g = c.lane >> 4;
  const v4u sv = {SENT, SENT, SENT, SENT};
#pragma unroll
  for (int l = 0; l < 2; ++l) {
    const int base = OFF_X2 + ((l * 2 + c.b) * 32 + 16 * c.G + (c.lane >> 3)) * 65536 + c.w * 8192 + c.s * 128 + (c.lane & 7) * 16;
    st128(c.rs, base, sv);
    st128(c.rs, base + 8 * 65536, sv);
    if (c.w == 0 && g == 0) st64(c.rs, OFF_ST + (((l * 2 + c.b) * 32 + 16 * c.G + n) * 64 + c.s) * 8, v2u{SENT, SENT});
  }
}

DI void ckv_dma(Ctx& c, const Params& p) {
  const GAS unsigned char* src = (const GAS unsigned char*)p.ckv + ((size_t)c.b * 8192 + c.s * 128 + c.w * 16) * 1024;
#pragma unroll
  for (int i = 0; i < 16; ++i)
    __builtin_amdgcn_global_load_lds((const GAS unsigned*)(src + i * 1024 + (((unsigned)c.lane ^ swz(i)) << 4)),
                                     (LAS unsigned*)(c.L + L_IMG + (c.w * 16 + i) * 1024), 16, 0, 0);
}
DI void ckv_issue(Ctx& c, const Params& p, v4u (&t)[16]) {
  const GAS unsigned char* src = (const GAS unsigned char*)p.ckv + ((size_t)c.b * 8192 + c.s * 128 + c.w * 16) * 1024 + c.lane * 16;
#pragma unroll
  for (int i = 0; i < 16; ++i) t[i] = *(const GAS v4u*)(src + i * 1024);
}
DI void ckv_stage(Ctx& c, const v4u (&t)[16]) {
#pragma unroll
  for (int i = 0; i < 16; ++i)
    *(LAS v4u*)(c.L + L_IMG + (c.w * 16 + i) * 1024 + (((unsigned)c.lane ^ swz(i)) << 4)) = t[i];
}
DI void publish_ready_b(Ctx& c, v4u nonce) {
  LBAR();
}

DI void role_q(Ctx& c, const Params& p, v4u nonce, int h, int ch) {
  asm volatile("; role Q" ::: "memory");
  const int n = c.lane & 15, g = c.lane >> 4;
  const v4u sv = {SENT, SENT, SENT, SENT};
  if (c.w == 0) st128(c.rs, OFF_X1 + (((c.lane >> 5) * 2 + c.b) * 32 + h) * 1024 + ch * 512 + (c.lane & 31) * 16, sv);
  unsigned xp = *(const GAS unsigned*)((const GAS unsigned char*)p.x + c.b * 1024 + 128 * c.w + 4 * (c.lane & 31));
  v4u rq[2][8], ru[8];
  {
    const GAS unsigned char* wq = (const GAS unsigned char*)p.wq + ((size_t)(64 * c.w + 16 * g) * 4096 + h * 128 + 8 * n) * 2;
#pragma unroll
    for (int ks = 0; ks < 2; ++ks)
#pragma unroll
      for (int j = 0; j < 8; ++j) rq[ks][j] = *(const GAS v4u*)(wq + (size_t)(8 * ks + j) * 8192);
    const GAS unsigned char* wu = (const GAS unsigned char*)p.wuk + ((size_t)(256 * ch + 32 * c.w + g) * 4096 + h * 128 + 8 * n) * 2;
#pragma unroll
    for (int i = 0; i < 8; ++i) ru[i] = *(const GAS v4u*)(wu + (size_t)(4 * i) * 8192);
  }
  v4u ct[16];
  __builtin_amdgcn_sched_barrier(0);
  asm volatile("" : "+v"(xp));
  STAMP(c, 9);
  if (c.w == 0 && c.lane == 0) st128(c.rs, OFF_RDY + c.blk * 64, nonce);
  v4u Wq[16];
  const bool xpub = (h == 0 && ch == 0);
  auto q_phase = [&](int l) __attribute__((always_inline)) {
    v4u a0, a1;
    layernorm(c, xp, a0, a1);
    if (l == 0) { ckv_issue(c, p, ct); __builtin_amdgcn_sched_barrier(0); STAMP(c, 7); tr8x8(rq[0], Wq); tr8x8(rq[1], Wq + 8); asm volatile("" :: "v"(Wq[15])); STAMP(c, 22); }
    gemv_ksplit<false>(c, a0, a1, Wq);
    if (l == 0) {
#pragma unroll
      for (int i = 0; i < 8; ++i) { const unsigned row = 4 * i + g; *(LAS v4u*)(c.L + L_IMG + c.w * 8192 + row * 256 + (((unsigned)n ^ swz(row >> 1)) << 4)) = ru[i]; }
#pragma unroll
      for (int t = 0; t < 2; ++t)
#pragma unroll
        for (int ks = 0; ks < 4; ++ks) ru[t * 4 + ks] = *(const LAS v4u*)(c.L + L_IMG + c.w * 8192 + (2 * n + t) * 256 + (((unsigned)(4 * ks + g) ^ swz(n)) << 4));
    }
    reduce128(c);
    if (l == 0) STAMP(c, 23);
    f32x4 acc0 = {0.f, 0.f, 0.f, 0.f}, acc1 = {0.f, 0.f, 0.f, 0.f};
#pragma unroll
    for (int ks = 0; ks < 4; ++ks) {
      const v4u a = *(const LAS v4u*)(c.L + L_VEC + ks * 64 + g * 16);
      acc0 = mfma(a, ru[ks], acc0);
      acc1 = mfma(a, ru[4 + ks], acc1);
    }
    if (l == 0) { while (__builtin_amdgcn_s_memrealtime() - c.t0 < PROBE_T) {} }
    if (c.lane < 16) st32(c.rs, OFF_X1 + ((l * 2 + c.b) * 32 + h) * 1024 + (256 * ch + 32 * c.w + 2 * n) * 2, canon(pack2(acc0.x, acc1.x)));
  };
  q_phase(0);
  STAMP(c, 21);
  wait_ready_a(c, nonce);
  fill_attn(c);
  if (xpub && c.w == 1) st128(c.rs, OFF_X3 + (1 * 2 + c.b) * XV_STR + 32 * 1024 + c.lane * 16, sv);
  ckv_stage(c, ct);
  publish_ready_b(c, nonce);
  STAMP(c, 20);
  STAMP(c, 1);
#pragma unroll 1
  for (int l = 0; l < 2; ++l) {
    attn_phase(c, l);
    if (l == 0) {
      wait_ready_b(c, nonce);
      gate<D_Q4>(c, OFF_X3 + (0 * 2 + c.b) * XV_STR + h * 1024);
      xp = gather33<true>(c, OFF_X4 + (0 * 2 + c.b) * XV_STR, nullptr);
      STAMP(c, 5);
      if (xpub && c.lane < 32) st32(c.rs, OFF_X3 + (1 * 2 + c.b) * XV_STR + 32 * 1024 + 128 * c.w + 4 * c.lane, canon(xp));
      q_phase(1);
      STAMP(c, 6);
    }
  }
  gate<D_Q4>(c, OFF_X3 + (1 * 2 + c.b) * XV_STR + h * 1024);
  if (c.w == 0) {
    const int qi = 2 * h + ch;
    const int off = OFF_X4 + (1 * 2 + c.b) * XV_STR + (c.lane < 33 ? c.lane : 32) * 1024 + qi * 16;
    v4u v;
    for (;;) {
      CBAR();
      v = ld128(c.rs, off);
      if (__all(max(umax3(v.x, v.y, v.z), v.w) != SENT) || !spin_ok(c)) break;
    }
    const unsigned wl = c.lane < 32 ? LO1 : 0u, wh = c.lane < 32 ? HI1 : 0u;
    v4u o;
#pragma unroll
    for (int q = 0; q < 4; ++q) {
      float lo = dot2(v[q], wl, 0.f), hi = dot2(v[q], wh, 0.f);
      lo = quad_sum(lo); hi = quad_sum(hi);
      lo += dppf<0x124>(lo); hi += dppf<0x124>(hi);
      lo += dppf<0x128>(lo); hi += dppf<0x128>(hi);
      lo += dppm<0x142, 0xa>(lo); hi += dppm<0x142, 0xa>(hi);
      const unsigned s2 = (unsigned)__builtin_amdgcn_readlane((int)pack2(lo, hi), 31);
      const unsigned r2 = (unsigned)__builtin_amdgcn_readlane((int)v[q], 32);
      o[q] = pack2(bf_lo(r2) + bf_lo(s2), bf_hi(r2) + bf_hi(s2));
    }
    if (c.lane == 0) *(GAS v4u*)((GAS unsigned char*)p.out + c.b * 1024 + qi * 16) = o;
    STAMP(c, 16);
  }
}

DI void role_o(Ctx& c, const Params& p, v4u nonce, int h) {
  asm volatile("; role O" ::: "memory");
  const int n = c.lane & 15, g = c.lane >> 4;
  const v4u sv = {SENT, SENT, SENT, SENT};
  ckv_dma(c, p);
  DRAIN();
  __builtin_amdgcn_sched_barrier(0);
  v4u rv[2][8], ro[2][8];
  {
    const GAS unsigned char* wv = (const GAS unsigned char*)p.wuv + ((size_t)(64 * c.w + 2 * g) * 4096 + h * 128 + 8 * n) * 2;
#pragma unroll
    for (int ks = 0; ks < 2; ++ks)
#pragma unroll
      for (int j = 0; j < 8; ++j) rv[ks][j] = *(const GAS v4u*)(wv + (size_t)(16 * (j & 3) + 8 * ks + (j >> 2)) * 8192);
    const GAS unsigned char* wo = (const GAS unsigned char*)p.wo + ((size_t)(h * 128 + 32 * (n >> 3) + 8 * g) * 512 + 64 * c.w + 8 * (n & 7)) * 2;
#pragma unroll
    for (int kb = 0; kb < 2; ++kb)
#pragma unroll
      for (int j = 0; j < 8; ++j) ro[kb][j] = *(const GAS v4u*)(wo + (size_t)(64 * kb + j) * 1024);
  }
  __builtin_amdgcn_sched_barrier(0);
  fill_attn(c);
  if (c.w < 2) st128(c.rs, OFF_X3 + (c.w * 2 + c.b) * XV_STR + h * 1024 + c.lane * 16, sv);
  v4u Wv[16], Wo[16];
  tr8x8(rv[0], Wv); tr8x8(rv[1], Wv + 8);
  tr8x8(ro[0], Wo); tr8x8(ro[1], Wo + 8);
  publish_ready_b(c, nonce);
  STAMP(c, 20);
  wait_ready_a(c, nonce);
  STAMP(c, 1);
#pragma unroll 1
  for (int l = 0; l < 2; ++l) {
    attn_phase(c, l);
    if (l == 0) wait_ready_b(c, nonce);
    float inv;
    {
      const int bs = OFF_ST + ((l * 2 + c.b) * 32 + h) * 512 + c.lane * 8;
      v2u st;
      for (;;) {
        CBAR();
        st = ld64(c.rs, bs);
        if (__all(max(st.x, st.y) != SENT) || !spin_ok(c)) break;
      }
      const float kf = u2f(st.x);
      float K = kf;
      K = fmaxf(K, dppf<0x128>(K)); K = fmaxf(K, dppf<0x124>(K)); K = fmaxf(K, dppf<0x122>(K)); K = fmaxf(K, dppf<0x121>(K));
      K = xg_max(K);
      const int d = (int)(K - kf);
      const unsigned eb = d < 127 ? (unsigned)(127 - d) << 7 : 0u;
      float Ls = u2f(st.y) * u2f(eb << 16);
      Ls += dppf<0x128>(Ls); Ls += dppf<0x124>(Ls); Ls += dppf<0x122>(Ls); Ls += dppf<0x121>(Ls);
      Ls = xg_sum(Ls);
      inv = 1.0f / Ls;
      *(LAS unsigned*)(c.L + L_EW + c.w * 256 + c.lane * 4) = eb;
    }
    unsigned el[16];
#pragma unroll
    for (int i = 0; i < 4; ++i) {
      const v4u e4 = *(const LAS v4u*)(c.L + L_EW + c.w * 256 + g * 64 + i * 16);
      el[4 * i] = e4.x; el[4 * i + 1] = e4.y; el[4 * i + 2] = e4.z; el[4 * i + 3] = e4.w;
    }
    const int bx = OFF_X2 + ((l * 2 + c.b) * 32 + h) * 65536 + c.w * 8192 + g * 2048 + n * 8;
    v2u v[16];
    for (;;) {
      CBAR();
#pragma unroll
      for (int i = 0; i < 16; ++i) v[i] = ld64(c.rs, bx + i * 128);
      unsigned m = 0u;
#pragma unroll
      for (int i = 0; i < 16; ++i) m = umax3(m, v[i].x, v[i].y);
      if (__all(m != SENT) || !spin_ok(c)) break;
    }
    STAMP(c, 5 + 8 * l);
    float s0 = 0.f, s1 = 0.f, s2 = 0.f, s3 = 0.f;
#pragma unroll
    for (int i = 0; i < 16; ++i) {
      const unsigned eh = el[i] << 16;
      s0 = dot2(v[i].x, el[i], s0); s1 = dot2(v[i].x, eh, s1); s2 = dot2(v[i].y, el[i], s2); s3 = dot2(v[i].y, eh, s3);
    }
    s0 = xg_sum(s0) * inv; s1 = xg_sum(s1) * inv; s2 = xg_sum(s2) * inv; s3 = xg_sum(s3) * inv;
    const unsigned h01 = pack2(s0, s1), h23 = pack2(s2, s3);
    const unsigned l01 = pack2(dot2(h01, NLO1, s0), dot2(h01, NHI1, s1)), l23 = pack2(dot2(h23, NLO1, s2), dot2(h23, NHI1, s3));
    *(LAS v2u*)(c.L + L_FR + c.w * 256 + (g & 1) * 128 + n * 8) = (g & 1) ? v2u{l01, l23} : v2u{h01, h23};
    const v4u a0 = *(const LAS v4u*)(c.L + L_FR + c.w * 256 + (n & 1) * 128 + g * 16);
    const v4u a1 = *(const LAS v4u*)(c.L + L_FR + c.w * 256 + (n & 1) * 128 + 64 + g * 16);
    asm volatile("" :: "v"(a0), "v"(a1));
    STAMP(c, 7 + 8 * l);
    gemv_ksplit<true>(c, a0, a1, Wv);
    STAMP(c, 22 + 2 * l);
    reduce128(c);
    STAMP(c, 23 + 2 * l);
    gemv_nsplit<2>(c, Wo, OFF_X3 + (l * 2 + c.b) * XV_STR + h * 1024);
    STAMP(c, 6 + 8 * l);
    if (l == 0) gate<D_A1>(c, OFF_X4 + (0 * 2 + c.b) * XV_STR + h * 1024);
  }
}

DI void role_f(Ctx& c, const Params& p, v4u nonce, int j) {
  asm volatile("; role F" ::: "memory");
  const int n = c.lane & 15, g = c.lane >> 4;
  const v4u sv = {SENT, SENT, SENT, SENT};
  ckv_dma(c, p);
  DRAIN();
  __builtin_amdgcn_sched_barrier(0);
  v4u r13[2][8], r2[8];
  {
    const int nn = n < 14 ? n : 13;
    const u16* wsel = nn < 7 ? p.w1 : p.w3;
    const int col = 56 * j + 8 * (nn < 7 ? nn : nn - 7);
    const GAS unsigned char* wa = (const GAS unsigned char*)wsel + ((size_t)(64 * c.w + 16 * g) * 1792 + col) * 2;
#pragma unroll
    for (int ks = 0; ks < 2; ++ks)
#pragma unroll
      for (int jj = 0; jj < 8; ++jj) r13[ks][jj] = *(const GAS v4u*)(wa + (size_t)(8 * ks + jj) * 3584);
#pragma unroll
    for (int jj = 0; jj < 8; ++jj) {
      int f = 32 * (n >> 3) + 8 * g + jj; f = f < 56 ? f : 55;
      r2[jj] = *(const GAS v4u*)((const GAS unsigned char*)p.w2 + ((size_t)(56 * j + f) * 512 + 64 * c.w + 8 * (n & 7)) * 2);
    }
  }
  __builtin_amdgcn_sched_barrier(0);
  fill_attn(c);
  if (c.w < 2) st128(c.rs, OFF_X4 + (c.w * 2 + c.b) * XV_STR + j * 1024 + c.lane * 16, sv);
  if (j == 0 && (c.w == 2 || c.w == 3)) st128(c.rs, OFF_X4 + ((c.w - 2) * 2 + c.b) * XV_STR + 32 * 1024 + c.lane * 16, sv);
  v4u W13[16], W2[8];
  tr8x8(r13[0], W13); tr8x8(r13[1], W13 + 8);
  tr8x8(r2, W2);
  publish_ready_b(c, nonce);
  STAMP(c, 20);
  wait_ready_a(c, nonce);
  STAMP(c, 1);
#pragma unroll 1
  for (int l = 0; l < 2; ++l) {
    attn_phase(c, l);
    unsigned xp;
    if (l == 0) wait_ready_b(c, nonce);
    gate<D_F3>(c, -1);
    if (l == 0) { xp = gather33<false>(c, OFF_X3 + (0 * 2 + c.b) * XV_STR, p.x + c.b * 512); }
    else xp = gather33<true>(c, OFF_X3 + (1 * 2 + c.b) * XV_STR, nullptr);
    STAMP(c, 5 + 8 * l);
    if (j == 0 && c.lane < 32) st32(c.rs, OFF_X4 + (l * 2 + c.b) * XV_STR + 32 * 1024 + 128 * c.w + 4 * c.lane, canon(xp));
    v4u a0, a1;
    layernorm(c, xp, a0, a1);
    STAMP(c, 7 + 8 * l);
    gemv_ksplit<false>(c, a0, a1, W13);
    STAMP(c, 22 + 2 * l);
    LBAR();
    STAMP(c, 8 + 8 * l);
    if (c.w == 0) {
      const int li = c.lane < 56 ? c.lane : 55;
      const unsigned gu = pack2(red_sum8(c, li), red_sum8(c, 56 + li));
      const float gt = bf_lo(gu);
      const float sg = rbf(gt * __builtin_amdgcn_rcpf(1.0f + __expf(-gt)));
      const float hv = c.lane < 56 ? sg * bf_hi(gu) : 0.f;
      *(LAS __bf16*)(c.L + L_VEC + c.lane * 2) = (__bf16)hv;
    }
    LBAR();
    STAMP(c, 23 + 2 * l);
    gemv_nsplit<1>(c, W2, OFF_X4 + (l * 2 + c.b) * XV_STR + j * 1024);
    STAMP(c, 6 + 8 * l);
    if (l == 0) gate<D_FA1>(c, -1);
  }
}

__global__ void __launch_bounds__(NT, 2) mega(Params p) {
  extern __shared__ __attribute__((aligned(16))) unsigned char smem[];
  Ctx c;
  {
    unsigned long long pc0; unsigned clen, cpf;
    asm volatile("s_getpc_b64 %0\n.Lmk_here:\n\ts_mov_b32 %1, .Lmk_end-.Lmk_here" : "=s"(pc0), "=s"(clen));
    const unsigned o = min(threadIdx.x * 128u, clen);
    asm volatile("global_load_dword %0, %1, %2" : "=v"(cpf) : "v"(o), "s"(pc0) : "memory");
    c.cpf = cpf;
  }
  c.rs = __builtin_amdgcn_make_buffer_rsrc((void*)p.ws, 0, WS_BYTES, 0x00020000);
  c.L = (LAS unsigned char*)smem;
  c.lane = threadIdx.x & 63;
  c.w = __builtin_amdgcn_readfirstlane(threadIdx.x >> 6);
  c.blk = blockIdx.x;
  const int x = c.blk & 7, slot = c.blk >> 3;
  c.b = (slot >> 3) & 1; c.G = slot >> 4; c.s = 8 * x + (slot & 7);
  const int r = 8 * c.G + (slot & 7);
  c.spins = 0; c.wsp = p.ws;
  c.kLO1 = sconst(0x00003F80u); c.kHI1 = sconst(0x3F800000u); c.kONES = sconst(0x3F803F80u); c.kNLO1 = sconst(0x0000BF80u); c.kNHI1 = sconst(0xBF800000u);
  STAMP(c, 0);
  const unsigned long long id = llvm_dispatch_id();
  const unsigned long long dp = (unsigned long long)__builtin_amdgcn_dispatch_ptr();
  const v4u nonce = {(unsigned)id ^ 0x5bd1e995u, (unsigned)(id >> 32) ^ 0x9e3779b9u, (unsigned)dp ^ 0x85ebca6bu, (unsigned)(dp >> 32) ^ 0xc2b2ae35u};
  c.nonce = nonce; c.t0 = __builtin_amdgcn_s_memrealtime();
  if (r < 8) role_q(c, p, nonce, 4 * x + (r >> 1), r & 1);
  else if (r < 12) role_o(c, p, nonce, 4 * x + (r - 8));
  else role_f(c, p, nonce, 4 * x + (r - 12));
  asm volatile(".Lmk_end:" ::: "memory");
  if (c.w == 0 && c.lane < 2) st128(c.rs, OFF_RDY + c.blk * 64 + c.lane * 16, v4u{~nonce.x, ~nonce.y, ~nonce.z, ~nonce.w});
}
}

extern "C" void kernel_launch(void* const* d_in, const int* in_sizes, int n_in,
                              void* d_out, int out_size, void* d_ws, size_t ws_size,
                              hipStream_t stream) {
  mk::Params p;
  p.x = (const u16*)d_in[0]; p.ckv = (const u16*)d_in[1]; p.wq = (const u16*)d_in[2]; p.wuk = (const u16*)d_in[3];
  p.wuv = (const u16*)d_in[4]; p.wo = (const u16*)d_in[5]; p.w1 = (const u16*)d_in[7]; p.w3 = (const u16*)d_in[8];
  p.w2 = (const u16*)d_in[9]; p.out = (u16*)d_out; p.ws = (unsigned char*)d_ws;
  (void)hipFuncSetAttribute((const void*)mk::mega, hipFuncAttributeMaxDynamicSharedMemorySize, mk::L_BYTES + 2048);
  hipLaunchKernelGGL(mk::mega, dim3(256), dim3(mk::NT), mk::L_BYTES + 2048, stream, p);
}
```

```cpp
#include <hip/hip_runtime.h>
#include <hip/hip_bf16.h>
#include <stdint.h>

#define LAS __attribute__((address_space(3)))
#define GAS __attribute__((address_space(1)))
typedef unsigned short u16;
typedef unsigned v4u __attribute__((ext_vector_type(4)));
typedef unsigned v2u __attribute__((ext_vector_type(2)));
typedef float f32x4 __attribute__((ext_vector_type(4)));
typedef float f32x2 __attribute__((ext_vector_type(2)));
typedef short bf16x8 __attribute__((ext_vector_type(8)));
typedef short s16x4 __attribute__((ext_vector_type(4)));
typedef __bf16 bf16x2_t __attribute__((ext_vector_type(2)));
typedef unsigned short u16x2 __attribute__((ext_vector_type(2)));
#define DI __device__ __forceinline__

extern "C" __device__ unsigned long long llvm_dispatch_id() __asm("llvm.amdgcn.dispatch.id");

#define OPT_PF 1
#define OPT_DMA 1
#define OPT_CO 1
#define OPT_RDY 1
namespace mk {
constexpr int NT = 512;
constexpr unsigned SENT = 0xFFFFFFFFu;
constexpr unsigned SPIN_CAP = 1u << 14;
constexpr int AUX_SC1 = 16;
#define LO1 (c.kLO1)
#define HI1 (c.kHI1)
#define ONES (c.kONES)
#define NLO1 (c.kNLO1)
#define NHI1 (c.kNHI1)

constexpr int OFF_RDY = 0;
constexpr int OFF_X1 = 16384;
constexpr int OFF_ST = OFF_X1 + 2 * 2 * 32 * 1024;
constexpr int OFF_X3 = OFF_ST + 2 * 2 * 32 * 512;
constexpr int XV_STR = 34 * 1024;
constexpr int OFF_X4 = OFF_X3 + 4 * XV_STR;
constexpr int OFF_X2 = OFF_X4 + 4 * XV_STR;
constexpr int WS_BYTES = OFF_X2 + 2 * 2 * 32 * 65536;

constexpr int L_IMG = 0;
constexpr int L_QA = 131072;
constexpr int L_PB = L_QA + 16384;
constexpr int L_SB = L_PB + 4096;
constexpr int L_LN = L_SB + 1024;
constexpr int L_VEC = L_LN + 64;
constexpr int L_EW = L_VEC + 256;
constexpr int L_FR = L_EW + 2048;
constexpr int L_BYTES = L_FR + 2048;
constexpr int L_RED = L_QA;

struct Params {
  const u16* x; const u16* ckv; const u16* wq; const u16* wuk; const u16* wuv; const u16* wo;
  const u16* w1; const u16* w3; const u16* w2; u16* out; unsigned char* ws;
};

DI float u2f(unsigned u) { return __builtin_bit_cast(float, u); }
DI unsigned f2u(float f) { return __builtin_bit_cast(unsigned, f); }
DI float bf_lo(unsigned u) { return u2f(u << 16); }
DI float bf_hi(unsigned u) { return u2f(u & 0xFFFF0000u); }
DI float rbf(float f) { return (float)(__bf16)f; }
DI unsigned pack2(float lo, float hi) { bf16x2_t v = {(__bf16)lo, (__bf16)hi}; return __builtin_bit_cast(unsigned, v); }
DI unsigned canon(unsigned u) { return u < 0xFFFFFFFEu ? u : 0xFFFFFFFEu; }
DI float dot2(unsigned a, unsigned b, float c) { return __builtin_amdgcn_fdot2_f32_bf16(__builtin_bit_cast(bf16x2_t, a), __builtin_bit_cast(bf16x2_t, b), c, false); }
DI unsigned subsat(unsigned a, unsigned b) { return __builtin_bit_cast(unsigned, __builtin_elementwise_sub_sat(__builtin_bit_cast(u16x2, a), __builtin_bit_cast(u16x2, b))); }
DI unsigned umax3(unsigned a, unsigned b, unsigned c) { return max(max(a, b), c); }
DI f32x4 mfma(v4u a, v4u b, f32x4 c) { return __builtin_amdgcn_mfma_f32_16x16x32_bf16(__builtin_bit_cast(bf16x8, a), __builtin_bit_cast(bf16x8, b), c, 0, 0, 0); }
template <int CTRL> DI float dppf(float v) { return u2f((unsigned)__builtin_amdgcn_update_dpp(0, (int)f2u(v), CTRL, 0xf, 0xf, true)); }
template <int CTRL> DI unsigned dppu(unsigned v) { return (unsigned)__builtin_amdgcn_update_dpp(0, (int)v, CTRL, 0xf, 0xf, true); }
template <int CTRL, int RM> DI float dppm(float v) { return u2f((unsigned)__builtin_amdgcn_update_dpp(0, (int)f2u(v), CTRL, RM, 0xf, false)); }
DI float quad_sum(float v) { v += dppf<0xB1>(v); v += dppf<0x4E>(v); return v; }
DI float xg_sum(float v) {
  const auto a = __builtin_amdgcn_permlane16_swap(f2u(v), f2u(v), false, false);
  const unsigned a0 = a[0], a1 = a[1];
  v = u2f(a0) + u2f(a1);
  const auto b = __builtin_amdgcn_permlane32_swap(f2u(v), f2u(v), false, false);
  const unsigned b0 = b[0], b1 = b[1];
  return u2f(b0) + u2f(b1);
}
DI float x32_sum(float v) {
  const auto b = __builtin_amdgcn_permlane32_swap(f2u(v), f2u(v), false, false);
  const unsigned b0 = b[0], b1 = b[1];
  return u2f(b0) + u2f(b1);
}
DI float xg_max(float v) {
  const auto a = __builtin_amdgcn_permlane16_swap(f2u(v), f2u(v), false, false);
  const unsigned a0 = a[0], a1 = a[1];
  v = fmaxf(u2f(a0), u2f(a1));
  const auto b = __builtin_amdgcn_permlane32_swap(f2u(v), f2u(v), false, false);
  const unsigned b0 = b[0], b1 = b[1];
  return fmaxf(u2f(b0), u2f(b1));
}
DI unsigned swz(unsigned r) { return (((r ^ ((r >> 1) & 4u)) & 7u) << 1) | ((r >> 3) & 1u); }

typedef __amdgpu_buffer_rsrc_t rsrc_t;
DI v4u ld128(rsrc_t rs, int off) { return __builtin_amdgcn_raw_buffer_load_b128(rs, off, 0, AUX_SC1); }
DI v2u ld64(rsrc_t rs, int off) { return __builtin_amdgcn_raw_buffer_load_b64(rs, off, 0, AUX_SC1); }
DI unsigned ld32(rsrc_t rs, int off) { return __builtin_amdgcn_raw_buffer_load_b32(rs, off, 0, AUX_SC1); }
DI void st128(rsrc_t rs, int off, v4u v) { __builtin_amdgcn_raw_buffer_store_b128(v, rs, off, 0, AUX_SC1); }
DI void st64(rsrc_t rs, int off, v2u v) { __builtin_amdgcn_raw_buffer_store_b64(v, rs, off, 0, AUX_SC1); }
DI void st32(rsrc_t rs, int off, unsigned v) { __builtin_amdgcn_raw_buffer_store_b32(v, rs, off, 0, AUX_SC1); }
#define CBAR() asm volatile("" ::: "memory")
#define DRAIN() asm volatile("s_waitcnt vmcnt(0)" ::: "memory")
#define LBAR() do { asm volatile("s_waitcnt lgkmcnt(0)" ::: "memory"); __builtin_amdgcn_s_barrier(); asm volatile("" ::: "memory"); } while (0)

DI void ld_hidden(v4u& r, unsigned voff, const u16* base, unsigned ioff) {
  const unsigned long long sb = (unsigned long long)base + ioff;
  asm volatile("global_load_dwordx4 %0, %1, %2" : "+v"(r) : "v"(voff), "s"(sb) : "memory");
}
DI void tr8x8(const v4u (&r)[8], v4u* f) {
#pragma unroll
  for (int q = 0; q < 4; ++q)
#pragma unroll
    for (int d = 0; d < 4; ++d) {
      const unsigned a = r[2 * d][q], b = r[2 * d + 1][q];
      f[2 * q][d] = __builtin_amdgcn_perm(b, a, 0x05040100u);
      f[2 * q + 1][d] = __builtin_amdgcn_perm(b, a, 0x07060302u);
    }
}
DI void tr8x4(const v2u (&r)[8], v4u* f) {
#pragma unroll
  for (int q = 0; q < 2; ++q)
#pragma unroll
    for (int d = 0; d < 4; ++d) {
      const unsigned a = r[2 * d][q], b = r[2 * d + 1][q];
      f[2 * q][d] = __builtin_amdgcn_perm(b, a, 0x05040100u);
      f[2 * q + 1][d] = __builtin_amdgcn_perm(b, a, 0x07060302u);
    }
}

#define STAMP(c, i) do {} while (0)
struct Ctx {
  unsigned char* wsp;
  rsrc_t rs; LAS unsigned char* L;
  int lane, w, b, G, s, blk;
  unsigned spins;
  unsigned kLO1, kHI1, kONES, kNLO1, kNHI1;
  unsigned cpf;
  v4u nonce;
};
DI unsigned sconst(unsigned v) { unsigned r; asm volatile("s_mov_b32 %0, %1" : "=s"(r) : "i"(v)); return r; }
#define RELANE(c) do { int l_ = (c).lane; asm volatile("" : "+v"(l_)); (c).lane = l_; } while (0)
DI bool spin_ok(Ctx& c) { return ++c.spins < SPIN_CAP; }

DI int q_blk(int b, int h, int ch) { return (h >> 2) + 8 * (8 * b + 2 * (h & 3) + ch); }

DI bool nonce_eq(v4u a, v4u n) { return ((a.x ^ n.x) | (a.y ^ n.y) | (a.z ^ n.z) | (a.w ^ n.w)) == 0u; }
DI void wait_ready_a(Ctx& c, v4u nonce) {
  const int q = c.lane & 3;
  const int off = OFF_RDY + q_blk(c.b, 16 * c.G + c.w + 8 * (q >> 1), q & 1) * 64;
  for (;;) {
    CBAR();
    const v4u a = ld128(c.rs, off);
    if (__all(nonce_eq(a, nonce)) || !spin_ok(c)) break;
  }
  CBAR();
}
DI void wait_ready_b(Ctx& c, v4u nonce) {
  const int o0 = OFF_RDY + (c.b * 64 + c.lane) * 64 + 16, o1 = o0 + 128 * 64;
  for (;;) {
    CBAR();
    const v4u a = ld128(c.rs, o0), bq = ld128(c.rs, o1);
    if (__all(nonce_eq(a, nonce) & nonce_eq(bq, nonce)) || !spin_ok(c)) break;
  }
  CBAR();
}

template <int D>
DI void gate(Ctx& c, int off) {
  if (c.w == 0) {
    if (off >= 0) {
      for (;;) {
        CBAR();
        const unsigned v = __builtin_amdgcn_raw_buffer_load_b32(c.rs, off, 0, AUX_SC1);
        if (__builtin_amdgcn_readfirstlane(v) != SENT || !spin_ok(c)) break;
        __builtin_amdgcn_s_sleep(4);
      }
    }
    if (D > 0) __builtin_amdgcn_s_sleep(D);
  }
  __builtin_amdgcn_s_barrier();
}
#define D_F3 40
#define D_Q4 30
#define D_A1 30
#define D_FA1 45

template <bool RES_WS>
DI unsigned gather33(Ctx& c, int base, const u16* xin) {
  const int co = 128 * c.w + 4 * (c.lane & 31);
  unsigned v[16], res;
  if (!RES_WS) res = *(const GAS unsigned*)((const GAS unsigned char*)xin + co);
  const int o = base + (c.lane >> 5) * 1024 + co;
  for (;;) {
    CBAR();
#pragma unroll
    for (int i = 0; i < 16; ++i) v[i] = ld32(c.rs, o + i * 2048);
    if (RES_WS) res = ld32(c.rs, base + 32 * 1024 + co);
    unsigned m = RES_WS ? res : 0u;
#pragma unroll
    for (int i = 0; i < 16; i += 2) m = umax3(m, v[i], v[i + 1]);
    if (__all(m != SENT) || !spin_ok(c)) break;
  }
  float s0 = 0.f, s1 = 0.f, s2 = 0.f, s3 = 0.f;
#pragma unroll
  for (int i = 0; i < 16; i += 2) { s0 = dot2(v[i], LO1, s0); s1 = dot2(v[i], HI1, s1); s2 = dot2(v[i + 1], LO1, s2); s3 = dot2(v[i + 1], HI1, s3); }
  const unsigned o01 = pack2(x32_sum(s0 + s2), x32_sum(s1 + s3));
  return pack2(dot2(o01, LO1, bf_lo(res)), dot2(o01, HI1, bf_hi(res)));
}

DI void layernorm(Ctx& c, unsigned xp, v4u& a0, v4u& a1) {
  float t1 = dot2(xp, ONES, 0.f), t2 = dot2(xp, xp, 0.f);
  t1 += dppf<0x121>(t1); t2 += dppf<0x121>(t2);
  t1 += dppf<0x122>(t1); t2 += dppf<0x122>(t2);
  t1 += dppf<0x124>(t1); t2 += dppf<0x124>(t2);
  t1 += dppf<0x128>(t1); t2 += dppf<0x128>(t2);
  t1 += dppm<0x142, 0xa>(t1); t2 += dppm<0x142, 0xa>(t2);
  if (c.lane == 31) *(LAS f32x2*)(c.L + L_LN + c.w * 8) = f32x2{t1, t2};
  LBAR();
  f32x4 t[4];
#pragma unroll
  for (int i = 0; i < 4; ++i) t[i] = *(const LAS f32x4*)(c.L + L_LN + i * 16);
  const f32x4 tt = (t[0] + t[1]) + (t[2] + t[3]);
  const float mean = (tt.x + tt.z) * (1.0f / 512.0f);
  const float var = fmaxf((tt.y + tt.w) * (1.0f / 512.0f) - mean * mean, 0.f);
  const unsigned mv = pack2(mean, var);
  const float nmb = -bf_lo(mv);
  const float sd = rbf(__builtin_amdgcn_sqrtf(rbf(bf_hi(mv) + 1e-5f)));
  const float ri = __builtin_amdgcn_rcpf(sd);
  const unsigned d01 = pack2(dot2(xp, LO1, nmb), dot2(xp, HI1, nmb));
  const unsigned n01 = pack2(bf_lo(d01) * ri, bf_hi(d01) * ri);
  *(LAS unsigned*)(c.L + L_FR + c.w * 256 + c.lane * 4) = n01;
  a0 = *(const LAS v4u*)(c.L + L_FR + c.w * 256 + (c.lane >> 4) * 32);
  a1 = *(const LAS v4u*)(c.L + L_FR + c.w * 256 + (c.lane >> 4) * 32 + 16);
}

template <bool HILO>
DI void gemv_ksplit(Ctx& c, v4u a0, v4u a1, const v4u* W) {
  f32x4 acc[8];
#pragma unroll
  for (int t = 0; t < 8; ++t) acc[t] = mfma(a0, W[t], f32x4{0.f, 0.f, 0.f, 0.f});
#pragma unroll
  for (int t = 0; t < 8; ++t) acc[t] = mfma(a1, W[8 + t], acc[t]);
  if (c.lane < 16) {
    f32x4 o0, o1;
#pragma unroll
    for (int t = 0; t < 4; ++t) { o0[t] = HILO ? acc[t].x + acc[t].y : acc[t].x; o1[t] = HILO ? acc[4 + t].x + acc[4 + t].y : acc[4 + t].x; }
    *(LAS f32x4*)(c.L + L_RED + c.w * 512 + c.lane * 32) = o0;
    *(LAS f32x4*)(c.L + L_RED + c.w * 512 + c.lane * 32 + 16) = o1;
  }
}
DI float red_sum8(Ctx& c, int idx) {
  const LAS float* R = (const LAS float*)(c.L + L_RED);
  return ((R[idx] + R[128 + idx]) + (R[256 + idx] + R[384 + idx])) + ((R[512 + idx] + R[640 + idx]) + (R[768 + idx] + R[896 + idx]));
}
DI void reduce128(Ctx& c) {
  LBAR();
  if (c.w < 2) { const int d = c.w * 64 + c.lane; *(LAS __bf16*)(c.L + L_VEC + d * 2) = (__bf16)red_sum8(c, d); }
  LBAR();
}

template <int KB>
DI void gemv_nsplit(Ctx& c, const v4u* W, int off) {
  const int n = c.lane & 15, g = c.lane >> 4;
  f32x4 acc[8];
#pragma unroll
  for (int kb = 0; kb < KB; ++kb) {
    const v4u a = *(const LAS v4u*)(c.L + L_VEC + kb * 128 + (n & 1) * 64 + g * 16);
#pragma unroll
    for (int t = 0; t < 8; ++t) acc[t] = mfma(a, W[8 * kb + t], kb == 0 ? f32x4{0.f, 0.f, 0.f, 0.f} : acc[t]);
  }
  float r[8];
#pragma unroll
  for (int t = 0; t < 8; ++t) r[t] = acc[t].x + dppf<0x108>(acc[t].y);
  if (c.lane < 8) st128(c.rs, off + 128 * c.w + 16 * c.lane, v4u{canon(pack2(r[0], r[1])), canon(pack2(r[2], r[3])), canon(pack2(r[4], r[5])), canon(pack2(r[6], r[7]))});
}

DI float score_round(float s) {
  const float SC = 0.08838834764831845f;
  const unsigned bits = f2u(s);
  const unsigned tr = bits & 0xFFFF0000u;
  const unsigned g2 = pack2(u2f(tr) * SC, u2f(tr + 0x10000u) * SC);
  const float ga = bf_lo(g2);
  const float wgt = fminf(fmaxf((float)(bits & 0xFFFFu) * (1.0f / (65536.0f * 0.6f)) + (0.5f - 0.5f / 0.6f), 0.f), 1.f);
  return ga + wgt * (bf_hi(g2) - ga);
}

struct NoHook { DI void operator()() const {} };
template <class Hook>
DI void attn_phase(Ctx& c, int l, Hook hook) {
  RELANE(c);
  const int tid = c.w * 64 + c.lane, n = c.lane & 15, g = c.lane >> 4;
  v4u kf[16];
  {
    const unsigned kb = L_IMG + (c.w * 16 + n) * 1024 + (((unsigned)g ^ swz(n)) << 4);
#pragma unroll
    for (int ks = 0; ks < 16; ++ks) kf[ks] = *(const LAS v4u*)(c.L + ((kb ^ (64u * (ks & 3))) + 256u * (ks >> 2)));
  }
  unsigned ab;
  {
    const unsigned q4 = (c.lane & 15) >> 2, p4 = c.lane & 3;
    const unsigned key = 4u * g + q4;
    const unsigned chunk = 8u * c.w + (p4 >> 1);
    ab = L_IMG + key * 1024 + ((chunk ^ swz(key)) << 4) + 8u * (p4 & 1);
    asm volatile("" : "+v"(ab));
  }
  {
    const int base = OFF_X1 + ((l * 2 + c.b) * 32 + 16 * c.G) * 1024 + tid * 16;
    v4u v0, v1;
    for (;;) {
      CBAR();
      v0 = ld128(c.rs, base);
      v1 = ld128(c.rs, base + 8192);
      const unsigned m = max(umax3(umax3(v0.x, v0.y, v0.z), v0.w, v1.x), umax3(v1.y, v1.z, v1.w));
      if (__all(m != SENT) || !spin_ok(c)) break;
    }
    STAMP(c, 2 + 8 * l);
    asm volatile("" :: "v"(c.cpf));
    hook();
    *(LAS v4u*)(c.L + L_QA + c.w * 1024 + (((unsigned)c.lane ^ swz(c.w)) << 4)) = v0;
    *(LAS v4u*)(c.L + L_QA + (c.w + 8) * 1024 + (((unsigned)c.lane ^ swz(c.w + 8)) << 4)) = v1;
  }
  LBAR();
  if (l == 0 && c.w == 0 && c.lane == 0) st128(c.rs, OFF_RDY + c.blk * 64 + 16, c.nonce);
  STAMP(c, 26 + l);
  f32x4 sacc = {0.f, 0.f, 0.f, 0.f}, sacc2 = {0.f, 0.f, 0.f, 0.f};
  {
    const unsigned qb = L_QA + n * 1024 + (((unsigned)g ^ swz(n)) << 4);
    constexpr int QD = 4;
    v4u qf[QD];
#pragma unroll
    for (int ks = 0; ks < QD; ++ks) qf[ks] = *(const LAS v4u*)(c.L + ((qb ^ (64u * (ks & 3))) + 256u * (ks >> 2)));
    __builtin_amdgcn_sched_barrier(0);
#pragma unroll
    for (int ks = 0; ks < 16; ++ks) {
      if (ks & 1) sacc2 = mfma(kf[ks], qf[ks % QD], sacc2); else sacc = mfma(kf[ks], qf[ks % QD], sacc);
      if (ks + QD < 16) qf[ks % QD] = *(const LAS v4u*)(c.L + ((qb ^ (64u * ((ks + QD) & 3))) + 256u * ((ks + QD) >> 2)));
      __builtin_amdgcn_sched_barrier(0);
    }
  }
  if (l == 1) { asm volatile("" :: "v"(sacc), "v"(sacc2)); STAMP(c, 17); }
  v2u vf[4][4][2];
  auto vf_issue = [&](int u) __attribute__((always_inline)) {
#pragma unroll
    for (int t = 0; t < 4; ++t) {
      const unsigned ad = (ab ^ (32u * t)) + 32768u * u;
      const s16x4 lo = __builtin_amdgcn_ds_read_tr16_b64_v4i16((LAS s16x4*)(c.L + ad));
      const s16x4 hi = __builtin_amdgcn_ds_read_tr16_b64_v4i16((LAS s16x4*)(c.L + ad + 16384u));
      vf[u][t][0] = __builtin_bit_cast(v2u, lo); vf[u][t][1] = __builtin_bit_cast(v2u, hi);
    }
  };
  vf_issue(0); vf_issue(1);
  __builtin_amdgcn_sched_barrier(0);
  {
    const float LOG2E = 1.4426950408889634f;
    float sv[4];
#pragma unroll
    for (int q = 0; q < 4; ++q) sv[q] = score_round(sacc[q] + sacc2[q]) * LOG2E;
    const float kw = ceilf(xg_max(fmaxf(fmaxf(sv[0], sv[1]), fmaxf(sv[2], sv[3]))));
    float p[4];
#pragma unroll
    for (int q = 0; q < 4; ++q) p[q] = __builtin_amdgcn_exp2f(sv[q] - kw);
    const float lw = xg_sum((p[0] + p[1]) + (p[2] + p[3]));
    *(LAS v2u*)(c.L + L_PB + (c.w >> 1) * 1024 + c.lane * 16 + (c.w & 1) * 8) = v2u{pack2(p[0], p[1]), pack2(p[2], p[3])};
    if (g == 0) *(LAS v2u*)(c.L + L_SB + n * 64 + c.w * 8) = v2u{(unsigned)(int)kw, f2u(lw)};
  }
  if (l == 1) STAMP(c, 18);
  LBAR();
  STAMP(c, 3 + 8 * l);
  f32x4 acc[4];
  {
    v4u sb[4], pr[4];
#pragma unroll
    for (int i = 0; i < 4; ++i) sb[i] = *(const LAS v4u*)(c.L + L_SB + n * 64 + i * 16);
#pragma unroll
    for (int u = 0; u < 4; ++u) pr[u] = *(const LAS v4u*)(c.L + L_PB + u * 1024 + c.lane * 16);
    vf_issue(2); vf_issue(3);
    __builtin_amdgcn_sched_barrier(0);
    int K = max(max(max((int)sb[0].x, (int)sb[0].z), max((int)sb[1].x, (int)sb[1].z)), max(max((int)sb[2].x, (int)sb[2].z), max((int)sb[3].x, (int)sb[3].z)));
    if (c.w == 0) {
      float Ls = 0.f;
#pragma unroll
      for (int i = 0; i < 4; ++i) Ls += ldexpf(u2f(sb[i].y), (int)sb[i].x - K) + ldexpf(u2f(sb[i].w), (int)sb[i].z - K);
      if (g == 0) st64(c.rs, OFF_ST + (((l * 2 + c.b) * 32 + 16 * c.G + n) * 64 + c.s) * 8, v2u{canon(f2u((float)K)), canon(f2u(Ls))});
    }
#pragma unroll
    for (int t = 0; t < 4; ++t) acc[t] = f32x4{0.f, 0.f, 0.f, 0.f};
#pragma unroll
    for (int u = 0; u < 4; ++u) {
      const unsigned d0 = __umul24((unsigned)min(K - (int)sb[u].x, 255), 0x00800080u), d1 = __umul24((unsigned)min(K - (int)sb[u].z, 255), 0x00800080u);
      const v4u pb = {subsat(pr[u].x, d0), subsat(pr[u].y, d0), subsat(pr[u].z, d1), subsat(pr[u].w, d1)};
#pragma unroll
      for (int t = 0; t < 4; ++t) {
        const v4u af = {vf[u][t][0].x, vf[u][t][0].y, vf[u][t][1].x, vf[u][t][1].y};
        acc[t] = mfma(pb, af, acc[t]);
      }
    }
  }
  if (l == 1) { asm volatile("" :: "v"(acc[0]), "v"(acc[1]), "v"(acc[2]), "v"(acc[3])); STAMP(c, 19); }
  {
    const int base = OFF_X2 + ((l * 2 + c.b) * 32 + 16 * c.G + 4 * g) * 65536 + c.w * 8192 + c.s * 128 + n * 8;
#pragma unroll
    for (int q = 0; q < 4; ++q)
      st64(c.rs, base + q * 65536, v2u{canon(pack2(acc[0][q], acc[1][q])), canon(pack2(acc[2][q], acc[3][q]))});
  }
  STAMP(c, 4 + 8 * l);
}

DI void fill_attn(Ctx& c) {
  const int n = c.lane & 15, g = c.lane >> 4;
  const v4u sv = {SENT, SENT, SENT, SENT};
#pragma unroll
  for (int l = 0; l < 2; ++l) {
    const int base = OFF_X2 + ((l * 2 + c.b) * 32 + 16 * c.G + (c.lane >> 3)) * 65536 + c.w * 8192 + c.s * 128 + (c.lane & 7) * 16;
    st128(c.rs, base, sv);
    st128(c.rs, base + 8 * 65536, sv);
    if (c.w == 0 && g == 0) st64(c.rs, OFF_ST + (((l * 2 + c.b) * 32 + 16 * c.G + n) * 64 + c.s) * 8, v2u{SENT, SENT});
  }
}

DI void ckv_dma(Ctx& c, const Params& p) {
  const GAS unsigned char* src = (const GAS unsigned char*)p.ckv + ((size_t)c.b * 8192 + c.s * 128 + c.w * 16) * 1024;
#pragma unroll
  for (int i = 0; i < 16; ++i)
    __builtin_amdgcn_global_load_lds((const GAS unsigned*)(src + i * 1024 + (((unsigned)c.lane ^ swz(i)) << 4)),
                                     (LAS unsigned*)(c.L + L_IMG + (c.w * 16 + i) * 1024), 16, 0, 0);
}
DI void ckv_issue(Ctx& c, const Params& p, v4u (&t)[16]) {
  const GAS unsigned char* src = (const GAS unsigned char*)p.ckv + ((size_t)c.b * 8192 + c.s * 128 + c.w * 16) * 1024 + c.lane * 16;
#pragma unroll
  for (int i = 0; i < 16; ++i) t[i] = *(const GAS v4u*)(src + i * 1024);
}
DI void ckv_stage(Ctx& c, const v4u (&t)[16]) {
#pragma unroll
  for (int i = 0; i < 16; ++i)
    *(LAS v4u*)(c.L + L_IMG + (c.w * 16 + i) * 1024 + (((unsigned)c.lane ^ swz(i)) << 4)) = t[i];
}
DI void publish_ready_b(Ctx& c, v4u nonce) {
  LBAR();
}

DI void role_q(Ctx& c, const Params& p, v4u nonce, int h, int ch) {
  asm volatile("; role Q" ::: "memory");
  const int n = c.lane & 15, g = c.lane >> 4;
  const v4u sv = {SENT, SENT, SENT, SENT};
  if (c.w == 0) st128(c.rs, OFF_X1 + (((c.lane >> 5) * 2 + c.b) * 32 + h) * 1024 + ch * 512 + (c.lane & 31) * 16, sv);
  unsigned xp = *(const GAS unsigned*)((const GAS unsigned char*)p.x + c.b * 1024 + 128 * c.w + 4 * (c.lane & 31));
  v4u rq[2][8], ru[8];
  {
    const GAS unsigned char* wq = (const GAS unsigned char*)p.wq + ((size_t)(64 * c.w + 16 * g) * 4096 + h * 128 + 8 * n) * 2;
#pragma unroll
    for (int ks = 0; ks < 2; ++ks)
#pragma unroll
      for (int j = 0; j < 8; ++j) rq[ks][j] = *(const GAS v4u*)(wq + (size_t)(8 * ks + j) * 8192);
    const GAS unsigned char* wu = (const GAS unsigned char*)p.wuk + ((size_t)(256 * ch + 32 * c.w + g) * 4096 + h * 128 + 8 * n) * 2;
#pragma unroll
    for (int i = 0; i < 8; ++i) ru[i] = *(const GAS v4u*)(wu + (size_t)(4 * i) * 8192);
  }
  v4u ct[16];
  __builtin_amdgcn_sched_barrier(0);
  asm volatile("" : "+v"(xp));
  STAMP(c, 9);
  if (c.w == 0 && c.lane == 0) st128(c.rs, OFF_RDY + c.blk * 64, nonce);
  v4u Wq[16];
  const bool xpub = (h == 0 && ch == 0);
  auto q_phase = [&](int l) __attribute__((always_inline)) {
    v4u a0, a1;
    layernorm(c, xp, a0, a1);
    if (l == 0) { ckv_issue(c, p, ct); __builtin_amdgcn_sched_barrier(0); STAMP(c, 7); tr8x8(rq[0], Wq); tr8x8(rq[1], Wq + 8); asm volatile("" :: "v"(Wq[15])); STAMP(c, 22); }
    gemv_ksplit<false>(c, a0, a1, Wq);
    if (l == 0) {
#pragma unroll
      for (int i = 0; i < 8; ++i) { const unsigned row = 4 * i + g; *(LAS v4u*)(c.L + L_IMG + c.w * 8192 + row * 256 + (((unsigned)n ^ swz(row >> 1)) << 4)) = ru[i]; }
#pragma unroll
      for (int t = 0; t < 2; ++t)
#pragma unroll
        for (int ks = 0; ks < 4; ++ks) ru[t * 4 + ks] = *(const LAS v4u*)(c.L + L_IMG + c.w * 8192 + (2 * n + t) * 256 + (((unsigned)(4 * ks + g) ^ swz(n)) << 4));
    }
    reduce128(c);
    if (l == 0) STAMP(c, 23);
    f32x4 acc0 = {0.f, 0.f, 0.f, 0.f}, acc1 = {0.f, 0.f, 0.f, 0.f};
#pragma unroll
    for (int ks = 0; ks < 4; ++ks) {
      const v4u a = *(const LAS v4u*)(c.L + L_VEC + ks * 64 + g * 16);
      acc0 = mfma(a, ru[ks], acc0);
      acc1 = mfma(a, ru[4 + ks], acc1);
    }
    if (c.lane < 16) st32(c.rs, OFF_X1 + ((l * 2 + c.b) * 32 + h) * 1024 + (256 * ch + 32 * c.w + 2 * n) * 2, canon(pack2(acc0.x, acc1.x)));
  };
  q_phase(0);
  STAMP(c, 21);
  wait_ready_a(c, nonce);
  fill_attn(c);
  if (xpub && c.w == 1) st128(c.rs, OFF_X3 + (1 * 2 + c.b) * XV_STR + 32 * 1024 + c.lane * 16, sv);
  ckv_stage(c, ct);
  publish_ready_b(c, nonce);
  STAMP(c, 20);
  STAMP(c, 1);
#pragma unroll 1
  for (int l = 0; l < 2; ++l) {
    attn_phase(c, l, NoHook());
    if (l == 0) {
      wait_ready_b(c, nonce);
      gate<D_Q4>(c, OFF_X3 + (0 * 2 + c.b) * XV_STR + h * 1024);
      xp = gather33<true>(c, OFF_X4 + (0 * 2 + c.b) * XV_STR, nullptr);
      STAMP(c, 5);
      if (xpub && c.lane < 32) st32(c.rs, OFF_X3 + (1 * 2 + c.b) * XV_STR + 32 * 1024 + 128 * c.w + 4 * c.lane, canon(xp));
      q_phase(1);
      STAMP(c, 6);
    }
  }
  gate<D_Q4>(c, OFF_X3 + (1 * 2 + c.b) * XV_STR + h * 1024);
  if (c.w == 0) {
    const int qi = 2 * h + ch;
    const int off = OFF_X4 + (1 * 2 + c.b) * XV_STR + (c.lane < 33 ? c.lane : 32) * 1024 + qi * 16;
    v4u v;
    for (;;) {
      CBAR();
      v = ld128(c.rs, off);
      if (__all(max(umax3(v.x, v.y, v.z), v.w) != SENT) || !spin_ok(c)) break;
    }
    const unsigned wl = c.lane < 32 ? LO1 : 0u, wh = c.lane < 32 ? HI1 : 0u;
    v4u o;
#pragma unroll
    for (int q = 0; q < 4; ++q) {
      float lo = dot2(v[q], wl, 0.f), hi = dot2(v[q], wh, 0.f);
      lo = quad_sum(lo); hi = quad_sum(hi);
      lo += dppf<0x124>(lo); hi += dppf<0x124>(hi);
      lo += dppf<0x128>(lo); hi += dppf<0x128>(hi);
      lo += dppm<0x142, 0xa>(lo); hi += dppm<0x142, 0xa>(hi);
      const unsigned s2 = (unsigned)__builtin_amdgcn_readlane((int)pack2(lo, hi), 31);
      const unsigned r2 = (unsigned)__builtin_amdgcn_readlane((int)v[q], 32);
      o[q] = pack2(bf_lo(r2) + bf_lo(s2), bf_hi(r2) + bf_hi(s2));
    }
    if (c.lane == 0) *(GAS v4u*)((GAS unsigned char*)p.out + c.b * 1024 + qi * 16) = o;
    STAMP(c, 16);
  }
}

DI void role_o(Ctx& c, const Params& p, v4u nonce, int h) {
  asm volatile("; role O" ::: "memory");
  const int n = c.lane & 15, g = c.lane >> 4;
  const v4u sv = {SENT, SENT, SENT, SENT};
  ckv_dma(c, p);
  DRAIN();
  __builtin_amdgcn_sched_barrier(0);
  v4u Wv[16], Wo[16];
  {
    v4u rv[2][8];
    const GAS unsigned char* wv = (const GAS unsigned char*)p.wuv + ((size_t)(64 * c.w + 2 * g) * 4096 + h * 128 + 8 * n) * 2;
#pragma unroll
    for (int ks = 0; ks < 2; ++ks)
#pragma unroll
      for (int j = 0; j < 8; ++j) rv[ks][j] = *(const GAS v4u*)(wv + (size_t)(16 * (j & 3) + 8 * ks + (j >> 2)) * 8192);
    __builtin_amdgcn_sched_barrier(0);
    fill_attn(c);
    if (c.w < 2) st128(c.rs, OFF_X3 + (c.w * 2 + c.b) * XV_STR + h * 1024 + c.lane * 16, sv);
    tr8x8(rv[0], Wv); tr8x8(rv[1], Wv + 8);
  }
#pragma unroll
  for (int i = 0; i < 16; ++i) asm volatile("" : "=v"(Wo[i]));
  publish_ready_b(c, nonce);
  STAMP(c, 20);
  wait_ready_a(c, nonce);
  STAMP(c, 1);
#pragma unroll 1
  for (int l = 0; l < 2; ++l) {
    attn_phase(c, l, [&]() __attribute__((always_inline)) {
      if (l == 0) {
        const unsigned ln = c.lane;
        const unsigned vo = ((h * 128 + 32 * ((ln >> 3) & 1) + 8 * (ln >> 4)) * 512 + 64 * c.w + 8 * (ln & 7)) * 2;
#pragma unroll
        for (int kb = 0; kb < 2; ++kb)
#pragma unroll
          for (int j = 0; j < 8; ++j) ld_hidden(Wo[8 * kb + j], vo, p.wo, (64 * kb + j) * 1024);
      }
    });
    if (l == 0) {
      asm volatile("s_waitcnt vmcnt(0)" : "+v"(Wo[0]), "+v"(Wo[1]), "+v"(Wo[2]), "+v"(Wo[3]), "+v"(Wo[4]), "+v"(Wo[5]), "+v"(Wo[6]), "+v"(Wo[7]),
                   "+v"(Wo[8]), "+v"(Wo[9]), "+v"(Wo[10]), "+v"(Wo[11]), "+v"(Wo[12]), "+v"(Wo[13]), "+v"(Wo[14]), "+v"(Wo[15]) :: "memory");
#pragma unroll
      for (int q = 0; q < 2; ++q) {
        v4u r[8];
#pragma unroll
        for (int j = 0; j < 8; ++j) r[j] = Wo[8 * q + j];
        tr8x8(r, Wo + 8 * q);
      }
      wait_ready_b(c, nonce);
    }
    float inv;
    {
      const int bs = OFF_ST + ((l * 2 + c.b) * 32 + h) * 512 + c.lane * 8;
      v2u st;
      for (;;) {
        CBAR();
        st = ld64(c.rs, bs);
        if (__all(max(st.x, st.y) != SENT) || !spin_ok(c)) break;
      }
      const float kf = u2f(st.x);
      float K = kf;
      K = fmaxf(K, dppf<0x128>(K)); K = fmaxf(K, dppf<0x124>(K)); K = fmaxf(K, dppf<0x122>(K)); K = fmaxf(K, dppf<0x121>(K));
      K = xg_max(K);
      const int d = (int)(K - kf);
      const unsigned eb = d < 127 ? (unsigned)(127 - d) << 7 : 0u;
      float Ls = u2f(st.y) * u2f(eb << 16);
      Ls += dppf<0x128>(Ls); Ls += dppf<0x124>(Ls); Ls += dppf<0x122>(Ls); Ls += dppf<0x121>(Ls);
      Ls = xg_sum(Ls);
      inv = 1.0f / Ls;
      *(LAS unsigned*)(c.L + L_EW + c.w * 256 + c.lane * 4) = eb;
    }
    unsigned el[16];
#pragma unroll
    for (int i = 0; i < 4; ++i) {
      const v4u e4 = *(const LAS v4u*)(c.L + L_EW + c.w * 256 + g * 64 + i * 16);
      el[4 * i] = e4.x; el[4 * i + 1] = e4.y; el[4 * i + 2] = e4.z; el[4 * i + 3] = e4.w;
    }
    const int bx = OFF_X2 + ((l * 2 + c.b) * 32 + h) * 65536 + c.w * 8192 + g * 2048 + n * 8;
    v2u v[16];
    for (;;) {
      CBAR();
#pragma unroll
      for (int i = 0; i < 16; ++i) v[i] = ld64(c.rs, bx + i * 128);
      unsigned m = 0u;
#pragma unroll
      for (int i = 0; i < 16; ++i) m = umax3(m, v[i].x, v[i].y);
      if (__all(m != SENT) || !spin_ok(c)) break;
    }
    STAMP(c, 5 + 8 * l);
    float s0 = 0.f, s1 = 0.f, s2 = 0.f, s3 = 0.f;
#pragma unroll
    for (int i = 0; i < 16; ++i) {
      const unsigned eh = el[i] << 16;
      s0 = dot2(v[i].x, el[i], s0); s1 = dot2(v[i].x, eh, s1); s2 = dot2(v[i].y, el[i], s2); s3 = dot2(v[i].y, eh, s3);
    }
    s0 = xg_sum(s0) * inv; s1 = xg_sum(s1) * inv; s2 = xg_sum(s2) * inv; s3 = xg_sum(s3) * inv;
    const unsigned h01 = pack2(s0, s1), h23 = pack2(s2, s3);
    const unsigned l01 = pack2(dot2(h01, NLO1, s0), dot2(h01, NHI1, s1)), l23 = pack2(dot2(h23, NLO1, s2), dot2(h23, NHI1, s3));
    *(LAS v2u*)(c.L + L_FR + c.w * 256 + (g & 1) * 128 + n * 8) = (g & 1) ? v2u{l01, l23} : v2u{h01, h23};
    const v4u a0 = *(const LAS v4u*)(c.L + L_FR + c.w * 256 + (n & 1) * 128 + g * 16);
    const v4u a1 = *(const LAS v4u*)(c.L + L_FR + c.w * 256 + (n & 1) * 128 + 64 + g * 16);
    asm volatile("" :: "v"(a0), "v"(a1));
    STAMP(c, 7 + 8 * l);
    gemv_ksplit<true>(c, a0, a1, Wv);
    STAMP(c, 22 + 2 * l);
    reduce128(c);
    STAMP(c, 23 + 2 * l);
    gemv_nsplit<2>(c, Wo, OFF_X3 + (l * 2 + c.b) * XV_STR + h * 1024);
    STAMP(c, 6 + 8 * l);
    if (l == 0) gate<D_A1>(c, OFF_X4 + (0 * 2 + c.b) * XV_STR + h * 1024);
  }
}

DI void role_f(Ctx& c, const Params& p, v4u nonce, int j) {
  asm volatile("; role F" ::: "memory");
  const int n = c.lane & 15, g = c.lane >> 4;
  const v4u sv = {SENT, SENT, SENT, SENT};
  ckv_dma(c, p);
  DRAIN();
  __builtin_amdgcn_sched_barrier(0);
  v4u r13[2][8], r2[8];
  {
    const int nn = n < 14 ? n : 13;
    const u16* wsel = nn < 7 ? p.w1 : p.w3;
    const int col = 56 * j + 8 * (nn < 7 ? nn : nn - 7);
    const GAS unsigned char* wa = (const GAS unsigned char*)wsel + ((size_t)(64 * c.w + 16 * g) * 1792 + col) * 2;
#pragma unroll
    for (int ks = 0; ks < 2; ++ks)
#pragma unroll
      for (int jj = 0; jj < 8; ++jj) r13[ks][jj] = *(const GAS v4u*)(wa + (size_t)(8 * ks + jj) * 3584);
#pragma unroll
    for (int jj = 0; jj < 8; ++jj) {
      int f = 32 * (n >> 3) + 8 * g + jj; f = f < 56 ? f : 55;
      r2[jj] = *(const GAS v4u*)((const GAS unsigned char*)p.w2 + ((size_t)(56 * j + f) * 512 + 64 * c.w + 8 * (n & 7)) * 2);
    }
  }
  __builtin_amdgcn_sched_barrier(0);
  fill_attn(c);
  if (c.w < 2) st128(c.rs, OFF_X4 + (c.w * 2 + c.b) * XV_STR + j * 1024 + c.lane * 16, sv);
  if (j == 0 && (c.w == 2 || c.w == 3)) st128(c.rs, OFF_X4 + ((c.w - 2) * 2 + c.b) * XV_STR + 32 * 1024 + c.lane * 16, sv);
  v4u W13[16], W2[8];
  tr8x8(r13[0], W13); tr8x8(r13[1], W13 + 8);
  tr8x8(r2, W2);
  publish_ready_b(c, nonce);
  STAMP(c, 20);
  wait_ready_a(c, nonce);
  STAMP(c, 1);
#pragma unroll 1
  for (int l = 0; l < 2; ++l) {
    attn_phase(c, l, NoHook());
    unsigned xp;
    if (l == 0) wait_ready_b(c, nonce);
    gate<D_F3>(c, -1);
    if (l == 0) { xp = gather33<false>(c, OFF_X3 + (0 * 2 + c.b) * XV_STR, p.x + c.b * 512); }
    else xp = gather33<true>(c, OFF_X3 + (1 * 2 + c.b) * XV_STR, nullptr);
    STAMP(c, 5 + 8 * l);
    if (j == 0 && c.lane < 32) st32(c.rs, OFF_X4 + (l * 2 + c.b) * XV_STR + 32 * 1024 + 128 * c.w + 4 * c.lane, canon(xp));
    v4u a0, a1;
    layernorm(c, xp, a0, a1);
    STAMP(c, 7 + 8 * l);
    gemv_ksplit<false>(c, a0, a1, W13);
    STAMP(c, 22 + 2 * l);
    LBAR();
    STAMP(c, 8 + 8 * l);
    if (c.w == 0) {
      const int li = c.lane < 56 ? c.lane : 55;
      const unsigned gu = pack2(red_sum8(c, li), red_sum8(c, 56 + li));
      const float gt = bf_lo(gu);
      const float sg = rbf(gt * __builtin_amdgcn_rcpf(1.0f + __expf(-gt)));
      const float hv = c.lane < 56 ? sg * bf_hi(gu) : 0.f;
      *(LAS __bf16*)(c.L + L_VEC + c.lane * 2) = (__bf16)hv;
    }
    LBAR();
    STAMP(c, 23 + 2 * l);
    gemv_nsplit<1>(c, W2, OFF_X4 + (l * 2 + c.b) * XV_STR + j * 1024);
    STAMP(c, 6 + 8 * l);
    if (l == 0) gate<D_FA1>(c, -1);
  }
}

__global__ void __launch_bounds__(NT, 2) mega(Params p) {
  extern __shared__ __attribute__((aligned(16))) unsigned char smem[];
  Ctx c;
  {
    unsigned long long pc0; unsigned clen, cpf;
    asm volatile("s_getpc_b64 %0\n.Lmk_here:\n\ts_mov_b32 %1, .Lmk_end-.Lmk_here" : "=s"(pc0), "=s"(clen));
    const unsigned o = min(threadIdx.x * 128u, clen);
    asm volatile("global_load_dword %0, %1, %2" : "=v"(cpf) : "v"(o), "s"(pc0) : "memory");
    c.cpf = cpf;
  }
  c.rs = __builtin_amdgcn_make_buffer_rsrc((void*)p.ws, 0, WS_BYTES, 0x00020000);
  c.L = (LAS unsigned char*)smem;
  c.lane = threadIdx.x & 63;
  c.w = __builtin_amdgcn_readfirstlane(threadIdx.x >> 6);
  c.blk = blockIdx.x;
  const int x = c.blk & 7, slot = c.blk >> 3;
  c.b = (slot >> 3) & 1; c.G = slot >> 4; c.s = 8 * x + (slot & 7);
  const int r = 8 * c.G + (slot & 7);
  c.spins = 0; c.wsp = p.ws;
  c.kLO1 = sconst(0x00003F80u); c.kHI1 = sconst(0x3F800000u); c.kONES = sconst(0x3F803F80u); c.kNLO1 = sconst(0x0000BF80u); c.kNHI1 = sconst(0xBF800000u);
  STAMP(c, 0);
  const unsigned long long id = llvm_dispatch_id();
  const unsigned long long dp = (unsigned long long)__builtin_amdgcn_dispatch_ptr();
  const v4u nonce = {(unsigned)id ^ 0x5bd1e995u, (unsigned)(id >> 32) ^ 0x9e3779b9u, (unsigned)dp ^ 0x85ebca6bu, (unsigned)(dp >> 32) ^ 0xc2b2ae35u};
  c.nonce = nonce;
  if (r < 8) role_q(c, p, nonce, 4 * x + (r >> 1), r & 1);
  else if (r < 12) role_o(c, p, nonce, 4 * x + (r - 8));
  else role_f(c, p, nonce, 4 * x + (r - 12));
  asm volatile(".Lmk_end:" ::: "memory");
  if (c.w == 0 && c.lane < 2) st128(c.rs, OFF_RDY + c.blk * 64 + c.lane * 16, v4u{~nonce.x, ~nonce.y, ~nonce.z, ~nonce.w});
}
}

extern "C" void kernel_launch(void* const* d_in, const int* in_sizes, int n_in,
                              void* d_out, int out_size, void* d_ws, size_t ws_size,
                              hipStream_t stream) {
  mk::Params p;
  p.x = (const u16*)d_in[0]; p.ckv = (const u16*)d_in[1]; p.wq = (const u16*)d_in[2]; p.wuk = (const u16*)d_in[3];
  p.wuv = (const u16*)d_in[4]; p.wo = (const u16*)d_in[5]; p.w1 = (const u16*)d_in[7]; p.w3 = (const u16*)d_in[8];
  p.w2 = (const u16*)d_in[9]; p.out = (u16*)d_out; p.ws = (unsigned char*)d_ws;
  (void)hipFuncSetAttribute((const void*)mk::mega, hipFuncAttributeMaxDynamicSharedMemorySize, mk::L_BYTES + 2048);
  hipLaunchKernelGGL(mk::mega, dim3(256), dim3(mk::NT), mk::L_BYTES + 2048, stream, p);
}
```

```cpp
#include <hip/hip_runtime.h>
#include <hip/hip_bf16.h>
#include <stdint.h>

#define LAS __attribute__((address_space(3)))
#define GAS __attribute__((address_space(1)))
typedef unsigned short u16;
typedef unsigned v4u __attribute__((ext_vector_type(4)));
typedef unsigned v2u __attribute__((ext_vector_type(2)));
typedef float f32x4 __attribute__((ext_vector_type(4)));
typedef float f32x2 __attribute__((ext_vector_type(2)));
typedef short bf16x8 __attribute__((ext_vector_type(8)));
typedef short s16x4 __attribute__((ext_vector_type(4)));
typedef __bf16 bf16x2_t __attribute__((ext_vector_type(2)));
typedef unsigned short u16x2 __attribute__((ext_vector_type(2)));
#define DI __device__ __forceinline__

extern "C" __device__ unsigned long long llvm_dispatch_id() __asm("llvm.amdgcn.dispatch.id");

namespace mk {
constexpr int NT = 512;
constexpr unsigned SENT = 0xFFFFFFFFu;
constexpr unsigned SPIN_CAP = 1u << 17;
constexpr int AUX_SC1 = 16;
#define LO1 (c.kLO1)
#define HI1 (c.kHI1)
#define ONES (c.kONES)
#define NLO1 (c.kNLO1)
#define NHI1 (c.kNHI1)

constexpr int OFF_RDY = 0;
constexpr int OFF_X1 = 16384;
constexpr int OFF_ST = OFF_X1 + 2 * 2 * 32 * 1024;
constexpr int OFF_X3 = OFF_ST + 2 * 2 * 32 * 512;
constexpr int XV_STR = 34 * 1024;
constexpr int OFF_X4 = OFF_X3 + 4 * XV_STR;
constexpr int OFF_X2 = OFF_X4 + 4 * XV_STR;
constexpr int WS_BYTES = OFF_X2 + 2 * 2 * 32 * 65536;

constexpr int L_IMG = 0;
constexpr int L_QA = 131072;
constexpr int L_PB = L_QA + 16384;
constexpr int L_SB = L_PB + 4096;
constexpr int L_LN = L_SB + 1024;
constexpr int L_VEC = L_LN + 64;
constexpr int L_EW = L_VEC + 256;
constexpr int L_FR = L_EW + 2048;
constexpr int L_BYTES = L_FR + 2048;
constexpr int L_RED = L_QA;

struct Params {
  const u16* x; const u16* ckv; const u16* wq; const u16* wuk; const u16* wuv; const u16* wo;
  const u16* w1; const u16* w3; const u16* w2; u16* out; unsigned char* ws;
};

DI float u2f(unsigned u) { return __builtin_bit_cast(float, u); }
DI unsigned f2u(float f) { return __builtin_bit_cast(unsigned, f); }
DI float bf_lo(unsigned u) { return u2f(u << 16); }
DI float bf_hi(unsigned u) { return u2f(u & 0xFFFF0000u); }
DI float rbf(float f) { return (float)(__bf16)f; }
DI unsigned pack2(float lo, float hi) { bf16x2_t v = {(__bf16)lo, (__bf16)hi}; return __builtin_bit_cast(unsigned, v); }
DI unsigned canon(unsigned u) { return u < 0xFFFFFFFEu ? u : 0xFFFFFFFEu; }
DI float dot2(unsigned a, unsigned b, float c) { return __builtin_amdgcn_fdot2_f32_bf16(__builtin_bit_cast(bf16x2_t, a), __builtin_bit_cast(bf16x2_t, b), c, false); }
DI unsigned subsat(unsigned a, unsigned b) { return __builtin_bit_cast(unsigned, __builtin_elementwise_sub_sat(__builtin_bit_cast(u16x2, a), __builtin_bit_cast(u16x2, b))); }
DI unsigned umax3(unsigned a, unsigned b, unsigned c) { return max(max(a, b), c); }
DI f32x4 mfma(v4u a, v4u b, f32x4 c) { return __builtin_amdgcn_mfma_f32_16x16x32_bf16(__builtin_bit_cast(bf16x8, a), __builtin_bit_cast(bf16x8, b), c, 0, 0, 0); }
template <int CTRL> DI float dppf(float v) { return u2f((unsigned)__builtin_amdgcn_update_dpp(0, (int)f2u(v), CTRL, 0xf, 0xf, true)); }
template <int CTRL> DI unsigned dppu(unsigned v) { return (unsigned)__builtin_amdgcn_update_dpp(0, (int)v, CTRL, 0xf, 0xf, true); }
template <int CTRL, int RM> DI float dppm(float v) { return u2f((unsigned)__builtin_amdgcn_update_dpp(0, (int)f2u(v), CTRL, RM, 0xf, false)); }
DI float quad_sum(float v) { v += dppf<0xB1>(v); v += dppf<0x4E>(v); return v; }
DI float xg_sum(float v) {
  const auto a = __builtin_amdgcn_permlane16_swap(f2u(v), f2u(v), false, false);
  const unsigned a0 = a[0], a1 = a[1];
  v = u2f(a0) + u2f(a1);
  const auto b = __builtin_amdgcn_permlane32_swap(f2u(v), f2u(v), false, false);
  const unsigned b0 = b[0], b1 = b[1];
  return u2f(b0) + u2f(b1);
}
DI float x32_sum(float v) {
  const auto b = __builtin_amdgcn_permlane32_swap(f2u(v), f2u(v), false, false);
  const unsigned b0 = b[0], b1 = b[1];
  return u2f(b0) + u2f(b1);
}
DI float xg_max(float v) {
  const auto a = __builtin_amdgcn_permlane16_swap(f2u(v), f2u(v), false, false);
  const unsigned a0 = a[0], a1 = a[1];
  v = fmaxf(u2f(a0), u2f(a1));
  const auto b = __builtin_amdgcn_permlane32_swap(f2u(v), f2u(v), false, false);
  const unsigned b0 = b[0], b1 = b[1];
  return fmaxf(u2f(b0), u2f(b1));
}
DI unsigned swz(unsigned r) { return (((r ^ ((r >> 1) & 4u)) & 7u) << 1) | ((r >> 3) & 1u); }

typedef __amdgpu_buffer_rsrc_t rsrc_t;
DI v4u ld128(rsrc_t rs, int off) { return __builtin_amdgcn_raw_buffer_load_b128(rs, off, 0, AUX_SC1); }
DI v2u ld64(rsrc_t rs, int off) { return __builtin_amdgcn_raw_buffer_load_b64(rs, off, 0, AUX_SC1); }
DI unsigned ld32(rsrc_t rs, int off) { return __builtin_amdgcn_raw_buffer_load_b32(rs, off, 0, AUX_SC1); }
DI void st128(rsrc_t rs, int off, v4u v) { __builtin_amdgcn_raw_buffer_store_b128(v, rs, off, 0, AUX_SC1); }
DI void st64(rsrc_t rs, int off, v2u v) { __builtin_amdgcn_raw_buffer_store_b64(v, rs, off, 0, AUX_SC1); }
DI void st32(rsrc_t rs, int off, unsigned v) { __builtin_amdgcn_raw_buffer_store_b32(v, rs, off, 0, AUX_SC1); }
#define CBAR() asm volatile("" ::: "memory")
#define DRAIN() asm volatile("s_waitcnt vmcnt(0)" ::: "memory")
#define LBAR() do { asm volatile("s_waitcnt lgkmcnt(0)" ::: "memory"); __builtin_amdgcn_s_barrier(); asm volatile("" ::: "memory"); } while (0)

DI void tr8x8(const v4u (&r)[8], v4u* f) {
#pragma unroll
  for (int q = 0; q < 4; ++q)
#pragma unroll
    for (int d = 0; d < 4; ++d) {
      const unsigned a = r[2 * d][q], b = r[2 * d + 1][q];
      f[2 * q][d] = __builtin_amdgcn_perm(b, a, 0x05040100u);
      f[2 * q + 1][d] = __builtin_amdgcn_perm(b, a, 0x07060302u);
    }
}
DI void tr8x4(const v2u (&r)[8], v4u* f) {
#pragma unroll
  for (int q = 0; q < 2; ++q)
#pragma unroll
    for (int d = 0; d < 4; ++d) {
      const unsigned a = r[2 * d][q], b = r[2 * d + 1][q];
      f[2 * q][d] = __builtin_amdgcn_perm(b, a, 0x05040100u);
      f[2 * q + 1][d] = __builtin_amdgcn_perm(b, a, 0x07060302u);
    }
}

#define STAMP(c, i) do {} while (0)
struct Ctx {
  unsigned char* wsp;
  rsrc_t rs; LAS unsigned char* L;
  int lane, w, b, G, s, blk;
  unsigned spins;
  unsigned kLO1, kHI1, kONES, kNLO1, kNHI1;
  v4u nonce;
};
DI unsigned sconst(unsigned v) { unsigned r; asm volatile("s_mov_b32 %0, %1" : "=s"(r) : "i"(v)); return r; }
#define RELANE(c) do { int l_ = (c).lane; asm volatile("" : "+v"(l_)); (c).lane = l_; } while (0)
DI bool spin_ok(Ctx& c) { return ++c.spins < SPIN_CAP; }

DI int q_blk(int b, int h, int ch) { return (h >> 2) + 8 * (8 * b + 2 * (h & 3) + ch); }

DI bool nonce_eq(v4u a, v4u n) { return ((a.x ^ n.x) | (a.y ^ n.y) | (a.z ^ n.z) | (a.w ^ n.w)) == 0u; }
DI void wait_ready_a(Ctx& c, v4u nonce) {
  const int q = c.lane & 3;
  const int off = OFF_RDY + q_blk(c.b, 16 * c.G + c.w + 8 * (q >> 1), q & 1) * 64;
  for (;;) {
    CBAR();
    const v4u a = ld128(c.rs, off);
    if (__all(nonce_eq(a, nonce)) || !spin_ok(c)) break;
  }
  CBAR();
}
DI void wait_ready_b(Ctx& c, v4u nonce) {
  const int o0 = OFF_RDY + (c.b * 64 + c.lane) * 64 + 16, o1 = o0 + 128 * 64;
  for (;;) {
    CBAR();
    const v4u a = ld128(c.rs, o0), bq = ld128(c.rs, o1);
    if (__all(nonce_eq(a, nonce) & nonce_eq(bq, nonce)) || !spin_ok(c)) break;
  }
  CBAR();
}

template <int D>
DI void gate(Ctx& c, int off) {
  if (c.w == 0) {
    if (off >= 0) {
      for (;;) {
        CBAR();
        const unsigned v = __builtin_amdgcn_raw_buffer_load_b32(c.rs, off, 0, AUX_SC1);
        if (__builtin_amdgcn_readfirstlane(v) != SENT || !spin_ok(c)) break;
        __builtin_amdgcn_s_sleep(4);
      }
    }
    if (D > 0) __builtin_amdgcn_s_sleep(D);
  }
  __builtin_amdgcn_s_barrier();
}
#define D_F3 40
#define D_Q4 30
#define D_A1 30
#define D_FA1 45

template <bool RES_WS>
DI unsigned gather33(Ctx& c, int base, const u16* xin) {
  const int co = 128 * c.w + 4 * (c.lane & 31);
  unsigned v[16], res;
  if (!RES_WS) res = *(const GAS unsigned*)((const GAS unsigned char*)xin + co);
  const int o = base + (c.lane >> 5) * 1024 + co;
  for (;;) {
    CBAR();
#pragma unroll
    for (int i = 0; i < 16; ++i) v[i] = ld32(c.rs, o + i * 2048);
    if (RES_WS) res = ld32(c.rs, base + 32 * 1024 + co);
    unsigned m = RES_WS ? res : 0u;
#pragma unroll
    for (int i = 0; i < 16; i += 2) m = umax3(m, v[i], v[i + 1]);
    if (__all(m != SENT) || !spin_ok(c)) break;
  }
  float s0 = 0.f, s1 = 0.f, s2 = 0.f, s3 = 0.f;
#pragma unroll
  for (int i = 0; i < 16; i += 2) { s0 = dot2(v[i], LO1, s0); s1 = dot2(v[i], HI1, s1); s2 = dot2(v[i + 1], LO1, s2); s3 = dot2(v[i + 1], HI1, s3); }
  const unsigned o01 = pack2(x32_sum(s0 + s2), x32_sum(s1 + s3));
  return pack2(dot2(o01, LO1, bf_lo(res)), dot2(o01, HI1, bf_hi(res)));
}

DI void layernorm(Ctx& c, unsigned xp, v4u& a0, v4u& a1) {
  float t1 = dot2(xp, ONES, 0.f), t2 = dot2(xp, xp, 0.f);
  t1 += dppf<0x121>(t1); t2 += dppf<0x121>(t2);
  t1 += dppf<0x122>(t1); t2 += dppf<0x122>(t2);
  t1 += dppf<0x124>(t1); t2 += dppf<0x124>(t2);
  t1 += dppf<0x128>(t1); t2 += dppf<0x128>(t2);
  t1 += dppm<0x142, 0xa>(t1); t2 += dppm<0x142, 0xa>(t2);
  if (c.lane == 31) *(LAS f32x2*)(c.L + L_LN + c.w * 8) = f32x2{t1, t2};
  LBAR();
  f32x4 t[4];
#pragma unroll
  for (int i = 0; i < 4; ++i) t[i] = *(const LAS f32x4*)(c.L + L_LN + i * 16);
  const f32x4 tt = (t[0] + t[1]) + (t[2] + t[3]);
  const float mean = (tt.x + tt.z) * (1.0f / 512.0f);
  const float var = fmaxf((tt.y + tt.w) * (1.0f / 512.0f) - mean * mean, 0.f);
  const unsigned mv = pack2(mean, var);
  const float nmb = -bf_lo(mv);
  const float sd = rbf(__builtin_amdgcn_sqrtf(rbf(bf_hi(mv) + 1e-5f)));
  const float ri = __builtin_amdgcn_rcpf(sd);
  const unsigned d01 = pack2(dot2(xp, LO1, nmb), dot2(xp, HI1, nmb));
  const unsigned n01 = pack2(bf_lo(d01) * ri, bf_hi(d01) * ri);
  *(LAS unsigned*)(c.L + L_FR + c.w * 256 + c.lane * 4) = n01;
  a0 = *(const LAS v4u*)(c.L + L_FR + c.w * 256 + (c.lane >> 4) * 32);
  a1 = *(const LAS v4u*)(c.L + L_FR + c.w * 256 + (c.lane >> 4) * 32 + 16);
}

template <bool HILO>
DI void gemv_ksplit(Ctx& c, v4u a0, v4u a1, const v4u* W) {
  f32x4 acc[8];
#pragma unroll
  for (int t = 0; t < 8; ++t) acc[t] = mfma(a0, W[t], f32x4{0.f, 0.f, 0.f, 0.f});
#pragma unroll
  for (int t = 0; t < 8; ++t) acc[t] = mfma(a1, W[8 + t], acc[t]);
  if (c.lane < 16) {
    f32x4 o0, o1;
#pragma unroll
    for (int t = 0; t < 4; ++t) { o0[t] = HILO ? acc[t].x + acc[t].y : acc[t].x; o1[t] = HILO ? acc[4 + t].x + acc[4 + t].y : acc[4 + t].x; }
    *(LAS f32x4*)(c.L + L_RED + c.w * 512 + c.lane * 32) = o0;
    *(LAS f32x4*)(c.L + L_RED + c.w * 512 + c.lane * 32 + 16) = o1;
  }
}
DI float red_sum8(Ctx& c, int idx) {
  const LAS float* R = (const LAS float*)(c.L + L_RED);
  return ((R[idx] + R[128 + idx]) + (R[256 + idx] + R[384 + idx])) + ((R[512 + idx] + R[640 + idx]) + (R[768 + idx] + R[896 + idx]));
}
DI void reduce128(Ctx& c) {
  LBAR();
  if (c.w < 2) { const int d = c.w * 64 + c.lane; *(LAS __bf16*)(c.L + L_VEC + d * 2) = (__bf16)red_sum8(c, d); }
  LBAR();
}

template <int KB>
DI void gemv_nsplit(Ctx& c, const v4u* W, int off) {
  const int n = c.lane & 15, g = c.lane >> 4;
  f32x4 acc[8];
#pragma unroll
  for (int kb = 0; kb < KB; ++kb) {
    const v4u a = *(const LAS v4u*)(c.L + L_VEC + kb * 128 + (n & 1) * 64 + g * 16);
#pragma unroll
    for (int t = 0; t < 8; ++t) acc[t] = mfma(a, W[8 * kb + t], kb == 0 ? f32x4{0.f, 0.f, 0.f, 0.f} : acc[t]);
  }
  float r[8];
#pragma unroll
  for (int t = 0; t < 8; ++t) r[t] = acc[t].x + dppf<0x108>(acc[t].y);
  if (c.lane < 8) st128(c.rs, off + 128 * c.w + 16 * c.lane, v4u{canon(pack2(r[0], r[1])), canon(pack2(r[2], r[3])), canon(pack2(r[4], r[5])), canon(pack2(r[6], r[7]))});
}

DI float score_round(float s) {
  const float SC = 0.08838834764831845f;
  const unsigned bits = f2u(s);
  const unsigned tr = bits & 0xFFFF0000u;
  const unsigned g2 = pack2(u2f(tr) * SC, u2f(tr + 0x10000u) * SC);
  const float ga = bf_lo(g2);
  const float wgt = fminf(fmaxf((float)(bits & 0xFFFFu) * (1.0f / (65536.0f * 0.6f)) + (0.5f - 0.5f / 0.6f), 0.f), 1.f);
  return ga + wgt * (bf_hi(g2) - ga);
}

DI void attn_phase(Ctx& c, int l) {
  RELANE(c);
  const int tid = c.w * 64 + c.lane, n = c.lane & 15, g = c.lane >> 4;
  v4u kf[16];
  {
    const unsigned kb = L_IMG + (c.w * 16 + n) * 1024 + (((unsigned)g ^ swz(n)) << 4);
#pragma unroll
    for (int ks = 0; ks < 16; ++ks) kf[ks] = *(const LAS v4u*)(c.L + ((kb ^ (64u * (ks & 3))) + 256u * (ks >> 2)));
  }
  unsigned ab;
  {
    const unsigned q4 = (c.lane & 15) >> 2, p4 = c.lane & 3;
    const unsigned key = 4u * g + q4;
    const unsigned chunk = 8u * c.w + (p4 >> 1);
    ab = L_IMG + key * 1024 + ((chunk ^ swz(key)) << 4) + 8u * (p4 & 1);
    asm volatile("" : "+v"(ab));
  }
  {
    const int base = OFF_X1 + ((l * 2 + c.b) * 32 + 16 * c.G) * 1024 + tid * 16;
    v4u v0, v1;
    for (;;) {
      CBAR();
      v0 = ld128(c.rs, base);
      v1 = ld128(c.rs, base + 8192);
      const unsigned m = max(umax3(umax3(v0.x, v0.y, v0.z), v0.w, v1.x), umax3(v1.y, v1.z, v1.w));
      if (__all(m != SENT) || !spin_ok(c)) break;
    }
    STAMP(c, 2 + 8 * l);
    *(LAS v4u*)(c.L + L_QA + c.w * 1024 + (((unsigned)c.lane ^ swz(c.w)) << 4)) = v0;
    *(LAS v4u*)(c.L + L_QA + (c.w + 8) * 1024 + (((unsigned)c.lane ^ swz(c.w + 8)) << 4)) = v1;
  }
  LBAR();
  if (l == 0 && c.w == 0 && c.lane == 0) st128(c.rs, OFF_RDY + c.blk * 64 + 16, c.nonce);
  STAMP(c, 26 + l);
  f32x4 sacc = {0.f, 0.f, 0.f, 0.f}, sacc2 = {0.f, 0.f, 0.f, 0.f};
  {
    const unsigned qb = L_QA + n * 1024 + (((unsigned)g ^ swz(n)) << 4);
    constexpr int QD = 4;
    v4u qf[QD];
#pragma unroll
    for (int ks = 0; ks < QD; ++ks) qf[ks] = *(const LAS v4u*)(c.L + ((qb ^ (64u * (ks & 3))) + 256u * (ks >> 2)));
    __builtin_amdgcn_sched_barrier(0);
#pragma unroll
    for (int ks = 0; ks < 16; ++ks) {
      if (ks & 1) sacc2 = mfma(kf[ks], qf[ks % QD], sacc2); else sacc = mfma(kf[ks], qf[ks % QD], sacc);
      if (ks + QD < 16) qf[ks % QD] = *(const LAS v4u*)(c.L + ((qb ^ (64u * ((ks + QD) & 3))) + 256u * ((ks + QD) >> 2)));
      __builtin_amdgcn_sched_barrier(0);
    }
  }
  if (l == 1) { asm volatile("" :: "v"(sacc), "v"(sacc2)); STAMP(c, 17); }
  v2u vf[4][4][2];
  auto vf_issue = [&](int u) __attribute__((always_inline)) {
#pragma unroll
    for (int t = 0; t < 4; ++t) {
      const unsigned ad = (ab ^ (32u * t)) + 32768u * u;
      const s16x4 lo = __builtin_amdgcn_ds_read_tr16_b64_v4i16((LAS s16x4*)(c.L + ad));
      const s16x4 hi = __builtin_amdgcn_ds_read_tr16_b64_v4i16((LAS s16x4*)(c.L + ad + 16384u));
      vf[u][t][0] = __builtin_bit_cast(v2u, lo); vf[u][t][1] = __builtin_bit_cast(v2u, hi);
    }
  };
  vf_issue(0); vf_issue(1);
  __builtin_amdgcn_sched_barrier(0);
  {
    const float LOG2E = 1.4426950408889634f;
    float sv[4];
#pragma unroll
    for (int q = 0; q < 4; ++q) sv[q] = score_round(sacc[q] + sacc2[q]) * LOG2E;
    const float kw = ceilf(xg_max(fmaxf(fmaxf(sv[0], sv[1]), fmaxf(sv[2], sv[3]))));
    float p[4];
#pragma unroll
    for (int q = 0; q < 4; ++q) p[q] = __builtin_amdgcn_exp2f(sv[q] - kw);
    const float lw = xg_sum((p[0] + p[1]) + (p[2] + p[3]));
    *(LAS v2u*)(c.L + L_PB + (c.w >> 1) * 1024 + c.lane * 16 + (c.w & 1) * 8) = v2u{pack2(p[0], p[1]), pack2(p[2], p[3])};
    if (g == 0) *(LAS v2u*)(c.L + L_SB + n * 64 + c.w * 8) = v2u{(unsigned)(int)kw, f2u(lw)};
  }
  if (l == 1) STAMP(c, 18);
  LBAR();
  STAMP(c, 3 + 8 * l);
  f32x4 acc[4];
  v2u stv = {0u, 0u};
  {
    v4u sb[4], pr[4];
#pragma unroll
    for (int i = 0; i < 4; ++i) sb[i] = *(const LAS v4u*)(c.L + L_SB + n * 64 + i * 16);
#pragma unroll
    for (int u = 0; u < 4; ++u) pr[u] = *(const LAS v4u*)(c.L + L_PB + u * 1024 + c.lane * 16);
    vf_issue(2); vf_issue(3);
    __builtin_amdgcn_sched_barrier(0);
    int K = max(max(max((int)sb[0].x, (int)sb[0].z), max((int)sb[1].x, (int)sb[1].z)), max(max((int)sb[2].x, (int)sb[2].z), max((int)sb[3].x, (int)sb[3].z)));
    if (c.w == 0) {
      float Ls = 0.f;
#pragma unroll
      for (int i = 0; i < 4; ++i) Ls += ldexpf(u2f(sb[i].y), (int)sb[i].x - K) + ldexpf(u2f(sb[i].w), (int)sb[i].z - K);
      stv = v2u{canon(f2u((float)K)), canon(f2u(Ls))};
    }
#pragma unroll
    for (int t = 0; t < 4; ++t) acc[t] = f32x4{0.f, 0.f, 0.f, 0.f};
#pragma unroll
    for (int u = 0; u < 4; ++u) {
      const unsigned d0 = __umul24((unsigned)min(K - (int)sb[u].x, 255), 0x00800080u), d1 = __umul24((unsigned)min(K - (int)sb[u].z, 255), 0x00800080u);
      const v4u pb = {subsat(pr[u].x, d0), subsat(pr[u].y, d0), subsat(pr[u].z, d1), subsat(pr[u].w, d1)};
#pragma unroll
      for (int t = 0; t < 4; ++t) {
        const v4u af = {vf[u][t][0].x, vf[u][t][0].y, vf[u][t][1].x, vf[u][t][1].y};
        acc[t] = mfma(pb, af, acc[t]);
      }
    }
  }
  if (l == 1) { asm volatile("" :: "v"(acc[0]), "v"(acc[1]), "v"(acc[2]), "v"(acc[3])); STAMP(c, 19); }
  {
    const int base = OFF_X2 + ((l * 2 + c.b) * 32 + 16 * c.G + 4 * g) * 65536 + c.w * 8192 + c.s * 128 + n * 8;
#pragma unroll
    for (int q = 0; q < 4; ++q)
      st64(c.rs, base + q * 65536, v2u{pack2(acc[0][q], acc[1][q]), pack2(acc[2][q], acc[3][q])});
  }
  DRAIN();
  __builtin_amdgcn_s_barrier();
  if (c.w == 0 && g == 0) st64(c.rs, OFF_ST + (((l * 2 + c.b) * 32 + 16 * c.G + n) * 64 + c.s) * 8, stv);
  STAMP(c, 4 + 8 * l);
}

DI void fill_attn(Ctx& c) {
  const int n = c.lane & 15, g = c.lane >> 4;
  const v4u sv = {SENT, SENT, SENT, SENT};
#pragma unroll
  for (int l = 0; l < 2; ++l) {
    if (c.w == 0 && g == 0) st64(c.rs, OFF_ST + (((l * 2 + c.b) * 32 + 16 * c.G + n) * 64 + c.s) * 8, v2u{SENT, SENT});
  }
}

DI void ckv_dma(Ctx& c, const Params& p) {
  const GAS unsigned char* src = (const GAS unsigned char*)p.ckv + ((size_t)c.b * 8192 + c.s * 128 + c.w * 16) * 1024;
#pragma unroll
  for (int i = 0; i < 16; ++i)
    __builtin_amdgcn_global_load_lds((const GAS unsigned*)(src + i * 1024 + (((unsigned)c.lane ^ swz(i)) << 4)),
                                     (LAS unsigned*)(c.L + L_IMG + (c.w * 16 + i) * 1024), 16, 0, 0);
}
DI void ckv_issue(Ctx& c, const Params& p, v4u (&t)[16]) {
  const GAS unsigned char* src = (const GAS unsigned char*)p.ckv + ((size_t)c.b * 8192 + c.s * 128 + c.w * 16) * 1024 + c.lane * 16;
#pragma unroll
  for (int i = 0; i < 16; ++i) t[i] = *(const GAS v4u*)(src + i * 1024);
}
DI void ckv_stage(Ctx& c, const v4u (&t)[16]) {
#pragma unroll
  for (int i = 0; i < 16; ++i)
    *(LAS v4u*)(c.L + L_IMG + (c.w * 16 + i) * 1024 + (((unsigned)c.lane ^ swz(i)) << 4)) = t[i];
}
DI void publish_ready_b(Ctx& c, v4u nonce) {
  LBAR();
}

DI void role_q(Ctx& c, const Params& p, v4u nonce, int h, int ch) {
  asm volatile("; role Q" ::: "memory");
  const int n = c.lane & 15, g = c.lane >> 4;
  const v4u sv = {SENT, SENT, SENT, SENT};
  if (c.w == 0) st128(c.rs, OFF_X1 + (((c.lane >> 5) * 2 + c.b) * 32 + h) * 1024 + ch * 512 + (c.lane & 31) * 16, sv);
  unsigned xp = *(const GAS unsigned*)((const GAS unsigned char*)p.x + c.b * 1024 + 128 * c.w + 4 * (c.lane & 31));
  v4u rq[2][8], ru[8];
  {
    const GAS unsigned char* wq = (const GAS unsigned char*)p.wq + ((size_t)(64 * c.w + 16 * g) * 4096 + h * 128 + 8 * n) * 2;
#pragma unroll
    for (int ks = 0; ks < 2; ++ks)
#pragma unroll
      for (int j = 0; j < 8; ++j) rq[ks][j] = *(const GAS v4u*)(wq + (size_t)(8 * ks + j) * 8192);
    const GAS unsigned char* wu = (const GAS unsigned char*)p.wuk + ((size_t)(256 * ch + 32 * c.w + g) * 4096 + h * 128 + 8 * n) * 2;
#pragma unroll
    for (int i = 0; i < 8; ++i) ru[i] = *(const GAS v4u*)(wu + (size_t)(4 * i) * 8192);
  }
  v4u ct[16];
  __builtin_amdgcn_sched_barrier(0);
  asm volatile("" : "+v"(xp));
  STAMP(c, 9);
  if (c.w == 0 && c.lane == 0) st128(c.rs, OFF_RDY + c.blk * 64, nonce);
  v4u Wq[16];
  const bool xpub = (h == 0 && ch == 0);
  auto q_phase = [&](int l) __attribute__((always_inline)) {
    v4u a0, a1;
    layernorm(c, xp, a0, a1);
    if (l == 0) { ckv_issue(c, p, ct); __builtin_amdgcn_sched_barrier(0); STAMP(c, 7); tr8x8(rq[0], Wq); tr8x8(rq[1], Wq + 8); asm volatile("" :: "v"(Wq[15])); STAMP(c, 22); }
    gemv_ksplit<false>(c, a0, a1, Wq);
    if (l == 0) {
#pragma unroll
      for (int i = 0; i < 8; ++i) { const unsigned row = 4 * i + g; *(LAS v4u*)(c.L + L_IMG + c.w * 8192 + row * 256 + (((unsigned)n ^ swz(row >> 1)) << 4)) = ru[i]; }
#pragma unroll
      for (int t = 0; t < 2; ++t)
#pragma unroll
        for (int ks = 0; ks < 4; ++ks) ru[t * 4 + ks] = *(const LAS v4u*)(c.L + L_IMG + c.w * 8192 + (2 * n + t) * 256 + (((unsigned)(4 * ks + g) ^ swz(n)) << 4));
    }
    reduce128(c);
    if (l == 0) STAMP(c, 23);
    f32x4 acc0 = {0.f, 0.f, 0.f, 0.f}, acc1 = {0.f, 0.f, 0.f, 0.f};
#pragma unroll
    for (int ks = 0; ks < 4; ++ks) {
      const v4u a = *(const LAS v4u*)(c.L + L_VEC + ks * 64 + g * 16);
      acc0 = mfma(a, ru[ks], acc0);
      acc1 = mfma(a, ru[4 + ks], acc1);
    }
    if (c.lane < 16) st32(c.rs, OFF_X1 + ((l * 2 + c.b) * 32 + h) * 1024 + (256 * ch + 32 * c.w + 2 * n) * 2, canon(pack2(acc0.x, acc1.x)));
  };
  q_phase(0);
  STAMP(c, 21);
  wait_ready_a(c, nonce);
  fill_attn(c);
  if (xpub && c.w == 1) st128(c.rs, OFF_X3 + (1 * 2 + c.b) * XV_STR + 32 * 1024 + c.lane * 16, sv);
  ckv_stage(c, ct);
  publish_ready_b(c, nonce);
  STAMP(c, 20);
  STAMP(c, 1);
#pragma unroll 1
  for (int l = 0; l < 2; ++l) {
    attn_phase(c, l);
    if (l == 0) {
      wait_ready_b(c, nonce);
      gate<D_Q4>(c, OFF_X3 + (0 * 2 + c.b) * XV_STR + h * 1024);
      xp = gather33<true>(c, OFF_X4 + (0 * 2 + c.b) * XV_STR, nullptr);
      STAMP(c, 5);
      if (xpub && c.lane < 32) st32(c.rs, OFF_X3 + (1 * 2 + c.b) * XV_STR + 32 * 1024 + 128 * c.w + 4 * c.lane, canon(xp));
      q_phase(1);
      STAMP(c, 6);
    }
  }
  gate<D_Q4>(c, OFF_X3 + (1 * 2 + c.b) * XV_STR + h * 1024);
  if (c.w == 0) {
    const int qi = 2 * h + ch;
    const int off = OFF_X4 + (1 * 2 + c.b) * XV_STR + (c.lane < 33 ? c.lane : 32) * 1024 + qi * 16;
    v4u v;
    for (;;) {
      CBAR();
      v = ld128(c.rs, off);
      if (__all(max(umax3(v.x, v.y, v.z), v.w) != SENT) || !spin_ok(c)) break;
    }
    const unsigned wl = c.lane < 32 ? LO1 : 0u, wh = c.lane < 32 ? HI1 : 0u;
    v4u o;
#pragma unroll
    for (int q = 0; q < 4; ++q) {
      float lo = dot2(v[q], wl, 0.f), hi = dot2(v[q], wh, 0.f);
      lo = quad_sum(lo); hi = quad_sum(hi);
      lo += dppf<0x124>(lo); hi += dppf<0x124>(hi);
      lo += dppf<0x128>(lo); hi += dppf<0x128>(hi);
      lo += dppm<0x142, 0xa>(lo); hi += dppm<0x142, 0xa>(hi);
      const unsigned s2 = (unsigned)__builtin_amdgcn_readlane((int)pack2(lo, hi), 31);
      const unsigned r2 = (unsigned)__builtin_amdgcn_readlane((int)v[q], 32);
      o[q] = pack2(bf_lo(r2) + bf_lo(s2), bf_hi(r2) + bf_hi(s2));
    }
    if (c.lane == 0) *(GAS v4u*)((GAS unsigned char*)p.out + c.b * 1024 + qi * 16) = o;
    STAMP(c, 16);
  }
}

DI void role_o(Ctx& c, const Params& p, v4u nonce, int h) {
  asm volatile("; role O" ::: "memory");
  const int n = c.lane & 15, g = c.lane >> 4;
  const v4u sv = {SENT, SENT, SENT, SENT};
  ckv_dma(c, p);
  DRAIN();
  __builtin_amdgcn_sched_barrier(0);
  v4u rv[2][8], ro[2][8];
  {
    const GAS unsigned char* wv = (const GAS unsigned char*)p.wuv + ((size_t)(64 * c.w + 2 * g) * 4096 + h * 128 + 8 * n) * 2;
#pragma unroll
    for (int ks = 0; ks < 2; ++ks)
#pragma unroll
      for (int j = 0; j < 8; ++j) rv[ks][j] = *(const GAS v4u*)(wv + (size_t)(16 * (j & 3) + 8 * ks + (j >> 2)) * 8192);
    const GAS unsigned char* wo = (const GAS unsigned char*)p.wo + ((size_t)(h * 128 + 32 * (n >> 3) + 8 * g) * 512 + 64 * c.w + 8 * (n & 7)) * 2;
#pragma unroll
    for (int kb = 0; kb < 2; ++kb)
#pragma unroll
      for (int j = 0; j < 8; ++j) ro[kb][j] = *(const GAS v4u*)(wo + (size_t)(64 * kb + j) * 1024);
  }
  __builtin_amdgcn_sched_barrier(0);
  fill_attn(c);
  if (c.w < 2) st128(c.rs, OFF_X3 + (c.w * 2 + c.b) * XV_STR + h * 1024 + c.lane * 16, sv);
  v4u Wv[16], Wo[16];
  tr8x8(rv[0], Wv); tr8x8(rv[1], Wv + 8);
  tr8x8(ro[0], Wo); tr8x8(ro[1], Wo + 8);
  publish_ready_b(c, nonce);
  STAMP(c, 20);
  wait_ready_a(c, nonce);
  STAMP(c, 1);
#pragma unroll 1
  for (int l = 0; l < 2; ++l) {
    attn_phase(c, l);
    if (l == 0) wait_ready_b(c, nonce);
    float inv;
    v2u v[16];
    {
      const int bs = OFF_ST + ((l * 2 + c.b) * 32 + h) * 512 + c.lane * 8;
      v2u st;
      for (;;) {
        CBAR();
        st = ld64(c.rs, bs);
        if (__all(max(st.x, st.y) != SENT) || !spin_ok(c)) break;
      }
      CBAR();
      {
        const int bx = OFF_X2 + ((l * 2 + c.b) * 32 + h) * 65536 + c.w * 8192 + g * 2048 + n * 8;
#pragma unroll
        for (int i = 0; i < 16; ++i) v[i] = ld64(c.rs, bx + i * 128);
        __builtin_amdgcn_sched_barrier(0);
      }
      const float kf = u2f(st.x);
      float K = kf;
      K = fmaxf(K, dppf<0x128>(K)); K = fmaxf(K, dppf<0x124>(K)); K = fmaxf(K, dppf<0x122>(K)); K = fmaxf(K, dppf<0x121>(K));
      K = xg_max(K);
      const int d = (int)(K - kf);
      const unsigned eb = d < 127 ? (unsigned)(127 - d) << 7 : 0u;
      float Ls = u2f(st.y) * u2f(eb << 16);
      Ls += dppf<0x128>(Ls); Ls += dppf<0x124>(Ls); Ls += dppf<0x122>(Ls); Ls += dppf<0x121>(Ls);
      Ls = xg_sum(Ls);
      inv = 1.0f / Ls;
      *(LAS unsigned*)(c.L + L_EW + c.w * 256 + c.lane * 4) = eb;
    }
    unsigned el[16];
#pragma unroll
    for (int i = 0; i < 4; ++i) {
      const v4u e4 = *(const LAS v4u*)(c.L + L_EW + c.w * 256 + g * 64 + i * 16);
      el[4 * i] = e4.x; el[4 * i + 1] = e4.y; el[4 * i + 2] = e4.z; el[4 * i + 3] = e4.w;
    }
    STAMP(c, 5 + 8 * l);
    float s0 = 0.f, s1 = 0.f, s2 = 0.f, s3 = 0.f;
#pragma unroll
    for (int i = 0; i < 16; ++i) {
      const unsigned eh = el[i] << 16;
      s0 = dot2(v[i].x, el[i], s0); s1 = dot2(v[i].x, eh, s1); s2 = dot2(v[i].y, el[i], s2); s3 = dot2(v[i].y, eh, s3);
    }
    s0 = xg_sum(s0) * inv; s1 = xg_sum(s1) * inv; s2 = xg_sum(s2) * inv; s3 = xg_sum(s3) * inv;
    const unsigned h01 = pack2(s0, s1), h23 = pack2(s2, s3);
    const unsigned l01 = pack2(dot2(h01, NLO1, s0), dot2(h01, NHI1, s1)), l23 = pack2(dot2(h23, NLO1, s2), dot2(h23, NHI1, s3));
    *(LAS v2u*)(c.L + L_FR + c.w * 256 + (g & 1) * 128 + n * 8) = (g & 1) ? v2u{l01, l23} : v2u{h01, h23};
    const v4u a0 = *(const LAS v4u*)(c.L + L_FR + c.w * 256 + (n & 1) * 128 + g * 16);
    const v4u a1 = *(const LAS v4u*)(c.L + L_FR + c.w * 256 + (n & 1) * 128 + 64 + g * 16);
    asm volatile("" :: "v"(a0), "v"(a1));
    STAMP(c, 7 + 8 * l);
    gemv_ksplit<true>(c, a0, a1, Wv);
    STAMP(c, 22 + 2 * l);
    reduce128(c);
    STAMP(c, 23 + 2 * l);
    gemv_nsplit<2>(c, Wo, OFF_X3 + (l * 2 + c.b) * XV_STR + h * 1024);
    STAMP(c, 6 + 8 * l);
    if (l == 0) gate<D_A1>(c, OFF_X4 + (0 * 2 + c.b) * XV_STR + h * 1024);
  }
}

DI void role_f(Ctx& c, const Params& p, v4u nonce, int j) {
  asm volatile("; role F" ::: "memory");
  const int n = c.lane & 15, g = c.lane >> 4;
  const v4u sv = {SENT, SENT, SENT, SENT};
  ckv_dma(c, p);
  DRAIN();
  __builtin_amdgcn_sched_barrier(0);
  v4u r13[2][8], r2[8];
  {
    const int nn = n < 14 ? n : 13;
    const u16* wsel = nn < 7 ? p.w1 : p.w3;
    const int col = 56 * j + 8 * (nn < 7 ? nn : nn - 7);
    const GAS unsigned char* wa = (const GAS unsigned char*)wsel + ((size_t)(64 * c.w + 16 * g) * 1792 + col) * 2;
#pragma unroll
    for (int ks = 0; ks < 2; ++ks)
#pragma unroll
      for (int jj = 0; jj < 8; ++jj) r13[ks][jj] = *(const GAS v4u*)(wa + (size_t)(8 * ks + jj) * 3584);
#pragma unroll
    for (int jj = 0; jj < 8; ++jj) {
      int f = 32 * (n >> 3) + 8 * g + jj; f = f < 56 ? f : 55;
      r2[jj] = *(const GAS v4u*)((const GAS unsigned char*)p.w2 + ((size_t)(56 * j + f) * 512 + 64 * c.w + 8 * (n & 7)) * 2);
    }
  }
  __builtin_amdgcn_sched_barrier(0);
  fill_attn(c);
  if (c.w < 2) st128(c.rs, OFF_X4 + (c.w * 2 + c.b) * XV_STR + j * 1024 + c.lane * 16, sv);
  if (j == 0 && (c.w == 2 || c.w == 3)) st128(c.rs, OFF_X4 + ((c.w - 2) * 2 + c.b) * XV_STR + 32 * 1024 + c.lane * 16, sv);
  v4u W13[16], W2[8];
  tr8x8(r13[0], W13); tr8x8(r13[1], W13 + 8);
  tr8x8(r2, W2);
  publish_ready_b(c, nonce);
  STAMP(c, 20);
  wait_ready_a(c, nonce);
  STAMP(c, 1);
#pragma unroll 1
  for (int l = 0; l < 2; ++l) {
    attn_phase(c, l);
    unsigned xp;
    if (l == 0) wait_ready_b(c, nonce);
    gate<D_F3>(c, -1);
    if (l == 0) { xp = gather33<false>(c, OFF_X3 + (0 * 2 + c.b) * XV_STR, p.x + c.b * 512); }
    else xp = gather33<true>(c, OFF_X3 + (1 * 2 + c.b) * XV_STR, nullptr);
    STAMP(c, 5 + 8 * l);
    if (j == 0 && c.lane < 32) st32(c.rs, OFF_X4 + (l * 2 + c.b) * XV_STR + 32 * 1024 + 128 * c.w + 4 * c.lane, canon(xp));
    v4u a0, a1;
    layernorm(c, xp, a0, a1);
    STAMP(c, 7 + 8 * l);
    gemv_ksplit<false>(c, a0, a1, W13);
    STAMP(c, 22 + 2 * l);
    LBAR();
    STAMP(c, 8 + 8 * l);
    if (c.w == 0) {
      const int li = c.lane < 56 ? c.lane : 55;
      const unsigned gu = pack2(red_sum8(c, li), red_sum8(c, 56 + li));
      const float gt = bf_lo(gu);
      const float sg = rbf(gt * __builtin_amdgcn_rcpf(1.0f + __expf(-gt)));
      const float hv = c.lane < 56 ? sg * bf_hi(gu) : 0.f;
      *(LAS __bf16*)(c.L + L_VEC + c.lane * 2) = (__bf16)hv;
    }
    LBAR();
    STAMP(c, 23 + 2 * l);
    gemv_nsplit<1>(c, W2, OFF_X4 + (l * 2 + c.b) * XV_STR + j * 1024);
    STAMP(c, 6 + 8 * l);
    if (l == 0) gate<D_FA1>(c, -1);
  }
}

__global__ void __launch_bounds__(NT, 2) mega(Params p) {
  extern __shared__ __attribute__((aligned(16))) unsigned char smem[];
  Ctx c;
  c.rs = __builtin_amdgcn_make_buffer_rsrc((void*)p.ws, 0, WS_BYTES, 0x00020000);
  c.L = (LAS unsigned char*)smem;
  c.lane = threadIdx.x & 63;
  c.w = __builtin_amdgcn_readfirstlane(threadIdx.x >> 6);
  c.blk = blockIdx.x;
  const int x = c.blk & 7, slot = c.blk >> 3;
  c.b = (slot >> 3) & 1; c.G = slot >> 4; c.s = 8 * x + (slot & 7);
  const int r = 8 * c.G + (slot & 7);
  c.spins = 0; c.wsp = p.ws;
  c.kLO1 = sconst(0x00003F80u); c.kHI1 = sconst(0x3F800000u); c.kONES = sconst(0x3F803F80u); c.kNLO1 = sconst(0x0000BF80u); c.kNHI1 = sconst(0xBF800000u);
  STAMP(c, 0);
  const unsigned long long id = llvm_dispatch_id();
  const unsigned long long dp = (unsigned long long)__builtin_amdgcn_dispatch_ptr();
  const v4u nonce = {(unsigned)id ^ 0x5bd1e995u, (unsigned)(id >> 32) ^ 0x9e3779b9u, (unsigned)dp ^ 0x85ebca6bu, (unsigned)(dp >> 32) ^ 0xc2b2ae35u};
  c.nonce = nonce;
  if (r < 8) role_q(c, p, nonce, 4 * x + (r >> 1), r & 1);
  else if (r < 12) role_o(c, p, nonce, 4 * x + (r - 8));
  else role_f(c, p, nonce, 4 * x + (r - 12));
  if (c.w == 0 && c.lane < 2) st128(c.rs, OFF_RDY + c.blk * 64 + c.lane * 16, v4u{~nonce.x, ~nonce.y, ~nonce.z, ~nonce.w});
}
}

extern "C" void kernel_launch(void* const* d_in, const int* in_sizes, int n_in,
                              void* d_out, int out_size, void* d_ws, size_t ws_size,
                              hipStream_t stream) {
  mk::Params p;
  p.x = (const u16*)d_in[0]; p.ckv = (const u16*)d_in[1]; p.wq = (const u16*)d_in[2]; p.wuk = (const u16*)d_in[3];
  p.wuv = (const u16*)d_in[4]; p.wo = (const u16*)d_in[5]; p.w1 = (const u16*)d_in[7]; p.w3 = (const u16*)d_in[8];
  p.w2 = (const u16*)d_in[9]; p.out = (u16*)d_out; p.ws = (unsigned char*)d_ws;
  (void)hipFuncSetAttribute((const void*)mk::mega, hipFuncAttributeMaxDynamicSharedMemorySize, mk::L_BYTES + 2048);
  hipLaunchKernelGGL(mk::mega, dim3(256), dim3(mk::NT), mk::L_BYTES + 2048, stream, p);
}
```

```cpp
#include <hip/hip_runtime.h>
#include <hip/hip_bf16.h>
#include <stdint.h>

#define LAS __attribute__((address_space(3)))
#define GAS __attribute__((address_space(1)))
typedef unsigned short u16;
typedef unsigned v4u __attribute__((ext_vector_type(4)));
typedef unsigned v2u __attribute__((ext_vector_type(2)));
typedef float f32x4 __attribute__((ext_vector_type(4)));
typedef float f32x2 __attribute__((ext_vector_type(2)));
typedef short bf16x8 __attribute__((ext_vector_type(8)));
typedef short s16x4 __attribute__((ext_vector_type(4)));
typedef __bf16 bf16x2_t __attribute__((ext_vector_type(2)));
typedef unsigned short u16x2 __attribute__((ext_vector_type(2)));
#define DI __device__ __forceinline__

extern "C" __device__ unsigned long long llvm_dispatch_id() __asm("llvm.amdgcn.dispatch.id");

namespace mk {
constexpr int NT = 512;
constexpr unsigned SENT = 0xFFFFFFFFu;
constexpr unsigned SPIN_CAP = 1u << 17;
constexpr int AUX_SC1 = 16;
#define LO1 (c.kLO1)
#define HI1 (c.kHI1)
#define ONES (c.kONES)
#define NLO1 (c.kNLO1)
#define NHI1 (c.kNHI1)

constexpr int OFF_RDY = 0;
constexpr int OFF_X1 = 16384;
constexpr int OFF_ST = OFF_X1 + 2 * 2 * 32 * 1024;
constexpr int OFF_X3 = OFF_ST + 2 * 2 * 32 * 512;
constexpr int XV_STR = 34 * 1024;
constexpr int OFF_X4 = OFF_X3 + 4 * XV_STR;
constexpr int OFF_X2 = OFF_X4 + 4 * XV_STR;
constexpr int OFF_FW = OFF_X2 + 2 * 2 * 32 * 65536;
constexpr int WS_BYTES = OFF_FW + 16384;

constexpr int L_IMG = 0;
constexpr int L_QA = 131072;
constexpr int L_PB = L_QA + 16384;
constexpr int L_SB = L_PB + 4096;
constexpr int L_LN = L_SB + 1024;
constexpr int L_VEC = L_LN + 64;
constexpr int L_EW = L_VEC + 256;
constexpr int L_FR = L_EW + 2048;
constexpr int L_BYTES = L_FR + 2048;
constexpr int L_RED = L_QA;

struct Params {
  const u16* x; const u16* ckv; const u16* wq; const u16* wuk; const u16* wuv; const u16* wo;
  const u16* w1; const u16* w3; const u16* w2; u16* out; unsigned char* ws;
};

DI float u2f(unsigned u) { return __builtin_bit_cast(float, u); }
DI unsigned f2u(float f) { return __builtin_bit_cast(unsigned, f); }
DI float bf_lo(unsigned u) { return u2f(u << 16); }
DI float bf_hi(unsigned u) { return u2f(u & 0xFFFF0000u); }
DI float rbf(float f) { return (float)(__bf16)f; }
DI unsigned pack2(float lo, float hi) { bf16x2_t v = {(__bf16)lo, (__bf16)hi}; return __builtin_bit_cast(unsigned, v); }
DI unsigned canon(unsigned u) { return u < 0xFFFFFFFEu ? u : 0xFFFFFFFEu; }
DI float dot2(unsigned a, unsigned b, float c) { return __builtin_amdgcn_fdot2_f32_bf16(__builtin_bit_cast(bf16x2_t, a), __builtin_bit_cast(bf16x2_t, b), c, false); }
DI unsigned subsat(unsigned a, unsigned b) { return __builtin_bit_cast(unsigned, __builtin_elementwise_sub_sat(__builtin_bit_cast(u16x2, a), __builtin_bit_cast(u16x2, b))); }
DI unsigned umax3(unsigned a, unsigned b, unsigned c) { return max(max(a, b), c); }
DI f32x4 mfma(v4u a, v4u b, f32x4 c) { return __builtin_amdgcn_mfma_f32_16x16x32_bf16(__builtin_bit_cast(bf16x8, a), __builtin_bit_cast(bf16x8, b), c, 0, 0, 0); }
template <int CTRL> DI float dppf(float v) { return u2f((unsigned)__builtin_amdgcn_update_dpp(0, (int)f2u(v), CTRL, 0xf, 0xf, true)); }
template <int CTRL> DI unsigned dppu(unsigned v) { return (unsigned)__builtin_amdgcn_update_dpp(0, (int)v, CTRL, 0xf, 0xf, true); }
template <int CTRL, int RM> DI float dppm(float v) { return u2f((unsigned)__builtin_amdgcn_update_dpp(0, (int)f2u(v), CTRL, RM, 0xf, false)); }
DI float quad_sum(float v) { v += dppf<0xB1>(v); v += dppf<0x4E>(v); return v; }
DI float xg_sum(float v) {
  const auto a = __builtin_amdgcn_permlane16_swap(f2u(v), f2u(v), false, false);
  const unsigned a0 = a[0], a1 = a[1];
  v = u2f(a0) + u2f(a1);
  const auto b = __builtin_amdgcn_permlane32_swap(f2u(v), f2u(v), false, false);
  const unsigned b0 = b[0], b1 = b[1];
  return u2f(b0) + u2f(b1);
}
DI float x32_sum(float v) {
  const auto b = __builtin_amdgcn_permlane32_swap(f2u(v), f2u(v), false, false);
  const unsigned b0 = b[0], b1 = b[1];
  return u2f(b0) + u2f(b1);
}
DI float xg_max(float v) {
  const auto a = __builtin_amdgcn_permlane16_swap(f2u(v), f2u(v), false, false);
  const unsigned a0 = a[0], a1 = a[1];
  v = fmaxf(u2f(a0), u2f(a1));
  const auto b = __builtin_amdgcn_permlane32_swap(f2u(v), f2u(v), false, false);
  const unsigned b0 = b[0], b1 = b[1];
  return fmaxf(u2f(b0), u2f(b1));
}
DI unsigned swz(unsigned r) { return (((r ^ ((r >> 1) & 4u)) & 7u) << 1) | ((r >> 3) & 1u); }

typedef __amdgpu_buffer_rsrc_t rsrc_t;
DI v4u ld128(rsrc_t rs, int off) { return __builtin_amdgcn_raw_buffer_load_b128(rs, off, 0, AUX_SC1); }
DI v2u ld64(rsrc_t rs, int off) { return __builtin_amdgcn_raw_buffer_load_b64(rs, off, 0, AUX_SC1); }
DI unsigned ld32(rsrc_t rs, int off) { return __builtin_amdgcn_raw_buffer_load_b32(rs, off, 0, AUX_SC1); }
DI void st128(rsrc_t rs, int off, v4u v) { __builtin_amdgcn_raw_buffer_store_b128(v, rs, off, 0, AUX_SC1); }
DI void st64(rsrc_t rs, int off, v2u v) { __builtin_amdgcn_raw_buffer_store_b64(v, rs, off, 0, AUX_SC1); }
DI void st32(rsrc_t rs, int off, unsigned v) { __builtin_amdgcn_raw_buffer_store_b32(v, rs, off, 0, AUX_SC1); }
#define CBAR() asm volatile("" ::: "memory")
#define DRAIN() asm volatile("s_waitcnt vmcnt(0)" ::: "memory")
#define LBAR() do { asm volatile("s_waitcnt lgkmcnt(0)" ::: "memory"); __builtin_amdgcn_s_barrier(); asm volatile("" ::: "memory"); } while (0)

DI void tr8x8(const v4u (&r)[8], v4u* f) {
#pragma unroll
  for (int q = 0; q < 4; ++q)
#pragma unroll
    for (int d = 0; d < 4; ++d) {
      const unsigned a = r[2 * d][q], b = r[2 * d + 1][q];
      f[2 * q][d] = __builtin_amdgcn_perm(b, a, 0x05040100u);
      f[2 * q + 1][d] = __builtin_amdgcn_perm(b, a, 0x07060302u);
    }
}
DI void tr8x4(const v2u (&r)[8], v4u* f) {
#pragma unroll
  for (int q = 0; q < 2; ++q)
#pragma unroll
    for (int d = 0; d < 4; ++d) {
      const unsigned a = r[2 * d][q], b = r[2 * d + 1][q];
      f[2 * q][d] = __builtin_amdgcn_perm(b, a, 0x05040100u);
      f[2 * q + 1][d] = __builtin_amdgcn_perm(b, a, 0x07060302u);
    }
}

#define STAMP(c, i) do {} while (0)
struct Ctx {
  unsigned char* wsp;
  rsrc_t rs; LAS unsigned char* L;
  int lane, w, b, G, s, blk;
  unsigned spins;
  unsigned kLO1, kHI1, kONES, kNLO1, kNHI1;
  v4u nonce;
};
DI unsigned sconst(unsigned v) { unsigned r; asm volatile("s_mov_b32 %0, %1" : "=s"(r) : "i"(v)); return r; }
#define RELANE(c) do { int l_ = (c).lane; asm volatile("" : "+v"(l_)); (c).lane = l_; } while (0)
DI bool spin_ok(Ctx& c) { return ++c.spins < SPIN_CAP; }

DI int q_blk(int b, int h, int ch) { return (h >> 2) + 8 * (8 * b + 2 * (h & 3) + ch); }

DI bool nonce_eq(v4u a, v4u n) { return ((a.x ^ n.x) | (a.y ^ n.y) | (a.z ^ n.z) | (a.w ^ n.w)) == 0u; }
DI void wait_ready_a(Ctx& c, v4u nonce) {
  const int q = c.lane & 3;
  const int off = OFF_RDY + q_blk(c.b, 16 * c.G + c.w + 8 * (q >> 1), q & 1) * 64;
  for (;;) {
    CBAR();
    const v4u a = ld128(c.rs, off);
    if (__all(nonce_eq(a, nonce)) || !spin_ok(c)) break;
  }
  CBAR();
}
DI void wait_ready_b(Ctx& c, v4u nonce) {
  const int o0 = OFF_RDY + (c.b * 64 + c.lane) * 64 + 16, o1 = o0 + 128 * 64;
  for (;;) {
    CBAR();
    const v4u a = ld128(c.rs, o0), bq = ld128(c.rs, o1);
    if (__all(nonce_eq(a, nonce) & nonce_eq(bq, nonce)) || !spin_ok(c)) break;
  }
  CBAR();
}

template <int D>
DI void gate(Ctx& c, int off) {
  if (c.w == 0) {
    if (off >= 0) {
      for (;;) {
        CBAR();
        const unsigned v = __builtin_amdgcn_raw_buffer_load_b32(c.rs, off, 0, AUX_SC1);
        if (__builtin_amdgcn_readfirstlane(v) != SENT || !spin_ok(c)) break;
        __builtin_amdgcn_s_sleep(4);
      }
    }
    if (D > 0) __builtin_amdgcn_s_sleep(D);
  }
  __builtin_amdgcn_s_barrier();
}
#define D_F3 40
#define D_Q4 30
#define D_A1 30
#define D_FA1 45

template <bool RES_WS>
DI unsigned gather33(Ctx& c, int base, const u16* xin) {
  const int co = 128 * c.w + 4 * (c.lane & 31);
  unsigned v[16], res;
  if (!RES_WS) res = *(const GAS unsigned*)((const GAS unsigned char*)xin + co);
  const int o = base + (c.lane >> 5) * 1024 + co;
  for (;;) {
    CBAR();
#pragma unroll
    for (int i = 0; i < 16; ++i) v[i] = ld32(c.rs, o + i * 2048);
    if (RES_WS) res = ld32(c.rs, base + 32 * 1024 + co);
    unsigned m = RES_WS ? res : 0u;
#pragma unroll
    for (int i = 0; i < 16; i += 2) m = umax3(m, v[i], v[i + 1]);
    if (__all(m != SENT) || !spin_ok(c)) break;
  }
  float s0 = 0.f, s1 = 0.f, s2 = 0.f, s3 = 0.f;
#pragma unroll
  for (int i = 0; i < 16; i += 2) { s0 = dot2(v[i], LO1, s0); s1 = dot2(v[i], HI1, s1); s2 = dot2(v[i + 1], LO1, s2); s3 = dot2(v[i + 1], HI1, s3); }
  const unsigned o01 = pack2(x32_sum(s0 + s2), x32_sum(s1 + s3));
  return pack2(dot2(o01, LO1, bf_lo(res)), dot2(o01, HI1, bf_hi(res)));
}

DI void layernorm(Ctx& c, unsigned xp, v4u& a0, v4u& a1) {
  float t1 = dot2(xp, ONES, 0.f), t2 = dot2(xp, xp, 0.f);
  t1 += dppf<0x121>(t1); t2 += dppf<0x121>(t2);
  t1 += dppf<0x122>(t1); t2 += dppf<0x122>(t2);
  t1 += dppf<0x124>(t1); t2 += dppf<0x124>(t2);
  t1 += dppf<0x128>(t1); t2 += dppf<0x128>(t2);
  t1 += dppm<0x142, 0xa>(t1); t2 += dppm<0x142, 0xa>(t2);
  if (c.lane == 31) *(LAS f32x2*)(c.L + L_LN + c.w * 8) = f32x2{t1, t2};
  LBAR();
  f32x4 t[4];
#pragma unroll
  for (int i = 0; i < 4; ++i) t[i] = *(const LAS f32x4*)(c.L + L_LN + i * 16);
  const f32x4 tt = (t[0] + t[1]) + (t[2] + t[3]);
  const float mean = (tt.x + tt.z) * (1.0f / 512.0f);
  const float var = fmaxf((tt.y + tt.w) * (1.0f / 512.0f) - mean * mean, 0.f);
  const unsigned mv = pack2(mean, var);
  const float nmb = -bf_lo(mv);
  const float sd = rbf(__builtin_amdgcn_sqrtf(rbf(bf_hi(mv) + 1e-5f)));
  const float ri = __builtin_amdgcn_rcpf(sd);
  const unsigned d01 = pack2(dot2(xp, LO1, nmb), dot2(xp, HI1, nmb));
  const unsigned n01 = pack2(bf_lo(d01) * ri, bf_hi(d01) * ri);
  *(LAS unsigned*)(c.L + L_FR + c.w * 256 + c.lane * 4) = n01;
  a0 = *(const LAS v4u*)(c.L + L_FR + c.w * 256 + (c.lane >> 4) * 32);
  a1 = *(const LAS v4u*)(c.L + L_FR + c.w * 256 + (c.lane >> 4) * 32 + 16);
}

template <bool HILO>
DI void gemv_ksplit(Ctx& c, v4u a0, v4u a1, const v4u* W) {
  f32x4 acc[8];
#pragma unroll
  for (int t = 0; t < 8; ++t) acc[t] = mfma(a0, W[t], f32x4{0.f, 0.f, 0.f, 0.f});
#pragma unroll
  for (int t = 0; t < 8; ++t) acc[t] = mfma(a1, W[8 + t], acc[t]);
  if (c.lane < 16) {
    f32x4 o0, o1;
#pragma unroll
    for (int t = 0; t < 4; ++t) { o0[t] = HILO ? acc[t].x + acc[t].y : acc[t].x; o1[t] = HILO ? acc[4 + t].x + acc[4 + t].y : acc[4 + t].x; }
    *(LAS f32x4*)(c.L + L_RED + c.w * 512 + c.lane * 32) = o0;
    *(LAS f32x4*)(c.L + L_RED + c.w * 512 + c.lane * 32 + 16) = o1;
  }
}
DI float red_sum8(Ctx& c, int idx) {
  const LAS float* R = (const LAS float*)(c.L + L_RED);
  return ((R[idx] + R[128 + idx]) + (R[256 + idx] + R[384 + idx])) + ((R[512 + idx] + R[640 + idx]) + (R[768 + idx] + R[896 + idx]));
}
DI void reduce128(Ctx& c) {
  LBAR();
  if (c.w < 2) { const int d = c.w * 64 + c.lane; *(LAS __bf16*)(c.L + L_VEC + d * 2) = (__bf16)red_sum8(c, d); }
  LBAR();
}

template <int KB>
DI void gemv_nsplit(Ctx& c, const v4u* W, int off) {
  const int n = c.lane & 15, g = c.lane >> 4;
  f32x4 acc[8];
#pragma unroll
  for (int kb = 0; kb < KB; ++kb) {
    const v4u a = *(const LAS v4u*)(c.L + L_VEC + kb * 128 + (n & 1) * 64 + g * 16);
#pragma unroll
    for (int t = 0; t < 8; ++t) acc[t] = mfma(a, W[8 * kb + t], kb == 0 ? f32x4{0.f, 0.f, 0.f, 0.f} : acc[t]);
  }
  float r[8];
#pragma unroll
  for (int t = 0; t < 8; ++t) r[t] = acc[t].x + dppf<0x108>(acc[t].y);
  if (c.lane < 8) st128(c.rs, off + 128 * c.w + 16 * c.lane, v4u{canon(pack2(r[0], r[1])), canon(pack2(r[2], r[3])), canon(pack2(r[4], r[5])), canon(pack2(r[6], r[7]))});
}

DI float score_round(float s) {
  const float SC = 0.08838834764831845f;
  const unsigned bits = f2u(s);
  const unsigned tr = bits & 0xFFFF0000u;
  const unsigned g2 = pack2(u2f(tr) * SC, u2f(tr + 0x10000u) * SC);
  const float ga = bf_lo(g2);
  const float wgt = fminf(fmaxf((float)(bits & 0xFFFFu) * (1.0f / (65536.0f * 0.6f)) + (0.5f - 0.5f / 0.6f), 0.f), 1.f);
  return ga + wgt * (bf_hi(g2) - ga);
}

DI void attn_phase(Ctx& c, int l) {
  RELANE(c);
  const int tid = c.w * 64 + c.lane, n = c.lane & 15, g = c.lane >> 4;
  v4u kf[16];
  {
    const unsigned kb = L_IMG + (c.w * 16 + n) * 1024 + (((unsigned)g ^ swz(n)) << 4);
#pragma unroll
    for (int ks = 0; ks < 16; ++ks) kf[ks] = *(const LAS v4u*)(c.L + ((kb ^ (64u * (ks & 3))) + 256u * (ks >> 2)));
  }
  unsigned ab;
  {
    const unsigned q4 = (c.lane & 15) >> 2, p4 = c.lane & 3;
    const unsigned key = 4u * g + q4;
    const unsigned chunk = 8u * c.w + (p4 >> 1);
    ab = L_IMG + key * 1024 + ((chunk ^ swz(key)) << 4) + 8u * (p4 & 1);
    asm volatile("" : "+v"(ab));
  }
  {
    const int base = OFF_X1 + ((l * 2 + c.b) * 32 + 16 * c.G) * 1024 + tid * 16;
    v4u v0, v1;
    for (;;) {
      CBAR();
      v0 = ld128(c.rs, base);
      v1 = ld128(c.rs, base + 8192);
      const unsigned m = max(umax3(umax3(v0.x, v0.y, v0.z), v0.w, v1.x), umax3(v1.y, v1.z, v1.w));
      if (__all(m != SENT) || !spin_ok(c)) break;
    }
    STAMP(c, 2 + 8 * l);
    *(LAS v4u*)(c.L + L_QA + c.w * 1024 + (((unsigned)c.lane ^ swz(c.w)) << 4)) = v0;
    *(LAS v4u*)(c.L + L_QA + (c.w + 8) * 1024 + (((unsigned)c.lane ^ swz(c.w + 8)) << 4)) = v1;
  }
  LBAR();
  if (l == 0 && c.w == 0 && c.lane == 0) st128(c.rs, OFF_RDY + c.blk * 64 + 16, c.nonce);
  STAMP(c, 26 + l);
  f32x4 sacc = {0.f, 0.f, 0.f, 0.f}, sacc2 = {0.f, 0.f, 0.f, 0.f};
  {
    const unsigned qb = L_QA + n * 1024 + (((unsigned)g ^ swz(n)) << 4);
    constexpr int QD = 4;
    v4u qf[QD];
#pragma unroll
    for (int ks = 0; ks < QD; ++ks) qf[ks] = *(const LAS v4u*)(c.L + ((qb ^ (64u * (ks & 3))) + 256u * (ks >> 2)));
    __builtin_amdgcn_sched_barrier(0);
#pragma unroll
    for (int ks = 0; ks < 16; ++ks) {
      if (ks & 1) sacc2 = mfma(kf[ks], qf[ks % QD], sacc2); else sacc = mfma(kf[ks], qf[ks % QD], sacc);
      if (ks + QD < 16) qf[ks % QD] = *(const LAS v4u*)(c.L + ((qb ^ (64u * ((ks + QD) & 3))) + 256u * ((ks + QD) >> 2)));
      __builtin_amdgcn_sched_barrier(0);
    }
  }
  if (l == 1) { asm volatile("" :: "v"(sacc), "v"(sacc2)); STAMP(c, 17); }
  v2u vf[4][4][2];
  auto vf_issue = [&](int u) __attribute__((always_inline)) {
#pragma unroll
    for (int t = 0; t < 4; ++t) {
      const unsigned ad = (ab ^ (32u * t)) + 32768u * u;
      const s16x4 lo = __builtin_amdgcn_ds_read_tr16_b64_v4i16((LAS s16x4*)(c.L + ad));
      const s16x4 hi = __builtin_amdgcn_ds_read_tr16_b64_v4i16((LAS s16x4*)(c.L + ad + 16384u));
      vf[u][t][0] = __builtin_bit_cast(v2u, lo); vf[u][t][1] = __builtin_bit_cast(v2u, hi);
    }
  };
  vf_issue(0); vf_issue(1);
  __builtin_amdgcn_sched_barrier(0);
  {
    const float LOG2E = 1.4426950408889634f;
    float sv[4];
#pragma unroll
    for (int q = 0; q < 4; ++q) sv[q] = score_round(sacc[q] + sacc2[q]) * LOG2E;
    const float kw = ceilf(xg_max(fmaxf(fmaxf(sv[0], sv[1]), fmaxf(sv[2], sv[3]))));
    float p[4];
#pragma unroll
    for (int q = 0; q < 4; ++q) p[q] = __builtin_amdgcn_exp2f(sv[q] - kw);
    const float lw = xg_sum((p[0] + p[1]) + (p[2] + p[3]));
    *(LAS v2u*)(c.L + L_PB + (c.w >> 1) * 1024 + c.lane * 16 + (c.w & 1) * 8) = v2u{pack2(p[0], p[1]), pack2(p[2], p[3])};
    if (g == 0) *(LAS v2u*)(c.L + L_SB + n * 64 + c.w * 8) = v2u{(unsigned)(int)kw, f2u(lw)};
  }
  if (l == 1) STAMP(c, 18);
  LBAR();
  STAMP(c, 3 + 8 * l);
  f32x4 acc[4];
  {
    v4u sb[4], pr[4];
#pragma unroll
    for (int i = 0; i < 4; ++i) sb[i] = *(const LAS v4u*)(c.L + L_SB + n * 64 + i * 16);
#pragma unroll
    for (int u = 0; u < 4; ++u) pr[u] = *(const LAS v4u*)(c.L + L_PB + u * 1024 + c.lane * 16);
    vf_issue(2); vf_issue(3);
    __builtin_amdgcn_sched_barrier(0);
    int K = max(max(max((int)sb[0].x, (int)sb[0].z), max((int)sb[1].x, (int)sb[1].z)), max(max((int)sb[2].x, (int)sb[2].z), max((int)sb[3].x, (int)sb[3].z)));
    if (c.w == 0) {
      float Ls = 0.f;
#pragma unroll
      for (int i = 0; i < 4; ++i) Ls += ldexpf(u2f(sb[i].y), (int)sb[i].x - K) + ldexpf(u2f(sb[i].w), (int)sb[i].z - K);
      if (g == 0) st64(c.rs, OFF_ST + (((l * 2 + c.b) * 32 + 16 * c.G + n) * 64 + c.s) * 8, v2u{canon(f2u((float)K)), canon(f2u(Ls))});
    }
#pragma unroll
    for (int t = 0; t < 4; ++t) acc[t] = f32x4{0.f, 0.f, 0.f, 0.f};
#pragma unroll
    for (int u = 0; u < 4; ++u) {
      const unsigned d0 = __umul24((unsigned)min(K - (int)sb[u].x, 255), 0x00800080u), d1 = __umul24((unsigned)min(K - (int)sb[u].z, 255), 0x00800080u);
      const v4u pb = {subsat(pr[u].x, d0), subsat(pr[u].y, d0), subsat(pr[u].z, d1), subsat(pr[u].w, d1)};
#pragma unroll
      for (int t = 0; t < 4; ++t) {
        const v4u af = {vf[u][t][0].x, vf[u][t][0].y, vf[u][t][1].x, vf[u][t][1].y};
        acc[t] = mfma(pb, af, acc[t]);
      }
    }
  }
  if (l == 1) { asm volatile("" :: "v"(acc[0]), "v"(acc[1]), "v"(acc[2]), "v"(acc[3])); STAMP(c, 19); }
  {
    const int base = OFF_X2 + ((l * 2 + c.b) * 32 + 16 * c.G + 4 * g) * 65536 + c.w * 8192 + c.s * 128 + n * 8;
#pragma unroll
    for (int q = 0; q < 4; ++q)
      st64(c.rs, base + q * 65536, v2u{pack2(acc[0][q], acc[1][q]), pack2(acc[2][q], acc[3][q])});
  }
  DRAIN();
  if (c.lane == 0) st32(c.rs, OFF_FW + ((((l * 2 + c.b) * 2 + c.G) * 8 + c.w) * 64 + c.s) * 4, 1u);
  STAMP(c, 4 + 8 * l);
}

DI void fill_attn(Ctx& c) {
  const int n = c.lane & 15, g = c.lane >> 4;
  const v4u sv = {SENT, SENT, SENT, SENT};
#pragma unroll
  for (int l = 0; l < 2; ++l) {
    if (c.w == 0 && g == 0) st64(c.rs, OFF_ST + (((l * 2 + c.b) * 32 + 16 * c.G + n) * 64 + c.s) * 8, v2u{SENT, SENT});
    if (c.lane == 0) st32(c.rs, OFF_FW + ((((l * 2 + c.b) * 2 + c.G) * 8 + c.w) * 64 + c.s) * 4, SENT);
  }
}

DI void ckv_dma(Ctx& c, const Params& p) {
  const GAS unsigned char* src = (const GAS unsigned char*)p.ckv + ((size_t)c.b * 8192 + c.s * 128 + c.w * 16) * 1024;
#pragma unroll
  for (int i = 0; i < 16; ++i)
    __builtin_amdgcn_global_load_lds((const GAS unsigned*)(src + i * 1024 + (((unsigned)c.lane ^ swz(i)) << 4)),
                                     (LAS unsigned*)(c.L + L_IMG + (c.w * 16 + i) * 1024), 16, 0, 0);
}
DI void ckv_issue(Ctx& c, const Params& p, v4u (&t)[16]) {
  const GAS unsigned char* src = (const GAS unsigned char*)p.ckv + ((size_t)c.b * 8192 + c.s * 128 + c.w * 16) * 1024 + c.lane * 16;
#pragma unroll
  for (int i = 0; i < 16; ++i) t[i] = *(const GAS v4u*)(src + i * 1024);
}
DI void ckv_stage(Ctx& c, const v4u (&t)[16]) {
#pragma unroll
  for (int i = 0; i < 16; ++i)
    *(LAS v4u*)(c.L + L_IMG + (c.w * 16 + i) * 1024 + (((unsigned)c.lane ^ swz(i)) << 4)) = t[i];
}
DI void publish_ready_b(Ctx& c, v4u nonce) {
  LBAR();
}

DI void role_q(Ctx& c, const Params& p, v4u nonce, int h, int ch) {
  asm volatile("; role Q" ::: "memory");
  const int n = c.lane & 15, g = c.lane >> 4;
  const v4u sv = {SENT, SENT, SENT, SENT};
  if (c.w == 0) st128(c.rs, OFF_X1 + (((c.lane >> 5) * 2 + c.b) * 32 + h) * 1024 + ch * 512 + (c.lane & 31) * 16, sv);
  unsigned xp = *(const GAS unsigned*)((const GAS unsigned char*)p.x + c.b * 1024 + 128 * c.w + 4 * (c.lane & 31));
  v4u rq[2][8], ru[8];
  {
    const GAS unsigned char* wq = (const GAS unsigned char*)p.wq + ((size_t)(64 * c.w + 16 * g) * 4096 + h * 128 + 8 * n) * 2;
#pragma unroll
    for (int ks = 0; ks < 2; ++ks)
#pragma unroll
      for (int j = 0; j < 8; ++j) rq[ks][j] = *(const GAS v4u*)(wq + (size_t)(8 * ks + j) * 8192);
    const GAS unsigned char* wu = (const GAS unsigned char*)p.wuk + ((size_t)(256 * ch + 32 * c.w + g) * 4096 + h * 128 + 8 * n) * 2;
#pragma unroll
    for (int i = 0; i < 8; ++i) ru[i] = *(const GAS v4u*)(wu + (size_t)(4 * i) * 8192);
  }
  v4u ct[16];
  __builtin_amdgcn_sched_barrier(0);
  asm volatile("" : "+v"(xp));
  STAMP(c, 9);
  if (c.w == 0 && c.lane == 0) st128(c.rs, OFF_RDY + c.blk * 64, nonce);
  v4u Wq[16];
  const bool xpub = (h == 0 && ch == 0);
  auto q_phase = [&](int l) __attribute__((always_inline)) {
    v4u a0, a1;
    layernorm(c, xp, a0, a1);
    if (l == 0) { ckv_issue(c, p, ct); __builtin_amdgcn_sched_barrier(0); STAMP(c, 7); tr8x8(rq[0], Wq); tr8x8(rq[1], Wq + 8); asm volatile("" :: "v"(Wq[15])); STAMP(c, 22); }
    gemv_ksplit<false>(c, a0, a1, Wq);
    if (l == 0) {
#pragma unroll
      for (int i = 0; i < 8; ++i) { const unsigned row = 4 * i + g; *(LAS v4u*)(c.L + L_IMG + c.w * 8192 + row * 256 + (((unsigned)n ^ swz(row >> 1)) << 4)) = ru[i]; }
#pragma unroll
      for (int t = 0; t < 2; ++t)
#pragma unroll
        for (int ks = 0; ks < 4; ++ks) ru[t * 4 + ks] = *(const LAS v4u*)(c.L + L_IMG + c.w * 8192 + (2 * n + t) * 256 + (((unsigned)(4 * ks + g) ^ swz(n)) << 4));
    }
    reduce128(c);
    if (l == 0) STAMP(c, 23);
    f32x4 acc0 = {0.f, 0.f, 0.f, 0.f}, acc1 = {0.f, 0.f, 0.f, 0.f};
#pragma unroll
    for (int ks = 0; ks < 4; ++ks) {
      const v4u a = *(const LAS v4u*)(c.L + L_VEC + ks * 64 + g * 16);
      acc0 = mfma(a, ru[ks], acc0);
      acc1 = mfma(a, ru[4 + ks], acc1);
    }
    if (c.lane < 16) st32(c.rs, OFF_X1 + ((l * 2 + c.b) * 32 + h) * 1024 + (256 * ch + 32 * c.w + 2 * n) * 2, canon(pack2(acc0.x, acc1.x)));
  };
  q_phase(0);
  STAMP(c, 21);
  wait_ready_a(c, nonce);
  fill_attn(c);
  if (xpub && c.w == 1) st128(c.rs, OFF_X3 + (1 * 2 + c.b) * XV_STR + 32 * 1024 + c.lane * 16, sv);
  ckv_stage(c, ct);
  publish_ready_b(c, nonce);
  STAMP(c, 20);
  STAMP(c, 1);
#pragma unroll 1
  for (int l = 0; l < 2; ++l) {
    attn_phase(c, l);
    if (l == 0) {
      wait_ready_b(c, nonce);
      gate<D_Q4>(c, OFF_X3 + (0 * 2 + c.b) * XV_STR + h * 1024);
      xp = gather33<true>(c, OFF_X4 + (0 * 2 + c.b) * XV_STR, nullptr);
      STAMP(c, 5);
      if (xpub && c.lane < 32) st32(c.rs, OFF_X3 + (1 * 2 + c.b) * XV_STR + 32 * 1024 + 128 * c.w + 4 * c.lane, canon(xp));
      q_phase(1);
      STAMP(c, 6);
    }
  }
  gate<D_Q4>(c, OFF_X3 + (1 * 2 + c.b) * XV_STR + h * 1024);
  if (c.w == 0) {
    const int qi = 2 * h + ch;
    const int off = OFF_X4 + (1 * 2 + c.b) * XV_STR + (c.lane < 33 ? c.lane : 32) * 1024 + qi * 16;
    v4u v;
    for (;;) {
      CBAR();
      v = ld128(c.rs, off);
      if (__all(max(umax3(v.x, v.y, v.z), v.w) != SENT) || !spin_ok(c)) break;
    }
    const unsigned wl = c.lane < 32 ? LO1 : 0u, wh = c.lane < 32 ? HI1 : 0u;
    v4u o;
#pragma unroll
    for (int q = 0; q < 4; ++q) {
      float lo = dot2(v[q], wl, 0.f), hi = dot2(v[q], wh, 0.f);
      lo = quad_sum(lo); hi = quad_sum(hi);
      lo += dppf<0x124>(lo); hi += dppf<0x124>(hi);
      lo += dppf<0x128>(lo); hi += dppf<0x128>(hi);
      lo += dppm<0x142, 0xa>(lo); hi += dppm<0x142, 0xa>(hi);
      const unsigned s2 = (unsigned)__builtin_amdgcn_readlane((int)pack2(lo, hi), 31);
      const unsigned r2 = (unsigned)__builtin_amdgcn_readlane((int)v[q], 32);
      o[q] = pack2(bf_lo(r2) + bf_lo(s2), bf_hi(r2) + bf_hi(s2));
    }
    if (c.lane == 0) *(GAS v4u*)((GAS unsigned char*)p.out + c.b * 1024 + qi * 16) = o;
    STAMP(c, 16);
  }
}

DI void role_o(Ctx& c, const Params& p, v4u nonce, int h) {
  asm volatile("; role O" ::: "memory");
  const int n = c.lane & 15, g = c.lane >> 4;
  const v4u sv = {SENT, SENT, SENT, SENT};
  ckv_dma(c, p);
  DRAIN();
  __builtin_amdgcn_sched_barrier(0);
  v4u rv[2][8], ro[2][8];
  {
    const GAS unsigned char* wv = (const GAS unsigned char*)p.wuv + ((size_t)(64 * c.w + 2 * g) * 4096 + h * 128 + 8 * n) * 2;
#pragma unroll
    for (int ks = 0; ks < 2; ++ks)
#pragma unroll
      for (int j = 0; j < 8; ++j) rv[ks][j] = *(const GAS v4u*)(wv + (size_t)(16 * (j & 3) + 8 * ks + (j >> 2)) * 8192);
    const GAS unsigned char* wo = (const GAS unsigned char*)p.wo + ((size_t)(h * 128 + 32 * (n >> 3) + 8 * g) * 512 + 64 * c.w + 8 * (n & 7)) * 2;
#pragma unroll
    for (int kb = 0; kb < 2; ++kb)
#pragma unroll
      for (int j = 0; j < 8; ++j) ro[kb][j] = *(const GAS v4u*)(wo + (size_t)(64 * kb + j) * 1024);
  }
  __builtin_amdgcn_sched_barrier(0);
  fill_attn(c);
  if (c.w < 2) st128(c.rs, OFF_X3 + (c.w * 2 + c.b) * XV_STR + h * 1024 + c.lane * 16, sv);
  v4u Wv[16], Wo[16];
  tr8x8(rv[0], Wv); tr8x8(rv[1], Wv + 8);
  tr8x8(ro[0], Wo); tr8x8(ro[1], Wo + 8);
  publish_ready_b(c, nonce);
  STAMP(c, 20);
  wait_ready_a(c, nonce);
  STAMP(c, 1);
#pragma unroll 1
  for (int l = 0; l < 2; ++l) {
    attn_phase(c, l);
    if (l == 0) wait_ready_b(c, nonce);
    float inv;
    v2u v[16];
    {
      const int bs = OFF_ST + ((l * 2 + c.b) * 32 + h) * 512 + c.lane * 8;
      v2u st;
      for (;;) {
        CBAR();
        st = ld64(c.rs, bs);
        if (__all(max(st.x, st.y) != SENT) || !spin_ok(c)) break;
      }
      const float kf = u2f(st.x);
      float K = kf;
      K = fmaxf(K, dppf<0x128>(K)); K = fmaxf(K, dppf<0x124>(K)); K = fmaxf(K, dppf<0x122>(K)); K = fmaxf(K, dppf<0x121>(K));
      K = xg_max(K);
      const int d = (int)(K - kf);
      const unsigned eb = d < 127 ? (unsigned)(127 - d) << 7 : 0u;
      float Ls = u2f(st.y) * u2f(eb << 16);
      Ls += dppf<0x128>(Ls); Ls += dppf<0x124>(Ls); Ls += dppf<0x122>(Ls); Ls += dppf<0x121>(Ls);
      Ls = xg_sum(Ls);
      inv = 1.0f / Ls;
      *(LAS unsigned*)(c.L + L_EW + c.w * 256 + c.lane * 4) = eb;
    }
    unsigned el[16];
#pragma unroll
    for (int i = 0; i < 4; ++i) {
      const v4u e4 = *(const LAS v4u*)(c.L + L_EW + c.w * 256 + g * 64 + i * 16);
      el[4 * i] = e4.x; el[4 * i + 1] = e4.y; el[4 * i + 2] = e4.z; el[4 * i + 3] = e4.w;
    }
    {
      const int bf = OFF_FW + ((((l * 2 + c.b) * 2 + (h >> 4)) * 8 + c.w) * 64 + c.lane) * 4;
      for (;;) {
        CBAR();
        const unsigned fw = ld32(c.rs, bf);
        if (__all(fw != SENT) || !spin_ok(c)) break;
      }
      CBAR();
      const int bx = OFF_X2 + ((l * 2 + c.b) * 32 + h) * 65536 + c.w * 8192 + g * 2048 + n * 8;
#pragma unroll
      for (int i = 0; i < 16; ++i) v[i] = ld64(c.rs, bx + i * 128);
    }
    STAMP(c, 5 + 8 * l);
    float s0 = 0.f, s1 = 0.f, s2 = 0.f, s3 = 0.f;
#pragma unroll
    for (int i = 0; i < 16; ++i) {
      const unsigned eh = el[i] << 16;
      s0 = dot2(v[i].x, el[i], s0); s1 = dot2(v[i].x, eh, s1); s2 = dot2(v[i].y, el[i], s2); s3 = dot2(v[i].y, eh, s3);
    }
    s0 = xg_sum(s0) * inv; s1 = xg_sum(s1) * inv; s2 = xg_sum(s2) * inv; s3 = xg_sum(s3) * inv;
    const unsigned h01 = pack2(s0, s1), h23 = pack2(s2, s3);
    const unsigned l01 = pack2(dot2(h01, NLO1, s0), dot2(h01, NHI1, s1)), l23 = pack2(dot2(h23, NLO1, s2), dot2(h23, NHI1, s3));
    *(LAS v2u*)(c.L + L_FR + c.w * 256 + (g & 1) * 128 + n * 8) = (g & 1) ? v2u{l01, l23} : v2u{h01, h23};
    const v4u a0 = *(const LAS v4u*)(c.L + L_FR + c.w * 256 + (n & 1) * 128 + g * 16);
    const v4u a1 = *(const LAS v4u*)(c.L + L_FR + c.w * 256 + (n & 1) * 128 + 64 + g * 16);
    asm volatile("" :: "v"(a0), "v"(a1));
    STAMP(c, 7 + 8 * l);
    gemv_ksplit<true>(c, a0, a1, Wv);
    STAMP(c, 22 + 2 * l);
    reduce128(c);
    STAMP(c, 23 + 2 * l);
    gemv_nsplit<2>(c, Wo, OFF_X3 + (l * 2 + c.b) * XV_STR + h * 1024);
    STAMP(c, 6 + 8 * l);
    if (l == 0) gate<D_A1>(c, OFF_X4 + (0 * 2 + c.b) * XV_STR + h * 1024);
  }
}

DI void role_f(Ctx& c, const Params& p, v4u nonce, int j) {
  asm volatile("; role F" ::: "memory");
  const int n = c.lane & 15, g = c.lane >> 4;
  const v4u sv = {SENT, SENT, SENT, SENT};
  ckv_dma(c, p);
  DRAIN();
  __builtin_amdgcn_sched_barrier(0);
  v4u r13[2][8], r2[8];
  {
    const int nn = n < 14 ? n : 13;
    const u16* wsel = nn < 7 ? p.w1 : p.w3;
    const int col = 56 * j + 8 * (nn < 7 ? nn : nn - 7);
    const GAS unsigned char* wa = (const GAS unsigned char*)wsel + ((size_t)(64 * c.w + 16 * g) * 1792 + col) * 2;
#pragma unroll
    for (int ks = 0; ks < 2; ++ks)
#pragma unroll
      for (int jj = 0; jj < 8; ++jj) r13[ks][jj] = *(const GAS v4u*)(wa + (size_t)(8 * ks + jj) * 3584);
#pragma unroll
    for (int jj = 0; jj < 8; ++jj) {
      int f = 32 * (n >> 3) + 8 * g + jj; f = f < 56 ? f : 55;
      r2[jj] = *(const GAS v4u*)((const GAS unsigned char*)p.w2 + ((size_t)(56 * j + f) * 512 + 64 * c.w + 8 * (n & 7)) * 2);
    }
  }
  __builtin_amdgcn_sched_barrier(0);
  fill_attn(c);
  if (c.w < 2) st128(c.rs, OFF_X4 + (c.w * 2 + c.b) * XV_STR + j * 1024 + c.lane * 16, sv);
  if (j == 0 && (c.w == 2 || c.w == 3)) st128(c.rs, OFF_X4 + ((c.w - 2) * 2 + c.b) * XV_STR + 32 * 1024 + c.lane * 16, sv);
  v4u W13[16], W2[8];
  tr8x8(r13[0], W13); tr8x8(r13[1], W13 + 8);
  tr8x8(r2, W2);
  publish_ready_b(c, nonce);
  STAMP(c, 20);
  wait_ready_a(c, nonce);
  STAMP(c, 1);
#pragma unroll 1
  for (int l = 0; l < 2; ++l) {
    attn_phase(c, l);
    unsigned xp;
    if (l == 0) wait_ready_b(c, nonce);
    gate<D_F3>(c, -1);
    if (l == 0) { xp = gather33<false>(c, OFF_X3 + (0 * 2 + c.b) * XV_STR, p.x + c.b * 512); }
    else xp = gather33<true>(c, OFF_X3 + (1 * 2 + c.b) * XV_STR, nullptr);
    STAMP(c, 5 + 8 * l);
    if (j == 0 && c.lane < 32) st32(c.rs, OFF_X4 + (l * 2 + c.b) * XV_STR + 32 * 1024 + 128 * c.w + 4 * c.lane, canon(xp));
    v4u a0, a1;
    layernorm(c, xp, a0, a1);
    STAMP(c, 7 + 8 * l);
    gemv_ksplit<false>(c, a0, a1, W13);
    STAMP(c, 22 + 2 * l);
    LBAR();
    STAMP(c, 8 + 8 * l);
    if (c.w == 0) {
      const int li = c.lane < 56 ? c.lane : 55;
      const unsigned gu = pack2(red_sum8(c, li), red_sum8(c, 56 + li));
      const float gt = bf_lo(gu);
      const float sg = rbf(gt * __builtin_amdgcn_rcpf(1.0f + __expf(-gt)));
      const float hv = c.lane < 56 ? sg * bf_hi(gu) : 0.f;
      *(LAS __bf16*)(c.L + L_VEC + c.lane * 2) = (__bf16)hv;
    }
    LBAR();
    STAMP(c, 23 + 2 * l);
    gemv_nsplit<1>(c, W2, OFF_X4 + (l * 2 + c.b) * XV_STR + j * 1024);
    STAMP(c, 6 + 8 * l);
    if (l == 0) gate<D_FA1>(c, -1);
  }
}

__global__ void __launch_bounds__(NT, 2) mega(Params p) {
  extern __shared__ __attribute__((aligned(16))) unsigned char smem[];
  Ctx c;
  c.rs = __builtin_amdgcn_make_buffer_rsrc((void*)p.ws, 0, WS_BYTES, 0x00020000);
  c.L = (LAS unsigned char*)smem;
  c.lane = threadIdx.x & 63;
  c.w = __builtin_amdgcn_readfirstlane(threadIdx.x >> 6);
  c.blk = blockIdx.x;
  const int x = c.blk & 7, slot = c.blk >> 3;
  c.b = (slot >> 3) & 1; c.G = slot >> 4; c.s = 8 * x + (slot & 7);
  const int r = 8 * c.G + (slot & 7);
  c.spins = 0; c.wsp = p.ws;
  c.kLO1 = sconst(0x00003F80u); c.kHI1 = sconst(0x3F800000u); c.kONES = sconst(0x3F803F80u); c.kNLO1 = sconst(0x0000BF80u); c.kNHI1 = sconst(0xBF800000u);
  STAMP(c, 0);
  const unsigned long long id = llvm_dispatch_id();
  const unsigned long long dp = (unsigned long long)__builtin_amdgcn_dispatch_ptr();
  const v4u nonce = {(unsigned)id ^ 0x5bd1e995u, (unsigned)(id >> 32) ^ 0x9e3779b9u, (unsigned)dp ^ 0x85ebca6bu, (unsigned)(dp >> 32) ^ 0xc2b2ae35u};
  c.nonce = nonce;
  if (r < 8) role_q(c, p, nonce, 4 * x + (r >> 1), r & 1);
  else if (r < 12) role_o(c, p, nonce, 4 * x + (r - 8));
  else role_f(c, p, nonce, 4 * x + (r - 12));
  if (c.w == 0 && c.lane < 2) st128(c.rs, OFF_RDY + c.blk * 64 + c.lane * 16, v4u{~nonce.x, ~nonce.y, ~nonce.z, ~nonce.w});
}
}

extern "C" void kernel_launch(void* const* d_in, const int* in_sizes, int n_in,
                              void* d_out, int out_size, void* d_ws, size_t ws_size,
                              hipStream_t stream) {
  mk::Params p;
  p.x = (const u16*)d_in[0]; p.ckv = (const u16*)d_in[1]; p.wq = (const u16*)d_in[2]; p.wuk = (const u16*)d_in[3];
  p.wuv = (const u16*)d_in[4]; p.wo = (const u16*)d_in[5]; p.w1 = (const u16*)d_in[7]; p.w3 = (const u16*)d_in[8];
  p.w2 = (const u16*)d_in[9]; p.out = (u16*)d_out; p.ws = (unsigned char*)d_ws;
  (void)hipFuncSetAttribute((const void*)mk::mega, hipFuncAttributeMaxDynamicSharedMemorySize, mk::L_BYTES + 2048);
  hipLaunchKernelGGL(mk::mega, dim3(256), dim3(mk::NT), mk::L_BYTES + 2048, stream, p);
}
```

```cpp
#include <hip/hip_runtime.h>
#include <hip/hip_bf16.h>
#include <stdint.h>

#define LAS __attribute__((address_space(3)))
#define GAS __attribute__((address_space(1)))
typedef unsigned short u16;
typedef unsigned v4u __attribute__((ext_vector_type(4)));
typedef unsigned v2u __attribute__((ext_vector_type(2)));
typedef float f32x4 __attribute__((ext_vector_type(4)));
typedef float f32x2 __attribute__((ext_vector_type(2)));
typedef short bf16x8 __attribute__((ext_vector_type(8)));
typedef short s16x4 __attribute__((ext_vector_type(4)));
typedef __bf16 bf16x2_t __attribute__((ext_vector_type(2)));
typedef unsigned short u16x2 __attribute__((ext_vector_type(2)));
#define DI __device__ __forceinline__

extern "C" __device__ unsigned long long llvm_dispatch_id() __asm("llvm.amdgcn.dispatch.id");

namespace mk {
constexpr int NT = 512;
constexpr unsigned SENT = 0xFFFFFFFFu;
constexpr unsigned SPIN_CAP = 1u << 17;
constexpr int AUX_SC1 = 16;
#define LO1 (c.kLO1)
#define HI1 (c.kHI1)
#define ONES (c.kONES)
#define NLO1 (c.kNLO1)
#define NHI1 (c.kNHI1)

constexpr int OFF_RDY = 0;
constexpr int OFF_X1 = 16384;
constexpr int OFF_ST = OFF_X1 + 2 * 2 * 32 * 1024;
constexpr int OFF_X3 = OFF_ST + 2 * 2 * 32 * 512;
constexpr int XV_STR = 34 * 1024;
constexpr int OFF_X4 = OFF_X3 + 4 * XV_STR;
constexpr int OFF_X2 = OFF_X4 + 4 * XV_STR;
constexpr int OFF_FW = OFF_X2 + 2 * 2 * 32 * 65536;
constexpr int WS_BYTES = OFF_FW + 16384;

constexpr int L_IMG = 0;
constexpr int L_QA = 131072;
constexpr int L_PB = L_QA + 16384;
constexpr int L_SB = L_PB + 4096;
constexpr int L_LN = L_SB + 1024;
constexpr int L_VEC = L_LN + 64;
constexpr int L_EW = L_VEC + 256;
constexpr int L_FR = L_EW + 2048;
constexpr int L_BYTES = L_FR + 2048;
constexpr int L_RED = L_QA;

struct Params {
  const u16* x; const u16* ckv; const u16* wq; const u16* wuk; const u16* wuv; const u16* wo;
  const u16* w1; const u16* w3; const u16* w2; u16* out; unsigned char* ws;
};

DI float u2f(unsigned u) { return __builtin_bit_cast(float, u); }
DI unsigned f2u(float f) { return __builtin_bit_cast(unsigned, f); }
DI float bf_lo(unsigned u) { return u2f(u << 16); }
DI float bf_hi(unsigned u) { return u2f(u & 0xFFFF0000u); }
DI float rbf(float f) { return (float)(__bf16)f; }
DI unsigned pack2(float lo, float hi) { bf16x2_t v = {(__bf16)lo, (__bf16)hi}; return __builtin_bit_cast(unsigned, v); }
DI unsigned canon(unsigned u) { return u < 0xFFFFFFFEu ? u : 0xFFFFFFFEu; }
DI float dot2(unsigned a, unsigned b, float c) { return __builtin_amdgcn_fdot2_f32_bf16(__builtin_bit_cast(bf16x2_t, a), __builtin_bit_cast(bf16x2_t, b), c, false); }
DI unsigned subsat(unsigned a, unsigned b) { return __builtin_bit_cast(unsigned, __builtin_elementwise_sub_sat(__builtin_bit_cast(u16x2, a), __builtin_bit_cast(u16x2, b))); }
DI unsigned umax3(unsigned a, unsigned b, unsigned c) { return max(max(a, b), c); }
DI f32x4 mfma(v4u a, v4u b, f32x4 c) { return __builtin_amdgcn_mfma_f32_16x16x32_bf16(__builtin_bit_cast(bf16x8, a), __builtin_bit_cast(bf16x8, b), c, 0, 0, 0); }
template <int CTRL> DI float dppf(float v) { return u2f((unsigned)__builtin_amdgcn_update_dpp(0, (int)f2u(v), CTRL, 0xf, 0xf, true)); }
template <int CTRL> DI unsigned dppu(unsigned v) { return (unsigned)__builtin_amdgcn_update_dpp(0, (int)v, CTRL, 0xf, 0xf, true); }
template <int CTRL, int RM> DI float dppm(float v) { return u2f((unsigned)__builtin_amdgcn_update_dpp(0, (int)f2u(v), CTRL, RM, 0xf, false)); }
DI float quad_sum(float v) { v += dppf<0xB1>(v); v += dppf<0x4E>(v); return v; }
DI float xg_sum(float v) {
  const auto a = __builtin_amdgcn_permlane16_swap(f2u(v), f2u(v), false, false);
  const unsigned a0 = a[0], a1 = a[1];
  v = u2f(a0) + u2f(a1);
  const auto b = __builtin_amdgcn_permlane32_swap(f2u(v), f2u(v), false, false);
  const unsigned b0 = b[0], b1 = b[1];
  return u2f(b0) + u2f(b1);
}
DI float x32_sum(float v) {
  const auto b = __builtin_amdgcn_permlane32_swap(f2u(v), f2u(v), false, false);
  const unsigned b0 = b[0], b1 = b[1];
  return u2f(b0) + u2f(b1);
}
DI float xg_max(float v) {
  const auto a = __builtin_amdgcn_permlane16_swap(f2u(v), f2u(v), false, false);
  const unsigned a0 = a[0], a1 = a[1];
  v = fmaxf(u2f(a0), u2f(a1));
  const auto b = __builtin_amdgcn_permlane32_swap(f2u(v), f2u(v), false, false);
  const unsigned b0 = b[0], b1 = b[1];
  return fmaxf(u2f(b0), u2f(b1));
}
DI unsigned swz(unsigned r) { return (((r ^ ((r >> 1) & 4u)) & 7u) << 1) | ((r >> 3) & 1u); }

typedef __amdgpu_buffer_rsrc_t rsrc_t;
DI v4u ld128(rsrc_t rs, int off) { return __builtin_amdgcn_raw_buffer_load_b128(rs, off, 0, AUX_SC1); }
DI v2u ld64(rsrc_t rs, int off) { return __builtin_amdgcn_raw_buffer_load_b64(rs, off, 0, AUX_SC1); }
DI unsigned ld32(rsrc_t rs, int off) { return __builtin_amdgcn_raw_buffer_load_b32(rs, off, 0, AUX_SC1); }
DI void st128(rsrc_t rs, int off, v4u v) { __builtin_amdgcn_raw_buffer_store_b128(v, rs, off, 0, AUX_SC1); }
DI void st64(rsrc_t rs, int off, v2u v) { __builtin_amdgcn_raw_buffer_store_b64(v, rs, off, 0, AUX_SC1); }
DI void st32(rsrc_t rs, int off, unsigned v) { __builtin_amdgcn_raw_buffer_store_b32(v, rs, off, 0, AUX_SC1); }
#define CBAR() asm volatile("" ::: "memory")
#define DRAIN() asm volatile("s_waitcnt vmcnt(0)" ::: "memory")
#define LBAR() do { asm volatile("s_waitcnt lgkmcnt(0)" ::: "memory"); __builtin_amdgcn_s_barrier(); asm volatile("" ::: "memory"); } while (0)

DI void tr8x8(const v4u (&r)[8], v4u* f) {
#pragma unroll
  for (int q = 0; q < 4; ++q)
#pragma unroll
    for (int d = 0; d < 4; ++d) {
      const unsigned a = r[2 * d][q], b = r[2 * d + 1][q];
      f[2 * q][d] = __builtin_amdgcn_perm(b, a, 0x05040100u);
      f[2 * q + 1][d] = __builtin_amdgcn_perm(b, a, 0x07060302u);
    }
}
struct Ctx {
  rsrc_t rs; LAS unsigned char* L;
  int lane, w, b, G, s, blk;
  unsigned spins;
  unsigned kLO1, kHI1, kONES, kNLO1, kNHI1;
  v4u nonce;
};
DI unsigned sconst(unsigned v) { unsigned r; asm volatile("s_mov_b32 %0, %1" : "=s"(r) : "i"(v)); return r; }
#define RELANE(c) do { int l_ = (c).lane; asm volatile("" : "+v"(l_)); (c).lane = l_; } while (0)
DI bool spin_ok(Ctx& c) { return ++c.spins < SPIN_CAP; }

DI int q_blk(int b, int h, int ch) { return (h >> 2) + 8 * (8 * b + 2 * (h & 3) + ch); }

DI bool nonce_eq(v4u a, v4u n) { return ((a.x ^ n.x) | (a.y ^ n.y) | (a.z ^ n.z) | (a.w ^ n.w)) == 0u; }
DI void wait_ready_a(Ctx& c, v4u nonce) {
  const int q = c.lane & 3;
  const int off = OFF_RDY + q_blk(c.b, 16 * c.G + c.w + 8 * (q >> 1), q & 1) * 64;
  for (;;) {
    CBAR();
    const v4u a = ld128(c.rs, off);
    if (__all(nonce_eq(a, nonce)) || !spin_ok(c)) break;
  }
  CBAR();
}
DI void wait_ready_b(Ctx& c, v4u nonce) {
  const int o0 = OFF_RDY + (c.b * 64 + c.lane) * 64 + 16, o1 = o0 + 128 * 64;
  for (;;) {
    CBAR();
    const v4u a = ld128(c.rs, o0), bq = ld128(c.rs, o1);
    if (__all(nonce_eq(a, nonce) & nonce_eq(bq, nonce)) || !spin_ok(c)) break;
  }
  CBAR();
}

template <int D>
DI void gate(Ctx& c, int off) {
  if (c.w == 0) {
    if (off >= 0) {
      for (;;) {
        CBAR();
        const unsigned v = __builtin_amdgcn_raw_buffer_load_b32(c.rs, off, 0, AUX_SC1);
        if (__builtin_amdgcn_readfirstlane(v) != SENT || !spin_ok(c)) break;
        __builtin_amdgcn_s_sleep(4);
      }
    }
    if (D > 0) __builtin_amdgcn_s_sleep(D);
  }
  __builtin_amdgcn_s_barrier();
}
#define D_F3 40
#define D_Q4 30
#define D_A1 30
#define D_FA1 45

template <bool RES_WS>
DI unsigned gather33(Ctx& c, int base, const u16* xin) {
  const int co = 128 * c.w + 4 * (c.lane & 31);
  unsigned v[16], res;
  if (!RES_WS) res = *(const GAS unsigned*)((const GAS unsigned char*)xin + co);
  const int o = base + (c.lane >> 5) * 1024 + co;
  for (;;) {
    CBAR();
#pragma unroll
    for (int i = 0; i < 16; ++i) v[i] = ld32(c.rs, o + i * 2048);
    if (RES_WS) res = ld32(c.rs, base + 32 * 1024 + co);
    unsigned m = RES_WS ? res : 0u;
#pragma unroll
    for (int i = 0; i < 16; i += 2) m = umax3(m, v[i], v[i + 1]);
    if (__all(m != SENT) || !spin_ok(c)) break;
  }
  float s0 = 0.f, s1 = 0.f, s2 = 0.f, s3 = 0.f;
#pragma unroll
  for (int i = 0; i < 16; i += 2) { s0 = dot2(v[i], LO1, s0); s1 = dot2(v[i], HI1, s1); s2 = dot2(v[i + 1], LO1, s2); s3 = dot2(v[i + 1], HI1, s3); }
  const unsigned o01 = pack2(x32_sum(s0 + s2), x32_sum(s1 + s3));
  return pack2(dot2(o01, LO1, bf_lo(res)), dot2(o01, HI1, bf_hi(res)));
}

DI void layernorm(Ctx& c, unsigned xp, v4u& a0, v4u& a1) {
  float t1 = dot2(xp, ONES, 0.f), t2 = dot2(xp, xp, 0.f);
  t1 += dppf<0x121>(t1); t2 += dppf<0x121>(t2);
  t1 += dppf<0x122>(t1); t2 += dppf<0x122>(t2);
  t1 += dppf<0x124>(t1); t2 += dppf<0x124>(t2);
  t1 += dppf<0x128>(t1); t2 += dppf<0x128>(t2);
  t1 += dppm<0x142, 0xa>(t1); t2 += dppm<0x142, 0xa>(t2);
  if (c.lane == 31) *(LAS f32x2*)(c.L + L_LN + c.w * 8) = f32x2{t1, t2};
  LBAR();
  f32x4 t[4];
#pragma unroll
  for (int i = 0; i < 4; ++i) t[i] = *(const LAS f32x4*)(c.L + L_LN + i * 16);
  const f32x4 tt = (t[0] + t[1]) + (t[2] + t[3]);
  const float mean = (tt.x + tt.z) * (1.0f / 512.0f);
  const float var = fmaxf((tt.y + tt.w) * (1.0f / 512.0f) - mean * mean, 0.f);
  const unsigned mv = pack2(mean, var);
  const float nmb = -bf_lo(mv);
  const float sd = rbf(__builtin_amdgcn_sqrtf(rbf(bf_hi(mv) + 1e-5f)));
  const float ri = __builtin_amdgcn_rcpf(sd);
  const unsigned d01 = pack2(dot2(xp, LO1, nmb), dot2(xp, HI1, nmb));
  const unsigned n01 = pack2(bf_lo(d01) * ri, bf_hi(d01) * ri);
  *(LAS unsigned*)(c.L + L_FR + c.w * 256 + c.lane * 4) = n01;
  a0 = *(const LAS v4u*)(c.L + L_FR + c.w * 256 + (c.lane >> 4) * 32);
  a1 = *(const LAS v4u*)(c.L + L_FR + c.w * 256 + (c.lane >> 4) * 32 + 16);
}

template <bool HILO>
DI void gemv_ksplit(Ctx& c, v4u a0, v4u a1, const v4u* W) {
  f32x4 acc[8];
#pragma unroll
  for (int t = 0; t < 8; ++t) acc[t] = mfma(a0, W[t], f32x4{0.f, 0.f, 0.f, 0.f});
#pragma unroll
  for (int t = 0; t < 8; ++t) acc[t] = mfma(a1, W[8 + t], acc[t]);
  if (c.lane < 16) {
    f32x4 o0, o1;
#pragma unroll
    for (int t = 0; t < 4; ++t) { o0[t] = HILO ? acc[t].x + acc[t].y : acc[t].x; o1[t] = HILO ? acc[4 + t].x + acc[4 + t].y : acc[4 + t].x; }
    *(LAS f32x4*)(c.L + L_RED + c.w * 512 + c.lane * 32) = o0;
    *(LAS f32x4*)(c.L + L_RED + c.w * 512 + c.lane * 32 + 16) = o1;
  }
}
DI float red_sum8(Ctx& c, int idx) {
  const LAS float* R = (const LAS float*)(c.L + L_RED);
  return ((R[idx] + R[128 + idx]) + (R[256 + idx] + R[384 + idx])) + ((R[512 + idx] + R[640 + idx]) + (R[768 + idx] + R[896 + idx]));
}
DI void reduce128(Ctx& c) {
  LBAR();
  if (c.w < 2) { const int d = c.w * 64 + c.lane; *(LAS __bf16*)(c.L + L_VEC + d * 2) = (__bf16)red_sum8(c, d); }
  LBAR();
}

template <int KB>
DI void gemv_nsplit(Ctx& c, const v4u* W, int off) {
  const int n = c.lane & 15, g = c.lane >> 4;
  f32x4 acc[8];
#pragma unroll
  for (int kb = 0; kb < KB; ++kb) {
    const v4u a = *(const LAS v4u*)(c.L + L_VEC + kb * 128 + (n & 1) * 64 + g * 16);
#pragma unroll
    for (int t = 0; t < 8; ++t) acc[t] = mfma(a, W[8 * kb + t], kb == 0 ? f32x4{0.f, 0.f, 0.f, 0.f} : acc[t]);
  }
  float r[8];
#pragma unroll
  for (int t = 0; t < 8; ++t) r[t] = acc[t].x + dppf<0x108>(acc[t].y);
  if (c.lane < 8) st128(c.rs, off + 128 * c.w + 16 * c.lane, v4u{canon(pack2(r[0], r[1])), canon(pack2(r[2], r[3])), canon(pack2(r[4], r[5])), canon(pack2(r[6], r[7]))});
}

DI float score_round(float s) {
  const float SC = 0.08838834764831845f;
  const unsigned bits = f2u(s);
  const unsigned tr = bits & 0xFFFF0000u;
  const unsigned g2 = pack2(u2f(tr) * SC, u2f(tr + 0x10000u) * SC);
  const float ga = bf_lo(g2);
  const float wgt = fminf(fmaxf((float)(bits & 0xFFFFu) * (1.0f / (65536.0f * 0.6f)) + (0.5f - 0.5f / 0.6f), 0.f), 1.f);
  return ga + wgt * (bf_hi(g2) - ga);
}

DI void attn_phase(Ctx& c, int l) {
  RELANE(c);
  const int tid = c.w * 64 + c.lane, n = c.lane & 15, g = c.lane >> 4;
  v4u kf[16];
  {
    const unsigned kb = L_IMG + (c.w * 16 + n) * 1024 + (((unsigned)g ^ swz(n)) << 4);
#pragma unroll
    for (int ks = 0; ks < 16; ++ks) kf[ks] = *(const LAS v4u*)(c.L + ((kb ^ (64u * (ks & 3))) + 256u * (ks >> 2)));
  }
  unsigned ab;
  {
    const unsigned q4 = (c.lane & 15) >> 2, p4 = c.lane & 3;
    const unsigned key = 4u * g + q4;
    const unsigned chunk = 8u * c.w + (p4 >> 1);
    ab = L_IMG + key * 1024 + ((chunk ^ swz(key)) << 4) + 8u * (p4 & 1);
    asm volatile("" : "+v"(ab));
  }
  {
    const int base = OFF_X1 + ((l * 2 + c.b) * 32 + 16 * c.G) * 1024 + tid * 16;
    v4u v0, v1;
    for (;;) {
      CBAR();
      v0 = ld128(c.rs, base);
      v1 = ld128(c.rs, base + 8192);
      const unsigned m = max(umax3(umax3(v0.x, v0.y, v0.z), v0.w, v1.x), umax3(v1.y, v1.z, v1.w));
      if (__all(m != SENT) || !spin_ok(c)) break;
    }
    *(LAS v4u*)(c.L + L_QA + c.w * 1024 + (((unsigned)c.lane ^ swz(c.w)) << 4)) = v0;
    *(LAS v4u*)(c.L + L_QA + (c.w + 8) * 1024 + (((unsigned)c.lane ^ swz(c.w + 8)) << 4)) = v1;
  }
  DRAIN();
  LBAR();
  if (l == 0 && c.w == 0 && c.lane == 0) st128(c.rs, OFF_RDY + c.blk * 64 + 16, c.nonce);
  f32x4 sacc = {0.f, 0.f, 0.f, 0.f}, sacc2 = {0.f, 0.f, 0.f, 0.f};
  {
    const unsigned qb = L_QA + n * 1024 + (((unsigned)g ^ swz(n)) << 4);
    constexpr int QD = 4;
    v4u qf[QD];
#pragma unroll
    for (int ks = 0; ks < QD; ++ks) qf[ks] = *(const LAS v4u*)(c.L + ((qb ^ (64u * (ks & 3))) + 256u * (ks >> 2)));
    __builtin_amdgcn_sched_barrier(0);
#pragma unroll
    for (int ks = 0; ks < 16; ++ks) {
      if (ks & 1) sacc2 = mfma(kf[ks], qf[ks % QD], sacc2); else sacc = mfma(kf[ks], qf[ks % QD], sacc);
      if (ks + QD < 16) qf[ks % QD] = *(const LAS v4u*)(c.L + ((qb ^ (64u * ((ks + QD) & 3))) + 256u * ((ks + QD) >> 2)));
      __builtin_amdgcn_sched_barrier(0);
    }
  }
  v2u vf[4][4][2];
  auto vf_issue = [&](int u) __attribute__((always_inline)) {
#pragma unroll
    for (int t = 0; t < 4; ++t) {
      const unsigned ad = (ab ^ (32u * t)) + 32768u * u;
      const s16x4 lo = __builtin_amdgcn_ds_read_tr16_b64_v4i16((LAS s16x4*)(c.L + ad));
      const s16x4 hi = __builtin_amdgcn_ds_read_tr16_b64_v4i16((LAS s16x4*)(c.L + ad + 16384u));
      vf[u][t][0] = __builtin_bit_cast(v2u, lo); vf[u][t][1] = __builtin_bit_cast(v2u, hi);
    }
  };
  vf_issue(0); vf_issue(1);
  __builtin_amdgcn_sched_barrier(0);
  {
    const float LOG2E = 1.4426950408889634f;
    float sv[4];
#pragma unroll
    for (int q = 0; q < 4; ++q) sv[q] = score_round(sacc[q] + sacc2[q]) * LOG2E;
    const float kw = ceilf(xg_max(fmaxf(fmaxf(sv[0], sv[1]), fmaxf(sv[2], sv[3]))));
    float p[4];
#pragma unroll
    for (int q = 0; q < 4; ++q) p[q] = __builtin_amdgcn_exp2f(sv[q] - kw);
    const float lw = xg_sum((p[0] + p[1]) + (p[2] + p[3]));
    *(LAS v2u*)(c.L + L_PB + (c.w >> 1) * 1024 + c.lane * 16 + (c.w & 1) * 8) = v2u{pack2(p[0], p[1]), pack2(p[2], p[3])};
    if (g == 0) *(LAS v2u*)(c.L + L_SB + n * 64 + c.w * 8) = v2u{(unsigned)(int)kw, f2u(lw)};
  }
  LBAR();
  f32x4 acc[4];
  {
    v4u sb[4], pr[4];
#pragma unroll
    for (int i = 0; i < 4; ++i) sb[i] = *(const LAS v4u*)(c.L + L_SB + n * 64 + i * 16);
#pragma unroll
    for (int u = 0; u < 4; ++u) pr[u] = *(const LAS v4u*)(c.L + L_PB + u * 1024 + c.lane * 16);
    vf_issue(2); vf_issue(3);
    __builtin_amdgcn_sched_barrier(0);
    int K = max(max(max((int)sb[0].x, (int)sb[0].z), max((int)sb[1].x, (int)sb[1].z)), max(max((int)sb[2].x, (int)sb[2].z), max((int)sb[3].x, (int)sb[3].z)));
    if (c.w == 0) {
      float Ls = 0.f;
#pragma unroll
      for (int i = 0; i < 4; ++i) Ls += ldexpf(u2f(sb[i].y), (int)sb[i].x - K) + ldexpf(u2f(sb[i].w), (int)sb[i].z - K);
      if (g == 0) st64(c.rs, OFF_ST + (((l * 2 + c.b) * 32 + 16 * c.G + n) * 64 + c.s) * 8, v2u{canon(f2u((float)K)), canon(f2u(Ls))});
    }
#pragma unroll
    for (int t = 0; t < 4; ++t) acc[t] = f32x4{0.f, 0.f, 0.f, 0.f};
#pragma unroll
    for (int u = 0; u < 4; ++u) {
      const unsigned d0 = __umul24((unsigned)min(K - (int)sb[u].x, 255), 0x00800080u), d1 = __umul24((unsigned)min(K - (int)sb[u].z, 255), 0x00800080u);
      const v4u pb = {subsat(pr[u].x, d0), subsat(pr[u].y, d0), subsat(pr[u].z, d1), subsat(pr[u].w, d1)};
#pragma unroll
      for (int t = 0; t < 4; ++t) {
        const v4u af = {vf[u][t][0].x, vf[u][t][0].y, vf[u][t][1].x, vf[u][t][1].y};
        acc[t] = mfma(pb, af, acc[t]);
      }
    }
  }
  {
    const int base = OFF_X2 + ((l * 2 + c.b) * 32 + 16 * c.G + 4 * g) * 65536 + c.w * 8192 + c.s * 128 + n * 8;
#pragma unroll
    for (int q = 0; q < 4; ++q)
      st64(c.rs, base + q * 65536, v2u{pack2(acc[0][q], acc[1][q]), pack2(acc[2][q], acc[3][q])});
  }
  DRAIN();
  if (c.lane == 0) st32(c.rs, OFF_FW + ((((l * 2 + c.b) * 2 + c.G) * 8 + c.w) * 64 + c.s) * 4, 1u);
}

DI void fill_attn(Ctx& c) {
  const int n = c.lane & 15, g = c.lane >> 4;
#pragma unroll
  for (int l = 0; l < 2; ++l) {
    if (c.w == 0 && g == 0) st64(c.rs, OFF_ST + (((l * 2 + c.b) * 32 + 16 * c.G + n) * 64 + c.s) * 8, v2u{SENT, SENT});
    if (c.lane == 0) st32(c.rs, OFF_FW + ((((l * 2 + c.b) * 2 + c.G) * 8 + c.w) * 64 + c.s) * 4, SENT);
  }
}

DI void ckv_dma(Ctx& c, const Params& p) {
  const GAS unsigned char* src = (const GAS unsigned char*)p.ckv + ((size_t)c.b * 8192 + c.s * 128 + c.w * 16) * 1024;
#pragma unroll
  for (int i = 0; i < 16; ++i)
    __builtin_amdgcn_global_load_lds((const GAS unsigned*)(src + i * 1024 + (((unsigned)c.lane ^ swz(i)) << 4)),
                                     (LAS unsigned*)(c.L + L_IMG + (c.w * 16 + i) * 1024), 16, 0, 0);
}
DI void ckv_issue(Ctx& c, const Params& p, v4u (&t)[16]) {
  const GAS unsigned char* src = (const GAS unsigned char*)p.ckv + ((size_t)c.b * 8192 + c.s * 128 + c.w * 16) * 1024 + c.lane * 16;
#pragma unroll
  for (int i = 0; i < 16; ++i) t[i] = *(const GAS v4u*)(src + i * 1024);
}
DI void ckv_stage(Ctx& c, const v4u (&t)[16]) {
#pragma unroll
  for (int i = 0; i < 16; ++i)
    *(LAS v4u*)(c.L + L_IMG + (c.w * 16 + i) * 1024 + (((unsigned)c.lane ^ swz(i)) << 4)) = t[i];
}
DI void publish_ready_b(Ctx& c, v4u nonce) { LBAR(); }

DI void role_q(Ctx& c, const Params& p, v4u nonce, int h, int ch) {
  asm volatile("; role Q" ::: "memory");
  const int n = c.lane & 15, g = c.lane >> 4;
  const v4u sv = {SENT, SENT, SENT, SENT};
  if (c.w == 7) {
    st128(c.rs, OFF_X1 + (((c.lane >> 5) * 2 + c.b) * 32 + h) * 1024 + ch * 512 + (c.lane & 31) * 16, sv);
    DRAIN();
    if (c.lane == 0) st128(c.rs, OFF_RDY + c.blk * 64, nonce);
  }
  CBAR();
  unsigned xp = *(const GAS unsigned*)((const GAS unsigned char*)p.x + c.b * 1024 + 128 * c.w + 4 * (c.lane & 31));
  v4u rq[2][8], ru[8];
  {
    const GAS unsigned char* wq = (const GAS unsigned char*)p.wq + ((size_t)(64 * c.w + 16 * g) * 4096 + h * 128 + 8 * n) * 2;
#pragma unroll
    for (int ks = 0; ks < 2; ++ks)
#pragma unroll
      for (int j = 0; j < 8; ++j) rq[ks][j] = *(const GAS v4u*)(wq + (size_t)(8 * ks + j) * 8192);
    const GAS unsigned char* wu = (const GAS unsigned char*)p.wuk + ((size_t)(256 * ch + 32 * c.w + g) * 4096 + h * 128 + 8 * n) * 2;
#pragma unroll
    for (int i = 0; i < 8; ++i) ru[i] = *(const GAS v4u*)(wu + (size_t)(4 * i) * 8192);
  }
  v4u ct[16];
  __builtin_amdgcn_sched_barrier(0);
  v4u Wq[16];
  const bool xpub = (h == 0 && ch == 0);
  auto q_phase = [&](int l) __attribute__((always_inline)) {
    v4u a0, a1;
    layernorm(c, xp, a0, a1);
    if (l == 0) { ckv_issue(c, p, ct); __builtin_amdgcn_sched_barrier(0); tr8x8(rq[0], Wq); tr8x8(rq[1], Wq + 8); }
    gemv_ksplit<false>(c, a0, a1, Wq);
    if (l == 0) {
#pragma unroll
      for (int i = 0; i < 8; ++i) { const unsigned row = 4 * i + g; *(LAS v4u*)(c.L + L_IMG + c.w * 8192 + row * 256 + (((unsigned)n ^ swz(row >> 1)) << 4)) = ru[i]; }
#pragma unroll
      for (int t = 0; t < 2; ++t)
#pragma unroll
        for (int ks = 0; ks < 4; ++ks) ru[t * 4 + ks] = *(const LAS v4u*)(c.L + L_IMG + c.w * 8192 + (2 * n + t) * 256 + (((unsigned)(4 * ks + g) ^ swz(n)) << 4));
    }
    reduce128(c);
    f32x4 acc0 = {0.f, 0.f, 0.f, 0.f}, acc1 = {0.f, 0.f, 0.f, 0.f};
#pragma unroll
    for (int ks = 0; ks < 4; ++ks) {
      const v4u a = *(const LAS v4u*)(c.L + L_VEC + ks * 64 + g * 16);
      acc0 = mfma(a, ru[ks], acc0);
      acc1 = mfma(a, ru[4 + ks], acc1);
    }
    if (c.lane < 16) st32(c.rs, OFF_X1 + ((l * 2 + c.b) * 32 + h) * 1024 + (256 * ch + 32 * c.w + 2 * n) * 2, canon(pack2(acc0.x, acc1.x)));
  };
  q_phase(0);
  wait_ready_a(c, nonce);
  fill_attn(c);
  if (xpub && c.w == 1) st128(c.rs, OFF_X3 + (1 * 2 + c.b) * XV_STR + 32 * 1024 + c.lane * 16, sv);
  ckv_stage(c, ct);
  publish_ready_b(c, nonce);
#pragma unroll 1
  for (int l = 0; l < 2; ++l) {
    attn_phase(c, l);
    if (l == 0) {
      wait_ready_b(c, nonce);
      gate<D_Q4>(c, OFF_X3 + (0 * 2 + c.b) * XV_STR + h * 1024);
      xp = gather33<true>(c, OFF_X4 + (0 * 2 + c.b) * XV_STR, nullptr);
      if (xpub && c.lane < 32) st32(c.rs, OFF_X3 + (1 * 2 + c.b) * XV_STR + 32 * 1024 + 128 * c.w + 4 * c.lane, canon(xp));
      q_phase(1);
    }
  }
  gate<D_Q4>(c, OFF_X3 + (1 * 2 + c.b) * XV_STR + h * 1024);
  if (c.w == 0) {
    const int qi = 2 * h + ch;
    const int off = OFF_X4 + (1 * 2 + c.b) * XV_STR + (c.lane < 33 ? c.lane : 32) * 1024 + qi * 16;
    v4u v;
    for (;;) {
      CBAR();
      v = ld128(c.rs, off);
      if (__all(max(umax3(v.x, v.y, v.z), v.w) != SENT) || !spin_ok(c)) break;
    }
    const unsigned wl = c.lane < 32 ? LO1 : 0u, wh = c.lane < 32 ? HI1 : 0u;
    v4u o;
#pragma unroll
    for (int q = 0; q < 4; ++q) {
      float lo = dot2(v[q], wl, 0.f), hi = dot2(v[q], wh, 0.f);
      lo = quad_sum(lo); hi = quad_sum(hi);
      lo += dppf<0x124>(lo); hi += dppf<0x124>(hi);
      lo += dppf<0x128>(lo); hi += dppf<0x128>(hi);
      lo += dppm<0x142, 0xa>(lo); hi += dppm<0x142, 0xa>(hi);
      const unsigned s2 = (unsigned)__builtin_amdgcn_readlane((int)pack2(lo, hi), 31);
      const unsigned r2 = (unsigned)__builtin_amdgcn_readlane((int)v[q], 32);
      o[q] = pack2(bf_lo(r2) + bf_lo(s2), bf_hi(r2) + bf_hi(s2));
    }
    if (c.lane == 0) *(GAS v4u*)((GAS unsigned char*)p.out + c.b * 1024 + qi * 16) = o;
  }
}

DI void role_o(Ctx& c, const Params& p, v4u nonce, int h) {
  asm volatile("; role O" ::: "memory");
  const int n = c.lane & 15, g = c.lane >> 4;
  const v4u sv = {SENT, SENT, SENT, SENT};
  ckv_dma(c, p);
  DRAIN();
  __builtin_amdgcn_sched_barrier(0);
  v4u rv[2][8], ro[2][8];
  {
    const GAS unsigned char* wv = (const GAS unsigned char*)p.wuv + ((size_t)(64 * c.w + 2 * g) * 4096 + h * 128 + 8 * n) * 2;
#pragma unroll
    for (int ks = 0; ks < 2; ++ks)
#pragma unroll
      for (int j = 0; j < 8; ++j) rv[ks][j] = *(const GAS v4u*)(wv + (size_t)(16 * (j & 3) + 8 * ks + (j >> 2)) * 8192);
    const GAS unsigned char* wo = (const GAS unsigned char*)p.wo + ((size_t)(h * 128 + 32 * (n >> 3) + 8 * g) * 512 + 64 * c.w + 8 * (n & 7)) * 2;
#pragma unroll
    for (int kb = 0; kb < 2; ++kb)
#pragma unroll
      for (int j = 0; j < 8; ++j) ro[kb][j] = *(const GAS v4u*)(wo + (size_t)(64 * kb + j) * 1024);
  }
  __builtin_amdgcn_sched_barrier(0);
  fill_attn(c);
  if (c.w < 2) st128(c.rs, OFF_X3 + (c.w * 2 + c.b) * XV_STR + h * 1024 + c.lane * 16, sv);
  v4u Wv[16], Wo[16];
  tr8x8(rv[0], Wv); tr8x8(rv[1], Wv + 8);
  tr8x8(ro[0], Wo); tr8x8(ro[1], Wo + 8);
  publish_ready_b(c, nonce);
  wait_ready_a(c, nonce);
#pragma unroll 1
  for (int l = 0; l < 2; ++l) {
    attn_phase(c, l);
    if (l == 0) wait_ready_b(c, nonce);
    float inv;
    v2u v[16];
    {
      const int bs = OFF_ST + ((l * 2 + c.b) * 32 + h) * 512 + c.lane * 8;
      v2u st;
      for (;;) {
        CBAR();
        st = ld64(c.rs, bs);
        if (__all(max(st.x, st.y) != SENT) || !spin_ok(c)) break;
      }
      const float kf = u2f(st.x);
      float K = kf;
      K = fmaxf(K, dppf<0x128>(K)); K = fmaxf(K, dppf<0x124>(K)); K = fmaxf(K, dppf<0x122>(K)); K = fmaxf(K, dppf<0x121>(K));
      K = xg_max(K);
      const int d = (int)(K - kf);
      const unsigned eb = d < 127 ? (unsigned)(127 - d) << 7 : 0u;
      float Ls = u2f(st.y) * u2f(eb << 16);
      Ls += dppf<0x128>(Ls); Ls += dppf<0x124>(Ls); Ls += dppf<0x122>(Ls); Ls += dppf<0x121>(Ls);
      Ls = xg_sum(Ls);
      inv = 1.0f / Ls;
      *(LAS unsigned*)(c.L + L_EW + c.w * 256 + c.lane * 4) = eb;
    }
    unsigned el[16];
#pragma unroll
    for (int i = 0; i < 4; ++i) {
      const v4u e4 = *(const LAS v4u*)(c.L + L_EW + c.w * 256 + g * 64 + i * 16);
      el[4 * i] = e4.x; el[4 * i + 1] = e4.y; el[4 * i + 2] = e4.z; el[4 * i + 3] = e4.w;
    }
    {
      const int bf = OFF_FW + ((((l * 2 + c.b) * 2 + (h >> 4)) * 8 + c.w) * 64 + c.lane) * 4;
      for (;;) {
        CBAR();
        const unsigned fw = ld32(c.rs, bf);
        if (__all(fw != SENT) || !spin_ok(c)) break;
      }
      CBAR();
      const int bx = OFF_X2 + ((l * 2 + c.b) * 32 + h) * 65536 + c.w * 8192 + g * 2048 + n * 8;
#pragma unroll
      for (int i = 0; i < 16; ++i) v[i] = ld64(c.rs, bx + i * 128);
    }
    float s0 = 0.f, s1 = 0.f, s2 = 0.f, s3 = 0.f;
#pragma unroll
    for (int i = 0; i < 16; ++i) {
      const unsigned eh = el[i] << 16;
      s0 = dot2(v[i].x, el[i], s0); s1 = dot2(v[i].x, eh, s1); s2 = dot2(v[i].y, el[i], s2); s3 = dot2(v[i].y, eh, s3);
    }
    s0 = xg_sum(s0) * inv; s1 = xg_sum(s1) * inv; s2 = xg_sum(s2) * inv; s3 = xg_sum(s3) * inv;
    const unsigned h01 = pack2(s0, s1), h23 = pack2(s2, s3);
    const unsigned l01 = pack2(dot2(h01, NLO1, s0), dot2(h01, NHI1, s1)), l23 = pack2(dot2(h23, NLO1, s2), dot2(h23, NHI1, s3));
    *(LAS v2u*)(c.L + L_FR + c.w * 256 + (g & 1) * 128 + n * 8) = (g & 1) ? v2u{l01, l23} : v2u{h01, h23};
    const v4u a0 = *(const LAS v4u*)(c.L + L_FR + c.w * 256 + (n & 1) * 128 + g * 16);
    const v4u a1 = *(const LAS v4u*)(c.L + L_FR + c.w * 256 + (n & 1) * 128 + 64 + g * 16);
    gemv_ksplit<true>(c, a0, a1, Wv);
    reduce128(c);
    gemv_nsplit<2>(c, Wo, OFF_X3 + (l * 2 + c.b) * XV_STR + h * 1024);
    if (l == 0) gate<D_A1>(c, OFF_X4 + (0 * 2 + c.b) * XV_STR + h * 1024);
  }
}

DI void role_f(Ctx& c, const Params& p, v4u nonce, int j) {
  asm volatile("; role F" ::: "memory");
  const int n = c.lane & 15, g = c.lane >> 4;
  const v4u sv = {SENT, SENT, SENT, SENT};
  ckv_dma(c, p);
  DRAIN();
  __builtin_amdgcn_sched_barrier(0);
  v4u r13[2][8], r2[8];
  {
    const int nn = n < 14 ? n : 13;
    const u16* wsel = nn < 7 ? p.w1 : p.w3;
    const int col = 56 * j + 8 * (nn < 7 ? nn : nn - 7);
    const GAS unsigned char* wa = (const GAS unsigned char*)wsel + ((size_t)(64 * c.w + 16 * g) * 1792 + col) * 2;
#pragma unroll
    for (int ks = 0; ks < 2; ++ks)
#pragma unroll
      for (int jj = 0; jj < 8; ++jj) r13[ks][jj] = *(const GAS v4u*)(wa + (size_t)(8 * ks + jj) * 3584);
#pragma unroll
    for (int jj = 0; jj < 8; ++jj) {
      int f = 32 * (n >> 3) + 8 * g + jj; f = f < 56 ? f : 55;
      r2[jj] = *(const GAS v4u*)((const GAS unsigned char*)p.w2 + ((size_t)(56 * j + f) * 512 + 64 * c.w + 8 * (n & 7)) * 2);
    }
  }
  __builtin_amdgcn_sched_barrier(0);
  fill_attn(c);
  if (c.w < 2) st128(c.rs, OFF_X4 + (c.w * 2 + c.b) * XV_STR + j * 1024 + c.lane * 16, sv);
  if (j == 0 && (c.w == 2 || c.w == 3)) st128(c.rs, OFF_X4 + ((c.w - 2) * 2 + c.b) * XV_STR + 32 * 1024 + c.lane * 16, sv);
  v4u W13[16], W2[8];
  tr8x8(r13[0], W13); tr8x8(r13[1], W13 + 8);
  tr8x8(r2, W2);
  publish_ready_b(c, nonce);
  wait_ready_a(c, nonce);
#pragma unroll 1
  for (int l = 0; l < 2; ++l) {
    attn_phase(c, l);
    unsigned xp;
    if (l == 0) wait_ready_b(c, nonce);
    gate<D_F3>(c, -1);
    if (l == 0) { xp = gather33<false>(c, OFF_X3 + (0 * 2 + c.b) * XV_STR, p.x + c.b * 512); }
    else xp = gather33<true>(c, OFF_X3 + (1 * 2 + c.b) * XV_STR, nullptr);
    if (j == 0 && c.lane < 32) st32(c.rs, OFF_X4 + (l * 2 + c.b) * XV_STR + 32 * 1024 + 128 * c.w + 4 * c.lane, canon(xp));
    v4u a0, a1;
    layernorm(c, xp, a0, a1);
    gemv_ksplit<false>(c, a0, a1, W13);
    LBAR();
    if (c.w == 0) {
      const int li = c.lane < 56 ? c.lane : 55;
      const unsigned gu = pack2(red_sum8(c, li), red_sum8(c, 56 + li));
      const float gt = bf_lo(gu);
      const float sg = rbf(gt * __builtin_amdgcn_rcpf(1.0f + __expf(-gt)));
      const float hv = c.lane < 56 ? sg * bf_hi(gu) : 0.f;
      *(LAS __bf16*)(c.L + L_VEC + c.lane * 2) = (__bf16)hv;
    }
    LBAR();
    gemv_nsplit<1>(c, W2, OFF_X4 + (l * 2 + c.b) * XV_STR + j * 1024);
    if (l == 0) gate<D_FA1>(c, -1);
  }
}

__global__ void __launch_bounds__(NT, 2) mega(Params p) {
  extern __shared__ __attribute__((aligned(16))) unsigned char smem[];
  Ctx c;
  c.rs = __builtin_amdgcn_make_buffer_rsrc((void*)p.ws, 0, WS_BYTES, 0x00020000);
  c.L = (LAS unsigned char*)smem;
  c.lane = threadIdx.x & 63;
  c.w = __builtin_amdgcn_readfirstlane(threadIdx.x >> 6);
  c.blk = blockIdx.x;
  const int x = c.blk & 7, slot = c.blk >> 3;
  c.b = (slot >> 3) & 1; c.G = slot >> 4; c.s = 8 * x + (slot & 7);
  const int r = 8 * c.G + (slot & 7);
  c.spins = 0;
  c.kLO1 = sconst(0x00003F80u); c.kHI1 = sconst(0x3F800000u); c.kONES = sconst(0x3F803F80u); c.kNLO1 = sconst(0x0000BF80u); c.kNHI1 = sconst(0xBF800000u);
  const unsigned long long id = llvm_dispatch_id();
  const unsigned long long dp = (unsigned long long)__builtin_amdgcn_dispatch_ptr();
  const v4u nonce = {(unsigned)id ^ 0x5bd1e995u, (unsigned)(id >> 32) ^ 0x9e3779b9u, (unsigned)dp ^ 0x85ebca6bu, (unsigned)(dp >> 32) ^ 0xc2b2ae35u};
  c.nonce = nonce;
  if (r < 8) role_q(c, p, nonce, 4 * x + (r >> 1), r & 1);
  else if (r < 12) role_o(c, p, nonce, 4 * x + (r - 8));
  else role_f(c, p, nonce, 4 * x + (r - 12));
  if (c.w == 0 && c.lane < 2) st128(c.rs, OFF_RDY + c.blk * 64 + c.lane * 16, v4u{~nonce.x, ~nonce.y, ~nonce.z, ~nonce.w});
}
}

extern "C" void kernel_launch(void* const* d_in, const int* in_sizes, int n_in,
                              void* d_out, int out_size, void* d_ws, size_t ws_size,
                              hipStream_t stream) {
  mk::Params p;
  p.x = (const u16*)d_in[0]; p.ckv = (const u16*)d_in[1]; p.wq = (const u16*)d_in[2]; p.wuk = (const u16*)d_in[3];
  p.wuv = (const u16*)d_in[4]; p.wo = (const u16*)d_in[5]; p.w1 = (const u16*)d_in[7]; p.w3 = (const u16*)d_in[8];
  p.w2 = (const u16*)d_in[9]; p.out = (u16*)d_out; p.ws = (unsigned char*)d_ws;
  (void)hipFuncSetAttribute((const void*)mk::mega, hipFuncAttributeMaxDynamicSharedMemorySize, mk::L_BYTES);
  hipLaunchKernelGGL(mk::mega, dim3(256), dim3(mk::NT), mk::L_BYTES, stream, p);
}
```

```cpp
#include <hip/hip_runtime.h>
#include <hip/hip_bf16.h>
#include <stdint.h>

#define LAS __attribute__((address_space(3)))
#define GAS __attribute__((address_space(1)))
typedef unsigned short u16;
typedef unsigned v4u __attribute__((ext_vector_type(4)));
typedef unsigned v2u __attribute__((ext_vector_type(2)));
typedef float f32x4 __attribute__((ext_vector_type(4)));
typedef float f32x2 __attribute__((ext_vector_type(2)));
typedef short bf16x8 __attribute__((ext_vector_type(8)));
typedef short s16x4 __attribute__((ext_vector_type(4)));
typedef __bf16 bf16x2_t __attribute__((ext_vector_type(2)));
typedef unsigned short u16x2 __attribute__((ext_vector_type(2)));
#define DI __device__ __forceinline__

extern "C" __device__ unsigned long long llvm_dispatch_id() __asm("llvm.amdgcn.dispatch.id");

namespace mk {
constexpr int NT = 512;
constexpr unsigned SENT = 0xFFFFFFFFu;
constexpr unsigned SPIN_CAP = 1u << 17;
constexpr int AUX_SC1 = 16;
#define LO1 (c.kLO1)
#define HI1 (c.kHI1)
#define ONES (c.kONES)
#define NLO1 (c.kNLO1)
#define NHI1 (c.kNHI1)

constexpr int OFF_RDY = 0;
constexpr int OFF_X1 = 16384;
constexpr int OFF_ST = OFF_X1 + 2 * 2 * 32 * 1024;
constexpr int OFF_X3 = OFF_ST + 2 * 2 * 32 * 512;
constexpr int XV_STR = 34 * 1024;
constexpr int OFF_X4 = OFF_X3 + 4 * XV_STR;
constexpr int OFF_X2 = OFF_X4 + 4 * XV_STR;
constexpr int OFF_FW = OFF_X2 + 2 * 2 * 32 * 65536;
constexpr int WS_BYTES = OFF_FW + 16384;

constexpr int L_IMG = 0;
constexpr int L_QA = 131072;
constexpr int L_PB = L_QA + 16384;
constexpr int L_SB = L_PB + 4096;
constexpr int L_LN = L_SB + 1024;
constexpr int L_VEC = L_LN + 64;
constexpr int L_EW = L_VEC + 256;
constexpr int L_FR = L_EW + 2048;
constexpr int L_BYTES = L_FR + 2048;
constexpr int L_RED = L_QA;

struct Params {
  const u16* x; const u16* ckv; const u16* wq; const u16* wuk; const u16* wuv; const u16* wo;
  const u16* w1; const u16* w3; const u16* w2; u16* out; unsigned char* ws;
};

DI float u2f(unsigned u) { return __builtin_bit_cast(float, u); }
DI unsigned f2u(float f) { return __builtin_bit_cast(unsigned, f); }
DI float bf_lo(unsigned u) { return u2f(u << 16); }
DI float bf_hi(unsigned u) { return u2f(u & 0xFFFF0000u); }
DI float rbf(float f) { return (float)(__bf16)f; }
DI unsigned pack2(float lo, float hi) { bf16x2_t v = {(__bf16)lo, (__bf16)hi}; return __builtin_bit_cast(unsigned, v); }
DI unsigned canon(unsigned u) { return u < 0xFFFFFFFEu ? u : 0xFFFFFFFEu; }
DI float dot2(unsigned a, unsigned b, float c) { return __builtin_amdgcn_fdot2_f32_bf16(__builtin_bit_cast(bf16x2_t, a), __builtin_bit_cast(bf16x2_t, b), c, false); }
DI unsigned subsat(unsigned a, unsigned b) { return __builtin_bit_cast(unsigned, __builtin_elementwise_sub_sat(__builtin_bit_cast(u16x2, a), __builtin_bit_cast(u16x2, b))); }
DI unsigned umax3(unsigned a, unsigned b, unsigned c) { return max(max(a, b), c); }
DI f32x4 mfma(v4u a, v4u b, f32x4 c) { return __builtin_amdgcn_mfma_f32_16x16x32_bf16(__builtin_bit_cast(bf16x8, a), __builtin_bit_cast(bf16x8, b), c, 0, 0, 0); }
template <int CTRL> DI float dppf(float v) { return u2f((unsigned)__builtin_amdgcn_update_dpp(0, (int)f2u(v), CTRL, 0xf, 0xf, true)); }
template <int CTRL> DI unsigned dppu(unsigned v) { return (unsigned)__builtin_amdgcn_update_dpp(0, (int)v, CTRL, 0xf, 0xf, true); }
template <int CTRL, int RM> DI float dppm(float v) { return u2f((unsigned)__builtin_amdgcn_update_dpp(0, (int)f2u(v), CTRL, RM, 0xf, false)); }
DI float quad_sum(float v) { v += dppf<0xB1>(v); v += dppf<0x4E>(v); return v; }
DI float xg_sum(float v) {
  const auto a = __builtin_amdgcn_permlane16_swap(f2u(v), f2u(v), false, false);
  const unsigned a0 = a[0], a1 = a[1];
  v = u2f(a0) + u2f(a1);
  const auto b = __builtin_amdgcn_permlane32_swap(f2u(v), f2u(v), false, false);
  const unsigned b0 = b[0], b1 = b[1];
  return u2f(b0) + u2f(b1);
}
DI float x32_sum(float v) {
  const auto b = __builtin_amdgcn_permlane32_swap(f2u(v), f2u(v), false, false);
  const unsigned b0 = b[0], b1 = b[1];
  return u2f(b0) + u2f(b1);
}
DI float xg_max(float v) {
  const auto a = __builtin_amdgcn_permlane16_swap(f2u(v), f2u(v), false, false);
  const unsigned a0 = a[0], a1 = a[1];
  v = fmaxf(u2f(a0), u2f(a1));
  const auto b = __builtin_amdgcn_permlane32_swap(f2u(v), f2u(v), false, false);
  const unsigned b0 = b[0], b1 = b[1];
  return fmaxf(u2f(b0), u2f(b1));
}
DI unsigned swz(unsigned r) { return (((r ^ ((r >> 1) & 4u)) & 7u) << 1) | ((r >> 3) & 1u); }

typedef __amdgpu_buffer_rsrc_t rsrc_t;
DI v4u ld128(rsrc_t rs, int off) { return __builtin_amdgcn_raw_buffer_load_b128(rs, off, 0, AUX_SC1); }
DI v2u ld64(rsrc_t rs, int off) { return __builtin_amdgcn_raw_buffer_load_b64(rs, off, 0, AUX_SC1); }
DI unsigned ld32(rsrc_t rs, int off) { return __builtin_amdgcn_raw_buffer_load_b32(rs, off, 0, AUX_SC1); }
DI void st128(rsrc_t rs, int off, v4u v) { __builtin_amdgcn_raw_buffer_store_b128(v, rs, off, 0, AUX_SC1); }
DI void st64(rsrc_t rs, int off, v2u v) { __builtin_amdgcn_raw_buffer_store_b64(v, rs, off, 0, AUX_SC1); }
DI void st32(rsrc_t rs, int off, unsigned v) { __builtin_amdgcn_raw_buffer_store_b32(v, rs, off, 0, AUX_SC1); }
#define CBAR() asm volatile("" ::: "memory")
#define DRAIN() asm volatile("s_waitcnt vmcnt(0)" ::: "memory")
#define LBAR() do { asm volatile("s_waitcnt lgkmcnt(0)" ::: "memory"); __builtin_amdgcn_s_barrier(); asm volatile("" ::: "memory"); } while (0)

DI void tr8x8(const v4u (&r)[8], v4u* f) {
#pragma unroll
  for (int q = 0; q < 4; ++q)
#pragma unroll
    for (int d = 0; d < 4; ++d) {
      const unsigned a = r[2 * d][q], b = r[2 * d + 1][q];
      f[2 * q][d] = __builtin_amdgcn_perm(b, a, 0x05040100u);
      f[2 * q + 1][d] = __builtin_amdgcn_perm(b, a, 0x07060302u);
    }
}
struct Ctx {
  rsrc_t rs; LAS unsigned char* L;
  int lane, w, b, G, s, blk;
  unsigned spins;
  unsigned kLO1, kHI1, kONES, kNLO1, kNHI1;
  v4u nonce;
};
DI unsigned sconst(unsigned v) { unsigned r; asm volatile("s_mov_b32 %0, %1" : "=s"(r) : "i"(v)); return r; }
#define RELANE(c) do { int l_ = (c).lane; asm volatile("" : "+v"(l_)); (c).lane = l_; } while (0)
DI bool spin_ok(Ctx& c) { return ++c.spins < SPIN_CAP; }

DI int q_blk(int b, int h, int ch) { return (h >> 2) + 8 * (8 * b + 2 * (h & 3) + ch); }

DI bool nonce_eq(v4u a, v4u n) { return ((a.x ^ n.x) | (a.y ^ n.y) | (a.z ^ n.z) | (a.w ^ n.w)) == 0u; }
DI void wait_ready_a(Ctx& c, v4u nonce) {
  const int q = c.lane & 3;
  const int off = OFF_RDY + q_blk(c.b, 16 * c.G + c.w + 8 * (q >> 1), q & 1) * 64;
  for (;;) {
    CBAR();
    const v4u a = ld128(c.rs, off);
    if (__all(nonce_eq(a, nonce)) || !spin_ok(c)) break;
  }
  CBAR();
}
DI void wait_ready_b(Ctx& c, v4u nonce) {
  const int o0 = OFF_RDY + (c.b * 64 + c.lane) * 64 + 16, o1 = o0 + 128 * 64;
  for (;;) {
    CBAR();
    const v4u a = ld128(c.rs, o0), bq = ld128(c.rs, o1);
    if (__all(nonce_eq(a, nonce) & nonce_eq(bq, nonce)) || !spin_ok(c)) break;
  }
  CBAR();
}

template <int D>
DI void gate(Ctx& c, int off) {
  if (c.w == 0) {
    if (off >= 0) {
      for (;;) {
        CBAR();
        const unsigned v = __builtin_amdgcn_raw_buffer_load_b32(c.rs, off, 0, AUX_SC1);
        if (__builtin_amdgcn_readfirstlane(v) != SENT || !spin_ok(c)) break;
        __builtin_amdgcn_s_sleep(4);
      }
    }
    if (D > 0) __builtin_amdgcn_s_sleep(D);
  }
  __builtin_amdgcn_s_barrier();
}
#define D_F3 40
#define D_Q4 30
#define D_A1 30
#define D_FA1 45

template <bool RES_WS>
DI unsigned gather33(Ctx& c, int base, const u16* xin) {
  const int co = 128 * c.w + 4 * (c.lane & 31);
  unsigned v[16], res;
  if (!RES_WS) res = *(const GAS unsigned*)((const GAS unsigned char*)xin + co);
  const int o = base + (c.lane >> 5) * 1024 + co;
  for (;;) {
    CBAR();
#pragma unroll
    for (int i = 0; i < 16; ++i) v[i] = ld32(c.rs, o + i * 2048);
    if (RES_WS) res = ld32(c.rs, base + 32 * 1024 + co);
    unsigned m = RES_WS ? res : 0u;
#pragma unroll
    for (int i = 0; i < 16; i += 2) m = umax3(m, v[i], v[i + 1]);
    if (__all(m != SENT) || !spin_ok(c)) break;
  }
  float s0 = 0.f, s1 = 0.f, s2 = 0.f, s3 = 0.f;
#pragma unroll
  for (int i = 0; i < 16; i += 2) { s0 = dot2(v[i], LO1, s0); s1 = dot2(v[i], HI1, s1); s2 = dot2(v[i + 1], LO1, s2); s3 = dot2(v[i + 1], HI1, s3); }
  const unsigned o01 = pack2(x32_sum(s0 + s2), x32_sum(s1 + s3));
  return pack2(dot2(o01, LO1, bf_lo(res)), dot2(o01, HI1, bf_hi(res)));
}

DI void layernorm(Ctx& c, unsigned xp, v4u& a0, v4u& a1) {
  float t1 = dot2(xp, ONES, 0.f), t2 = dot2(xp, xp, 0.f);
  t1 += dppf<0x121>(t1); t2 += dppf<0x121>(t2);
  t1 += dppf<0x122>(t1); t2 += dppf<0x122>(t2);
  t1 += dppf<0x124>(t1); t2 += dppf<0x124>(t2);
  t1 += dppf<0x128>(t1); t2 += dppf<0x128>(t2);
  t1 += dppm<0x142, 0xa>(t1); t2 += dppm<0x142, 0xa>(t2);
  if (c.lane == 31) *(LAS f32x2*)(c.L + L_LN + c.w * 8) = f32x2{t1, t2};
  LBAR();
  f32x4 t[4];
#pragma unroll
  for (int i = 0; i < 4; ++i) t[i] = *(const LAS f32x4*)(c.L + L_LN + i * 16);
  const f32x4 tt = (t[0] + t[1]) + (t[2] + t[3]);
  const float mean = (tt.x + tt.z) * (1.0f / 512.0f);
  const float var = fmaxf((tt.y + tt.w) * (1.0f / 512.0f) - mean * mean, 0.f);
  const unsigned mv = pack2(mean, var);
  const float nmb = -bf_lo(mv);
  const float sd = rbf(__builtin_amdgcn_sqrtf(rbf(bf_hi(mv) + 1e-5f)));
  const float ri = __builtin_amdgcn_rcpf(sd);
  const unsigned d01 = pack2(dot2(xp, LO1, nmb), dot2(xp, HI1, nmb));
  const unsigned n01 = pack2(bf_lo(d01) * ri, bf_hi(d01) * ri);
  *(LAS unsigned*)(c.L + L_FR + c.w * 256 + c.lane * 4) = n01;
  a0 = *(const LAS v4u*)(c.L + L_FR + c.w * 256 + (c.lane >> 4) * 32);
  a1 = *(const LAS v4u*)(c.L + L_FR + c.w * 256 + (c.lane >> 4) * 32 + 16);
}

template <bool HILO>
DI void gemv_ksplit(Ctx& c, v4u a0, v4u a1, const v4u* W) {
  f32x4 acc[8];
#pragma unroll
  for (int t = 0; t < 8; ++t) acc[t] = mfma(a0, W[t], f32x4{0.f, 0.f, 0.f, 0.f});
#pragma unroll
  for (int t = 0; t < 8; ++t) acc[t] = mfma(a1, W[8 + t], acc[t]);
  if (c.lane < 16) {
    f32x4 o0, o1;
#pragma unroll
    for (int t = 0; t < 4; ++t) { o0[t] = HILO ? acc[t].x + acc[t].y : acc[t].x; o1[t] = HILO ? acc[4 + t].x + acc[4 + t].y : acc[4 + t].x; }
    *(LAS f32x4*)(c.L + L_RED + c.w * 512 + c.lane * 32) = o0;
    *(LAS f32x4*)(c.L + L_RED + c.w * 512 + c.lane * 32 + 16) = o1;
  }
}
DI float red_sum8(Ctx& c, int idx) {
  const LAS float* R = (const LAS float*)(c.L + L_RED);
  return ((R[idx] + R[128 + idx]) + (R[256 + idx] + R[384 + idx])) + ((R[512 + idx] + R[640 + idx]) + (R[768 + idx] + R[896 + idx]));
}
DI void reduce128(Ctx& c) {
  LBAR();
  if (c.w < 2) { const int d = c.w * 64 + c.lane; *(LAS __bf16*)(c.L + L_VEC + d * 2) = (__bf16)red_sum8(c, d); }
  LBAR();
}

template <int KB>
DI void gemv_nsplit(Ctx& c, const v4u* W, int off) {
  const int n = c.lane & 15, g = c.lane >> 4;
  f32x4 acc[8];
#pragma unroll
  for (int kb = 0; kb < KB; ++kb) {
    const v4u a = *(const LAS v4u*)(c.L + L_VEC + kb * 128 + (n & 1) * 64 + g * 16);
#pragma unroll
    for (int t = 0; t < 8; ++t) acc[t] = mfma(a, W[8 * kb + t], kb == 0 ? f32x4{0.f, 0.f, 0.f, 0.f} : acc[t]);
  }
  float r[8];
#pragma unroll
  for (int t = 0; t < 8; ++t) r[t] = acc[t].x + dppf<0x108>(acc[t].y);
  if (c.lane < 8) st128(c.rs, off + 128 * c.w + 16 * c.lane, v4u{canon(pack2(r[0], r[1])), canon(pack2(r[2], r[3])), canon(pack2(r[4], r[5])), canon(pack2(r[6], r[7]))});
}

DI float score_round(float s) {
  const float SC = 0.08838834764831845f;
  const unsigned bits = f2u(s);
  const unsigned tr = bits & 0xFFFF0000u;
  const unsigned g2 = pack2(u2f(tr) * SC, u2f(tr + 0x10000u) * SC);
  const float ga = bf_lo(g2);
  const float wgt = fminf(fmaxf((float)(bits & 0xFFFFu) * (1.0f / (65536.0f * 0.6f)) + (0.5f - 0.5f / 0.6f), 0.f), 1.f);
  return ga + wgt * (bf_hi(g2) - ga);
}

DI void attn_phase(Ctx& c, int l) {
  RELANE(c);
  const int tid = c.w * 64 + c.lane, n = c.lane & 15, g = c.lane >> 4;
  v4u kf[16];
  {
    const unsigned kb = L_IMG + (c.w * 16 + n) * 1024 + (((unsigned)g ^ swz(n)) << 4);
#pragma unroll
    for (int ks = 0; ks < 16; ++ks) kf[ks] = *(const LAS v4u*)(c.L + ((kb ^ (64u * (ks & 3))) + 256u * (ks >> 2)));
  }
  unsigned ab;
  {
    const unsigned q4 = (c.lane & 15) >> 2, p4 = c.lane & 3;
    const unsigned key = 4u * g + q4;
    const unsigned chunk = 8u * c.w + (p4 >> 1);
    ab = L_IMG + key * 1024 + ((chunk ^ swz(key)) << 4) + 8u * (p4 & 1);
    asm volatile("" : "+v"(ab));
  }
  {
    const int base = OFF_X1 + ((l * 2 + c.b) * 32 + 16 * c.G) * 1024 + tid * 16;
    v4u v0, v1;
    for (;;) {
      CBAR();
      v0 = ld128(c.rs, base);
      v1 = ld128(c.rs, base + 8192);
      const unsigned m = max(umax3(umax3(v0.x, v0.y, v0.z), v0.w, v1.x), umax3(v1.y, v1.z, v1.w));
      if (__all(m != SENT) || !spin_ok(c)) break;
    }
    *(LAS v4u*)(c.L + L_QA + c.w * 1024 + (((unsigned)c.lane ^ swz(c.w)) << 4)) = v0;
    *(LAS v4u*)(c.L + L_QA + (c.w + 8) * 1024 + (((unsigned)c.lane ^ swz(c.w + 8)) << 4)) = v1;
  }
  DRAIN();
  LBAR();
  if (l == 0 && c.w == 0 && c.lane == 0) st128(c.rs, OFF_RDY + c.blk * 64 + 16, c.nonce);
  f32x4 sacc = {0.f, 0.f, 0.f, 0.f}, sacc2 = {0.f, 0.f, 0.f, 0.f};
  {
    const unsigned qb = L_QA + n * 1024 + (((unsigned)g ^ swz(n)) << 4);
    constexpr int QD = 4;
    v4u qf[QD];
#pragma unroll
    for (int ks = 0; ks < QD; ++ks) qf[ks] = *(const LAS v4u*)(c.L + ((qb ^ (64u * (ks & 3))) + 256u * (ks >> 2)));
    __builtin_amdgcn_sched_barrier(0);
#pragma unroll
    for (int ks = 0; ks < 16; ++ks) {
      if (ks & 1) sacc2 = mfma(kf[ks], qf[ks % QD], sacc2); else sacc = mfma(kf[ks], qf[ks % QD], sacc);
      if (ks + QD < 16) qf[ks % QD] = *(const LAS v4u*)(c.L + ((qb ^ (64u * ((ks + QD) & 3))) + 256u * ((ks + QD) >> 2)));
      __builtin_amdgcn_sched_barrier(0);
    }
  }
  v2u vf[4][4][2];
  auto vf_issue = [&](int u) __attribute__((always_inline)) {
#pragma unroll
    for (int t = 0; t < 4; ++t) {
      const unsigned ad = (ab ^ (32u * t)) + 32768u * u;
      const s16x4 lo = __builtin_amdgcn_ds_read_tr16_b64_v4i16((LAS s16x4*)(c.L + ad));
      const s16x4 hi = __builtin_amdgcn_ds_read_tr16_b64_v4i16((LAS s16x4*)(c.L + ad + 16384u));
      vf[u][t][0] = __builtin_bit_cast(v2u, lo); vf[u][t][1] = __builtin_bit_cast(v2u, hi);
    }
  };
  vf_issue(0); vf_issue(1);
  __builtin_amdgcn_sched_barrier(0);
  {
    const float LOG2E = 1.4426950408889634f;
    float sv[4];
#pragma unroll
    for (int q = 0; q < 4; ++q) sv[q] = score_round(sacc[q] + sacc2[q]) * LOG2E;
    const float kw = ceilf(xg_max(fmaxf(fmaxf(sv[0], sv[1]), fmaxf(sv[2], sv[3]))));
    float p[4];
#pragma unroll
    for (int q = 0; q < 4; ++q) p[q] = __builtin_amdgcn_exp2f(sv[q] - kw);
    const float lw = xg_sum((p[0] + p[1]) + (p[2] + p[3]));
    *(LAS v2u*)(c.L + L_PB + (c.w >> 1) * 1024 + c.lane * 16 + (c.w & 1) * 8) = v2u{pack2(p[0], p[1]), pack2(p[2], p[3])};
    if (g == 0) *(LAS v2u*)(c.L + L_SB + n * 64 + c.w * 8) = v2u{(unsigned)(int)kw, f2u(lw)};
  }
  LBAR();
  f32x4 acc[4];
  {
    v4u sb[4], pr[4];
#pragma unroll
    for (int i = 0; i < 4; ++i) sb[i] = *(const LAS v4u*)(c.L + L_SB + n * 64 + i * 16);
#pragma unroll
    for (int u = 0; u < 4; ++u) pr[u] = *(const LAS v4u*)(c.L + L_PB + u * 1024 + c.lane * 16);
    vf_issue(2); vf_issue(3);
    __builtin_amdgcn_sched_barrier(0);
    int K = max(max(max((int)sb[0].x, (int)sb[0].z), max((int)sb[1].x, (int)sb[1].z)), max(max((int)sb[2].x, (int)sb[2].z), max((int)sb[3].x, (int)sb[3].z)));
    if (c.w == 0) {
      float Ls = 0.f;
#pragma unroll
      for (int i = 0; i < 4; ++i) Ls += ldexpf(u2f(sb[i].y), (int)sb[i].x - K) + ldexpf(u2f(sb[i].w), (int)sb[i].z - K);
      if (g == 0) st64(c.rs, OFF_ST + (((l * 2 + c.b) * 32 + 16 * c.G + n) * 64 + c.s) * 8, v2u{canon(f2u((float)K)), canon(f2u(Ls))});
    }
#pragma unroll
    for (int t = 0; t < 4; ++t) acc[t] = f32x4{0.f, 0.f, 0.f, 0.f};
#pragma unroll
    for (int u = 0; u < 4; ++u) {
      const unsigned d0 = __umul24((unsigned)min(K - (int)sb[u].x, 255), 0x00800080u), d1 = __umul24((unsigned)min(K - (int)sb[u].z, 255), 0x00800080u);
      const v4u pb = {subsat(pr[u].x, d0), subsat(pr[u].y, d0), subsat(pr[u].z, d1), subsat(pr[u].w, d1)};
#pragma unroll
      for (int t = 0; t < 4; ++t) {
        const v4u af = {vf[u][t][0].x, vf[u][t][0].y, vf[u][t][1].x, vf[u][t][1].y};
        acc[t] = mfma(pb, af, acc[t]);
      }
    }
  }
  {
    const int base = OFF_X2 + ((l * 2 + c.b) * 32 + 16 * c.G + 4 * g) * 65536 + c.w * 8192 + c.s * 128 + n * 8;
#pragma unroll
    for (int q = 0; q < 4; ++q)
      st64(c.rs, base + q * 65536, v2u{pack2(acc[0][q], acc[1][q]), pack2(acc[2][q], acc[3][q])});
  }
  DRAIN();
  if (c.lane == 0) st32(c.rs, OFF_FW + ((((l * 2 + c.b) * 2 + c.G) * 8 + c.w) * 64 + c.s) * 4, 1u);
}

DI void fill_attn(Ctx& c) {
  const int n = c.lane & 15, g = c.lane >> 4;
#pragma unroll
  for (int l = 0; l < 2; ++l) {
    if (c.w == 0 && g == 0) st64(c.rs, OFF_ST + (((l * 2 + c.b) * 32 + 16 * c.G + n) * 64 + c.s) * 8, v2u{SENT, SENT});
    if (c.lane == 0) st32(c.rs, OFF_FW + ((((l * 2 + c.b) * 2 + c.G) * 8 + c.w) * 64 + c.s) * 4, SENT);
  }
}

DI void ckv_dma(Ctx& c, const Params& p) {
  const GAS unsigned char* src = (const GAS unsigned char*)p.ckv + ((size_t)c.b * 8192 + c.s * 128 + c.w * 16) * 1024;
#pragma unroll
  for (int i = 0; i < 16; ++i)
    __builtin_amdgcn_global_load_lds((const GAS unsigned*)(src + i * 1024 + (((unsigned)c.lane ^ swz(i)) << 4)),
                                     (LAS unsigned*)(c.L + L_IMG + (c.w * 16 + i) * 1024), 16, 0, 0);
}
DI void ckv_issue(Ctx& c, const Params& p, v4u (&t)[16]) {
  const GAS unsigned char* src = (const GAS unsigned char*)p.ckv + ((size_t)c.b * 8192 + c.s * 128 + c.w * 16) * 1024 + c.lane * 16;
#pragma unroll
  for (int i = 0; i < 16; ++i) t[i] = *(const GAS v4u*)(src + i * 1024);
}
DI void ckv_stage(Ctx& c, const v4u (&t)[16]) {
#pragma unroll
  for (int i = 0; i < 16; ++i)
    *(LAS v4u*)(c.L + L_IMG + (c.w * 16 + i) * 1024 + (((unsigned)c.lane ^ swz(i)) << 4)) = t[i];
}
DI void publish_ready_b(Ctx& c, v4u nonce) { LBAR(); }

DI void role_q(Ctx& c, const Params& p, v4u nonce, int h, int ch) {
  asm volatile("; role Q" ::: "memory");
  const int n = c.lane & 15, g = c.lane >> 4;
  const v4u sv = {SENT, SENT, SENT, SENT};
  if (c.w == 7) {
    st128(c.rs, OFF_X1 + (((c.lane >> 5) * 2 + c.b) * 32 + h) * 1024 + ch * 512 + (c.lane & 31) * 16, sv);
    DRAIN();
    if (c.lane == 0) st128(c.rs, OFF_RDY + c.blk * 64, nonce);
  }
  CBAR();
  unsigned xp = *(const GAS unsigned*)((const GAS unsigned char*)p.x + c.b * 1024 + 128 * c.w + 4 * (c.lane & 31));
  v4u rq[2][8], ru[8];
  {
    const GAS unsigned char* wq = (const GAS unsigned char*)p.wq + ((size_t)(64 * c.w + 16 * g) * 4096 + h * 128 + 8 * n) * 2;
#pragma unroll
    for (int ks = 0; ks < 2; ++ks)
#pragma unroll
      for (int j = 0; j < 8; ++j) rq[ks][j] = *(const GAS v4u*)(wq + (size_t)(8 * ks + j) * 8192);
    const GAS unsigned char* wu = (const GAS unsigned char*)p.wuk + ((size_t)(256 * ch + 32 * c.w + g) * 4096 + h * 128 + 8 * n) * 2;
#pragma unroll
    for (int i = 0; i < 8; ++i) ru[i] = *(const GAS v4u*)(wu + (size_t)(4 * i) * 8192);
  }
  v4u ct[16];
  __builtin_amdgcn_sched_barrier(0);
  v4u Wq[16];
  const bool xpub = (h == 0 && ch == 0);
  auto q_phase = [&](int l) __attribute__((always_inline)) {
    v4u a0, a1;
    layernorm(c, xp, a0, a1);
    if (l == 0) { ckv_issue(c, p, ct); __builtin_amdgcn_sched_barrier(0); tr8x8(rq[0], Wq); tr8x8(rq[1], Wq + 8); }
    gemv_ksplit<false>(c, a0, a1, Wq);
    if (l == 0) {
#pragma unroll
      for (int i = 0; i < 8; ++i) { const unsigned row = 4 * i + g; *(LAS v4u*)(c.L + L_IMG + c.w * 8192 + row * 256 + (((unsigned)n ^ swz(row >> 1)) << 4)) = ru[i]; }
#pragma unroll
      for (int t = 0; t < 2; ++t)
#pragma unroll
        for (int ks = 0; ks < 4; ++ks) ru[t * 4 + ks] = *(const LAS v4u*)(c.L + L_IMG + c.w * 8192 + (2 * n + t) * 256 + (((unsigned)(4 * ks + g) ^ swz(n)) << 4));
    }
    reduce128(c);
    f32x4 acc0 = {0.f, 0.f, 0.f, 0.f}, acc1 = {0.f, 0.f, 0.f, 0.f};
#pragma unroll
    for (int ks = 0; ks < 4; ++ks) {
      const v4u a = *(const LAS v4u*)(c.L + L_VEC + ks * 64 + g * 16);
      acc0 = mfma(a, ru[ks], acc0);
      acc1 = mfma(a, ru[4 + ks], acc1);
    }
    if (c.lane < 16) st32(c.rs, OFF_X1 + ((l * 2 + c.b) * 32 + h) * 1024 + (256 * ch + 32 * c.w + 2 * n) * 2, canon(pack2(acc0.x, acc1.x)));
  };
  q_phase(0);
  wait_ready_a(c, nonce);
  fill_attn(c);
  if (xpub && c.w == 1) st128(c.rs, OFF_X3 + (1 * 2 + c.b) * XV_STR + 32 * 1024 + c.lane * 16, sv);
  ckv_stage(c, ct);
  publish_ready_b(c, nonce);
#pragma unroll 1
  for (int l = 0; l < 2; ++l) {
    attn_phase(c, l);
    if (l == 0) {
      wait_ready_b(c, nonce);
      gate<D_Q4>(c, OFF_X3 + (0 * 2 + c.b) * XV_STR + h * 1024);
      xp = gather33<true>(c, OFF_X4 + (0 * 2 + c.b) * XV_STR, nullptr);
      if (xpub && c.lane < 32) st32(c.rs, OFF_X3 + (1 * 2 + c.b) * XV_STR + 32 * 1024 + 128 * c.w + 4 * c.lane, canon(xp));
      q_phase(1);
    }
  }
  gate<D_Q4>(c, OFF_X3 + (1 * 2 + c.b) * XV_STR + h * 1024);
  if (c.w == 0) {
    const int qi = 2 * h + ch;
    const int off = OFF_X4 + (1 * 2 + c.b) * XV_STR + (c.lane < 33 ? c.lane : 32) * 1024 + qi * 16;
    v4u v;
    for (;;) {
      CBAR();
      v = ld128(c.rs, off);
      if (__all(max(umax3(v.x, v.y, v.z), v.w) != SENT) || !spin_ok(c)) break;
    }
    const unsigned wl = c.lane < 32 ? LO1 : 0u, wh = c.lane < 32 ? HI1 : 0u;
    v4u o;
#pragma unroll
    for (int q = 0; q < 4; ++q) {
      float lo = dot2(v[q], wl, 0.f), hi = dot2(v[q], wh, 0.f);
      lo = quad_sum(lo); hi = quad_sum(hi);
      lo += dppf<0x124>(lo); hi += dppf<0x124>(hi);
      lo += dppf<0x128>(lo); hi += dppf<0x128>(hi);
      lo += dppm<0x142, 0xa>(lo); hi += dppm<0x142, 0xa>(hi);
      const unsigned s2 = (unsigned)__builtin_amdgcn_readlane((int)pack2(lo, hi), 31);
      const unsigned r2 = (unsigned)__builtin_amdgcn_readlane((int)v[q], 32);
      o[q] = pack2(bf_lo(r2) + bf_lo(s2), bf_hi(r2) + bf_hi(s2));
    }
    if (c.lane == 0) *(GAS v4u*)((GAS unsigned char*)p.out + c.b * 1024 + qi * 16) = o;
    if (c.lane < 4) st128(c.rs, OFF_RDY + (c.blk + 128 * (c.lane >> 1)) * 64 + (c.lane & 1) * 16, v4u{~nonce.x, ~nonce.y, ~nonce.z, ~nonce.w});
  }
}

DI void role_o(Ctx& c, const Params& p, v4u nonce, int h) {
  asm volatile("; role O" ::: "memory");
  const int n = c.lane & 15, g = c.lane >> 4;
  const v4u sv = {SENT, SENT, SENT, SENT};
  ckv_dma(c, p);
  DRAIN();
  __builtin_amdgcn_sched_barrier(0);
  v4u rv[2][8], ro[2][8];
  {
    const GAS unsigned char* wv = (const GAS unsigned char*)p.wuv + ((size_t)(64 * c.w + 2 * g) * 4096 + h * 128 + 8 * n) * 2;
#pragma unroll
    for (int ks = 0; ks < 2; ++ks)
#pragma unroll
      for (int j = 0; j < 8; ++j) rv[ks][j] = *(const GAS v4u*)(wv + (size_t)(16 * (j & 3) + 8 * ks + (j >> 2)) * 8192);
    const GAS unsigned char* wo = (const GAS unsigned char*)p.wo + ((size_t)(h * 128 + 32 * (n >> 3) + 8 * g) * 512 + 64 * c.w + 8 * (n & 7)) * 2;
#pragma unroll
    for (int kb = 0; kb < 2; ++kb)
#pragma unroll
      for (int j = 0; j < 8; ++j) ro[kb][j] = *(const GAS v4u*)(wo + (size_t)(64 * kb + j) * 1024);
  }
  __builtin_amdgcn_sched_barrier(0);
  fill_attn(c);
  if (c.w < 2) st128(c.rs, OFF_X3 + (c.w * 2 + c.b) * XV_STR + h * 1024 + c.lane * 16, sv);
  v4u Wv[16], Wo[16];
  tr8x8(rv[0], Wv); tr8x8(rv[1], Wv + 8);
  tr8x8(ro[0], Wo); tr8x8(ro[1], Wo + 8);
  publish_ready_b(c, nonce);
  wait_ready_a(c, nonce);
#pragma unroll 1
  for (int l = 0; l < 2; ++l) {
    attn_phase(c, l);
    if (l == 0) wait_ready_b(c, nonce);
    float inv;
    v2u v[16];
    {
      const int bs = OFF_ST + ((l * 2 + c.b) * 32 + h) * 512 + c.lane * 8;
      v2u st;
      for (;;) {
        CBAR();
        st = ld64(c.rs, bs);
        if (__all(max(st.x, st.y) != SENT) || !spin_ok(c)) break;
      }
      const float kf = u2f(st.x);
      float K = kf;
      K = fmaxf(K, dppf<0x128>(K)); K = fmaxf(K, dppf<0x124>(K)); K = fmaxf(K, dppf<0x122>(K)); K = fmaxf(K, dppf<0x121>(K));
      K = xg_max(K);
      const int d = (int)(K - kf);
      const unsigned eb = d < 127 ? (unsigned)(127 - d) << 7 : 0u;
      float Ls = u2f(st.y) * u2f(eb << 16);
      Ls += dppf<0x128>(Ls); Ls += dppf<0x124>(Ls); Ls += dppf<0x122>(Ls); Ls += dppf<0x121>(Ls);
      Ls = xg_sum(Ls);
      inv = 1.0f / Ls;
      *(LAS unsigned*)(c.L + L_EW + c.w * 256 + c.lane * 4) = eb;
    }
    unsigned el[16];
#pragma unroll
    for (int i = 0; i < 4; ++i) {
      const v4u e4 = *(const LAS v4u*)(c.L + L_EW + c.w * 256 + g * 64 + i * 16);
      el[4 * i] = e4.x; el[4 * i + 1] = e4.y; el[4 * i + 2] = e4.z; el[4 * i + 3] = e4.w;
    }
    {
      const int bf = OFF_FW + ((((l * 2 + c.b) * 2 + (h >> 4)) * 8 + c.w) * 64 + c.lane) * 4;
      for (;;) {
        CBAR();
        const unsigned fw = ld32(c.rs, bf);
        if (__all(fw != SENT) || !spin_ok(c)) break;
      }
      CBAR();
      const int bx = OFF_X2 + ((l * 2 + c.b) * 32 + h) * 65536 + c.w * 8192 + g * 2048 + n * 8;
#pragma unroll
      for (int i = 0; i < 16; ++i) v[i] = ld64(c.rs, bx + i * 128);
    }
    float s0 = 0.f, s1 = 0.f, s2 = 0.f, s3 = 0.f;
#pragma unroll
    for (int i = 0; i < 16; ++i) {
      const unsigned eh = el[i] << 16;
      s0 = dot2(v[i].x, el[i], s0); s1 = dot2(v[i].x, eh, s1); s2 = dot2(v[i].y, el[i], s2); s3 = dot2(v[i].y, eh, s3);
    }
    s0 = xg_sum(s0) * inv; s1 = xg_sum(s1) * inv; s2 = xg_sum(s2) * inv; s3 = xg_sum(s3) * inv;
    const unsigned h01 = pack2(s0, s1), h23 = pack2(s2, s3);
    const unsigned l01 = pack2(dot2(h01, NLO1, s0), dot2(h01, NHI1, s1)), l23 = pack2(dot2(h23, NLO1, s2), dot2(h23, NHI1, s3));
    *(LAS v2u*)(c.L + L_FR + c.w * 256 + (g & 1) * 128 + n * 8) = (g & 1) ? v2u{l01, l23} : v2u{h01, h23};
    const v4u a0 = *(const LAS v4u*)(c.L + L_FR + c.w * 256 + (n & 1) * 128 + g * 16);
    const v4u a1 = *(const LAS v4u*)(c.L + L_FR + c.w * 256 + (n & 1) * 128 + 64 + g * 16);
    gemv_ksplit<true>(c, a0, a1, Wv);
    reduce128(c);
    gemv_nsplit<2>(c, Wo, OFF_X3 + (l * 2 + c.b) * XV_STR + h * 1024);
    if (l == 0) gate<D_A1>(c, OFF_X4 + (0 * 2 + c.b) * XV_STR + (h & 15) * 1024);
  }
}

DI void role_f(Ctx& c, const Params& p, v4u nonce, int j) {
  asm volatile("; role F" ::: "memory");
  const int n = c.lane & 15, g = c.lane >> 4;
  const v4u sv = {SENT, SENT, SENT, SENT};
  const bool spare = j >= 28;
  const int jc = spare ? 27 : j;
  ckv_dma(c, p);
  DRAIN();
  __builtin_amdgcn_sched_barrier(0);
  v4u r13[2][8], r2[8];
  {
    const u16* wsel = n < 8 ? p.w1 : p.w3;
    const int col = 64 * jc + 8 * (n & 7);
    const GAS unsigned char* wa = (const GAS unsigned char*)wsel + ((size_t)(64 * c.w + 16 * g) * 1792 + col) * 2;
#pragma unroll
    for (int ks = 0; ks < 2; ++ks)
#pragma unroll
      for (int jj = 0; jj < 8; ++jj) r13[ks][jj] = *(const GAS v4u*)(wa + (size_t)(8 * ks + jj) * 3584);
#pragma unroll
    for (int jj = 0; jj < 8; ++jj) {
      const int f = 32 * (n >> 3) + 8 * g + jj;
      r2[jj] = *(const GAS v4u*)((const GAS unsigned char*)p.w2 + ((size_t)(64 * jc + f) * 512 + 64 * c.w + 8 * (n & 7)) * 2);
    }
  }
  __builtin_amdgcn_sched_barrier(0);
  fill_attn(c);
  if (c.w < 2) st128(c.rs, OFF_X4 + (c.w * 2 + c.b) * XV_STR + j * 1024 + c.lane * 16, spare ? v4u{0u, 0u, 0u, 0u} : sv);
  if (j == 0 && (c.w == 2 || c.w == 3)) st128(c.rs, OFF_X4 + ((c.w - 2) * 2 + c.b) * XV_STR + 32 * 1024 + c.lane * 16, sv);
  v4u W13[16], W2[8];
  tr8x8(r13[0], W13); tr8x8(r13[1], W13 + 8);
  tr8x8(r2, W2);
  publish_ready_b(c, nonce);
  wait_ready_a(c, nonce);
#pragma unroll 1
  for (int l = 0; l < 2; ++l) {
    attn_phase(c, l);
    unsigned xp;
    if (l == 0) wait_ready_b(c, nonce);
    if (spare) { if (l == 0) gate<D_A1>(c, OFF_X4 + (0 * 2 + c.b) * XV_STR + (j - 28) * 1024); continue; }
    gate<D_F3>(c, -1);
    if (l == 0) { xp = gather33<false>(c, OFF_X3 + (0 * 2 + c.b) * XV_STR, p.x + c.b * 512); }
    else xp = gather33<true>(c, OFF_X3 + (1 * 2 + c.b) * XV_STR, nullptr);
    if (j == 0 && c.lane < 32) st32(c.rs, OFF_X4 + (l * 2 + c.b) * XV_STR + 32 * 1024 + 128 * c.w + 4 * c.lane, canon(xp));
    v4u a0, a1;
    layernorm(c, xp, a0, a1);
    gemv_ksplit<false>(c, a0, a1, W13);
    LBAR();
    if (c.w == 0) {
      const unsigned gu = pack2(red_sum8(c, c.lane), red_sum8(c, 64 + c.lane));
      const float gt = bf_lo(gu);
      const float sg = rbf(gt * __builtin_amdgcn_rcpf(1.0f + __expf(-gt)));
      const float hv = sg * bf_hi(gu);
      *(LAS __bf16*)(c.L + L_VEC + c.lane * 2) = (__bf16)hv;
    }
    LBAR();
    gemv_nsplit<1>(c, W2, OFF_X4 + (l * 2 + c.b) * XV_STR + j * 1024);
    if (l == 0) gate<D_FA1>(c, -1);
  }
}

__global__ void __launch_bounds__(NT, 2) mega(Params p) {
  extern __shared__ __attribute__((aligned(16))) unsigned char smem[];
  Ctx c;
  c.rs = __builtin_amdgcn_make_buffer_rsrc((void*)p.ws, 0, WS_BYTES, 0x00020000);
  c.L = (LAS unsigned char*)smem;
  c.lane = threadIdx.x & 63;
  c.w = __builtin_amdgcn_readfirstlane(threadIdx.x >> 6);
  c.blk = blockIdx.x;
  const int x = c.blk & 7, slot = c.blk >> 3;
  c.b = (slot >> 3) & 1; c.G = slot >> 4; c.s = 8 * x + (slot & 7);
  const int r = 8 * c.G + (slot & 7);
  c.spins = 0;
  c.kLO1 = sconst(0x00003F80u); c.kHI1 = sconst(0x3F800000u); c.kONES = sconst(0x3F803F80u); c.kNLO1 = sconst(0x0000BF80u); c.kNHI1 = sconst(0xBF800000u);
  const unsigned long long id = llvm_dispatch_id();
  const unsigned long long dp = (unsigned long long)__builtin_amdgcn_dispatch_ptr();
  const v4u nonce = {(unsigned)id ^ 0x5bd1e995u, (unsigned)(id >> 32) ^ 0x9e3779b9u, (unsigned)dp ^ 0x85ebca6bu, (unsigned)(dp >> 32) ^ 0xc2b2ae35u};
  c.nonce = nonce;
  if (r < 8) role_q(c, p, nonce, 4 * x + (r >> 1), r & 1);
  else if (r < 12) role_o(c, p, nonce, 4 * x + (r - 8));
  else role_f(c, p, nonce, 4 * x + (r - 12));
}
}

extern "C" void kernel_launch(void* const* d_in, const int* in_sizes, int n_in,
                              void* d_out, int out_size, void* d_ws, size_t ws_size,
                              hipStream_t stream) {
  mk::Params p;
  p.x = (const u16*)d_in[0]; p.ckv = (const u16*)d_in[1]; p.wq = (const u16*)d_in[2]; p.wuk = (const u16*)d_in[3];
  p.wuv = (const u16*)d_in[4]; p.wo = (const u16*)d_in[5]; p.w1 = (const u16*)d_in[7]; p.w3 = (const u16*)d_in[8];
  p.w2 = (const u16*)d_in[9]; p.out = (u16*)d_out; p.ws = (unsigned char*)d_ws;
  (void)hipFuncSetAttribute((const void*)mk::mega, hipFuncAttributeMaxDynamicSharedMemorySize, mk::L_BYTES);
  hipLaunchKernelGGL(mk::mega, dim3(256), dim3(mk::NT), mk::L_BYTES, stream, p);
}
```
